```python
import torch

def _softmax(x, axis=-1):
    return torch.softmax(x, dim=axis)
def _gelu(x):
    return torch.nn.functional.gelu(x, approximate="tanh")
def _silu(x):
    return torch.nn.functional.silu(x)
def _sigmoid(x):
    return torch.sigmoid(x)
def _layernorm(x, axis=-1, eps=1e-5):
    m = x.mean(dim=axis, keepdim=True)
    v = x.var(dim=axis, keepdim=True, unbiased=False)
    return (x - m) / torch.sqrt(v + eps)
def _scatter_add(tgt, idx, src, axis=0):
    return tgt.clone().index_add_(axis, idx, src)
def _segment_sum(src, seg, n):
    out = torch.zeros((n,) + src.shape[1:], dtype=src.dtype)
    return out.index_add_(0, seg, src)
def _pad(x, pw):
    flat = tuple(v for pair in reversed(pw) for v in pair)
    return torch.nn.functional.pad(x, flat)

kernel_name = 'gated_mix__compressed_kv_cla__dense_activation_mish__prompt'

def setup_inputs():

    _w = (lambda a: (lambda t: t.to(torch.bfloat16) if t.is_floating_point() else t)(torch.as_tensor(a)))
    def _A_setup():
        B, H, Sq, Sk, D, M = 1, 32, 2048, 2048, 128, 512
        Dc = 4 * D
        return {
            "x": _w((lambda *s: torch.randn(*s, dtype=torch.bfloat16) * 0.05)(B, Sq, M)),
            "c_kv": _w((lambda *s: torch.randn(*s, dtype=torch.bfloat16) * 0.2)(B, Sk, Dc)),
            "w_q": _w((lambda *s: torch.randn(*s, dtype=torch.bfloat16) * 12.8)(M, H, D) * (M ** -0.5)),
            "w_uk": _w((lambda *s: torch.randn(*s, dtype=torch.bfloat16) * 12.8)(Dc, H, D) * (Dc ** -0.5)),
            "w_uv": _w((lambda *s: torch.randn(*s, dtype=torch.bfloat16) * 0.2)(Dc, H, D) * (Dc ** -0.5)),
            "w_o": _w((lambda *s: torch.randn(*s, dtype=torch.bfloat16) * 0.2)(H, D, M) * ((H * D) ** -0.5)),
        }
    _A_in = _A_setup()

    def _B_setup():
        B, S, D, F = 1, 2048, 512, 1792
        return {
            "x": _w((lambda *s: torch.randn(*s, dtype=torch.bfloat16) * 0.05)(B, S, D)),
            "w1": _w((lambda *s: torch.randn(*s, dtype=torch.bfloat16) * 6.4)(D, F) * (D ** -0.5)),
            "b1": _w((lambda *s: torch.randn(*s, dtype=torch.bfloat16) * 0.05)(F)),
            "w2": _w((lambda *s: torch.randn(*s, dtype=torch.bfloat16) * 0.05)(F, D) * (F ** -0.5)),
        }
    _B_in = _B_setup()

    return {
        "A_x": _A_in["x"],
        "A_c_kv": _A_in["c_kv"],
        "A_w_q": _A_in["w_q"],
        "A_w_uk": _A_in["w_uk"],
        "A_w_uv": _A_in["w_uv"],
        "A_w_o": _A_in["w_o"],
        "B_x": _B_in["x"],
        "B_w1": _B_in["w1"],
        "B_b1": _B_in["b1"],
        "B_w2": _B_in["w2"],
        "gate_w": _w((lambda *s: torch.randn(*s, dtype=torch.bfloat16) * 0.05)(512) * 16.0),
    }

def reference(A_x, A_c_kv, A_w_q, A_w_uk, A_w_uv, A_w_o, B_x, B_w1, B_b1, B_w2, gate_w):
    def _A_ref(x, c_kv, w_q, w_uk, w_uv, w_o):
        B, Sq, M = x.shape[0], x.shape[1], x.shape[2]
        Sk = c_kv.shape[1]
        H, D = w_q.shape[1], w_q.shape[2]
        q = torch.einsum("bqm,mhd->bhqd", x, w_q)
        k = torch.einsum("bkc,chd->bhkd", c_kv, w_uk)
        v = torch.einsum("bkc,chd->bhkd", c_kv, w_uv)
        scores = torch.einsum("bhqd,bhkd->bhqk", q, k) * (D ** -0.5)
        off = Sk - Sq
        qi = torch.arange(Sq)[:, None]
        kj = torch.arange(Sk)[None, :]
        cmask = kj <= (qi + off)
        scores = torch.where(cmask[None, None], scores, -1e9)
        p = _softmax(scores, axis=-1)
        ctx = torch.einsum("bhqk,bhkd->bhqd", p, v)
        return torch.einsum("bhqd,hdm->bqm", ctx, w_o)

    def _B_ref(x, w1, b1, w2):
        h = torch.einsum("bsd,df->bsf", x, w1) + b1
        sp = torch.log1p(torch.exp(-torch.abs(h))) + torch.where(h > 0, h, torch.zeros_like(h))
        act = h * torch.tanh(sp)
        return torch.einsum("bsf,fd->bsd", act, w2)

    x = A_x
    g = _sigmoid((x * gate_w).sum(-1, keepdims=True))
    a = _A_ref(x, A_c_kv, A_w_q, A_w_uk, A_w_uv, A_w_o)
    b = _B_ref(x, B_w1, B_b1, B_w2)
    return g * a + (1 - g) * b
```

```cpp
#include <hip/hip_runtime.h>
#include <hip/hip_bf16.h>
#include <stdint.h>

static constexpr int K1_LDS = 131072, K3_LDS = 131072;
static constexpr size_t QF_OFF = 0, KF_OFF = 16777216, VF_OFF = 33554432, XF_OFF = 50331648, G_OFF = 74448896, P_OFF = 74457088, FLAG_OFF = 91234304, OVF_OFF = 91267072, WS_USED = 91299840;

typedef unsigned short u16_t;
#define LDS_AS __attribute__((address_space(3)))
__device__ __forceinline__ float bfl(unsigned w) { return __uint_as_float(w << 16); }
__device__ __forceinline__ float bfh(unsigned w) { return __uint_as_float(w & 0xffff0000u); }
__device__ __forceinline__ unsigned ld_dev(const void* p) { return __hip_atomic_load((const unsigned*)p, __ATOMIC_RELAXED, __HIP_MEMORY_SCOPE_AGENT); }
__device__ __forceinline__ unsigned slot32(unsigned kk) { return (((kk & 15) >> 2) * 16) * 8 + ((kk >> 4) * 4 + (kk & 3)); }
__device__ __forceinline__ unsigned pk2(float lo, float hi) {
  typedef float f2_t __attribute__((ext_vector_type(2))); typedef __bf16 b2_t __attribute__((ext_vector_type(2)));
  f2_t v = {lo, hi}; b2_t b = __builtin_convertvector(v, b2_t); return __builtin_bit_cast(unsigned, b);
}
__device__ __forceinline__ void exact_rows(const u16_t* x, const u16_t* wq, char* ws, unsigned h, unsigned row0, unsigned w, unsigned lane) {
  volatile LDS_AS float* qb = (volatile LDS_AS float*)(131072u + w * 4096u + 3072u);
  volatile LDS_AS float* pb = qb + 128;
  const u16_t* kf = (const u16_t*)(ws + KF_OFF); const u16_t* vf = (const u16_t*)(ws + VF_OFF);
#pragma unroll 1
  for (unsigned r = row0; r < row0 + 32; ++r) {
    float a0 = 0.f, a1 = 0.f;
#pragma unroll 1
    for (unsigned k = 0; k < 512; ++k) {
      const float xv = __uint_as_float((unsigned)x[r * 512 + k] << 16);
      const unsigned wv = *(const unsigned*)(wq + (size_t)k * 4096 + h * 128 + 2 * lane);
      a0 += xv * bfl(wv); a1 += xv * bfh(wv);
    }
    const unsigned qp = pk2(a0 * 0.12751743082f, a1 * 0.12751743082f);
    qb[2 * lane] = bfl(qp); qb[2 * lane + 1] = bfh(qp);
    float m = -3.0e38f, l = 0.f, o0 = 0.f, o1 = 0.f;
#pragma unroll 1
    for (int pass = 0; pass < 2; ++pass) {
#pragma unroll 1
      for (unsigned kb = 0; kb <= (r >> 6); ++kb) {
        const unsigned key = kb * 64 + lane;
        float s = 0.f;
        const size_t kbase = (size_t)(h * 128 + (key >> 4)) * 4 * 512 + (key & 15) * 8;
#pragma unroll 1
        for (unsigned d = 0; d < 128; d += 2) {
          const unsigned kv = ld_dev(kf + kbase + (d >> 5) * 512 + slot32(d & 31));
          s += qb[d] * bfl(kv) + qb[d + 1] * bfh(kv);
        }
        if (pass == 0) { if (key <= r) m = fmaxf(m, s); continue; }
        const float p = key <= r ? __builtin_amdgcn_exp2f(s - m) : 0.f;
        l += p; pb[lane] = p;
#pragma unroll 1
        for (unsigned j = 0; j < 64; j += 2) {
          const unsigned k0 = kb * 64 + j, d0 = 2 * lane;
          const size_t vb = ((size_t)(h * 64 + (k0 >> 5)) * 8 + (d0 >> 4)) * 512 + (d0 & 15) * 8 + slot32(k0 & 31);
          const unsigned v0 = ld_dev(vf + vb), v1 = ld_dev(vf + vb + 8);
          const float p0 = pb[j], p1 = pb[j + 1];
          o0 += p0 * bfl(v0) + p1 * bfh(v0); o1 += p0 * bfl(v1) + p1 * bfh(v1);
        }
      }
      if (pass == 0) for (int o = 32; o; o >>= 1) m = fmaxf(m, __shfl_xor(m, o));
    }
    for (int o = 32; o; o >>= 1) l += __shfl_xor(l, o);
    const unsigned kk = 1792 + h * 128 + 2 * lane;
    const size_t xi = ((size_t)((r >> 7) * 184 + (kk >> 5)) * 8 + ((r >> 4) & 7)) * 512 + (r & 15) * 8 + slot32(kk & 31);
    __hip_atomic_store((unsigned*)(ws + XF_OFF + xi * 2), pk2(o0 / l, o1 / l), __ATOMIC_RELAXED, __HIP_MEMORY_SCOPE_AGENT);
  }
}

extern "C" __global__ __launch_bounds__(256, 1) void mla_fused(const void* x, const void* ckv, const void* wq, const void* wuk, const void* wuv,
    const void* w1, const void* b1, const void* gw, const void* wo, const void* w2, void* ws, void* out) {
  const unsigned tid = threadIdx.x, bid = blockIdx.x;
  unsigned flag, tl, th;
  asm volatile(
R"ASM(s_getpc_b64 s[60:61]
mla_fused_a_pf0:
s_and_b32 s61, s61, 0xffff
s_mov_b32 s62, mla_fused_a_tile2end-mla_fused_a_pf0
s_mov_b32 s63, 0x20000
v_lshlrev_b32 v239, 6, %[tid]
buffer_load_dword v238, v239, s[60:63], 0 offen
v_add_u32 v239, 0x4000, v239
buffer_load_dword v237, v239, s[60:63], 0 offen
v_add_u32 v239, 0x4000, v239
)ASM"
  :
  : [tid]"v"(tid)
  : "v236", "v237", "v238", "v239", "s60", "s61", "s62", "s63", "memory");
  asm volatile(
R"ASM(v_and_b32 v253, 63, %[tid]
v_lshrrev_b32 v254, 6, %[tid]
s_nop 0
v_readfirstlane_b32 s68, v254
s_mov_b32 s89, %[bid]
s_mov_b64 s[36:37], %[ckv]
s_and_b32 s37, s37, 0xffff
s_mov_b32 s38, 0x200000
s_mov_b32 s39, 0x20000
s_mov_b64 s[44:45], %[wuk]
s_and_b32 s45, s45, 0xffff
s_mov_b32 s46, 0x400000
s_mov_b32 s47, 0x20000
s_mov_b64 s[48:49], %[wuv]
s_and_b32 s49, s49, 0xffff
s_mov_b32 s50, 0x400000
s_mov_b32 s51, 0x20000
s_and_b32 s82, s89, 7
s_lshr_b32 s83, s89, 3
s_lshl_b32 s71, s82, 2
s_lshr_b32 s84, s83, 3
s_add_u32 s71, s71, s84
s_and_b32 s72, s83, 7
v_lshrrev_b32 v0, 3, v253
v_lshlrev_b32 v0, 10, v0
v_and_b32 v1, 7, v253
v_lshrrev_b32 v2, 4, v253
v_xor_b32 v3, v1, v2
v_lshl_add_u32 v246, v3, 4, v0
v_xor_b32 v3, 4, v3
v_lshl_add_u32 v247, v3, 4, v0
v_bfe_u32 v0, v253, 2, 3
v_lshrrev_b32 v1, 5, v253
v_lshlrev_b32 v1, 6, v1
v_and_b32 v2, 3, v253
v_lshl_add_u32 v3, v2, 4, v1
v_xor_b32 v4, 2, v2
v_lshl_add_u32 v4, v4, 4, v1
v_lshlrev_b32 v5, 13, v0
v_add_u32 v248, v5, v3
v_add_u32 v249, v5, v4
s_lshr_b32 s69, s68, 1
s_and_b32 s70, s68, 1
s_lshl_b32 s79, s69, 14
s_lshl_b32 s82, s70, 12
s_add_u32 s79, s79, s82
s_lshl_b32 s80, s68, 11
s_add_u32 s80, s80, 65536
s_sub_u32 s82, 15, s72
s_cmp_eq_u32 s69, 0
s_cselect_b32 s88, s82, s72
s_lshl_b32 s94, s88, 17
s_lshl_b32 s82, s70, 15
s_add_u32 s94, s94, s82
s_lshl_b32 s96, s68, 17
s_lshl_b32 s82, s71, 8
s_add_u32 s96, s96, s82
s_mov_b32 s86, 7
s_add_u32 s86, s86, 1
s_and_b32 s86, s86, 7
s_lshl_b32 s83, s86, 7
s_add_u32 s73, s94, s83
s_lshl_b32 s83, s86, 19
s_add_u32 s75, s96, s83
s_add_u32 m0, s79, 0
s_add_u32 s82, s73, 0
buffer_load_dwordx4 v246, s[36:39], s82 offen lds
s_add_u32 m0, s80, 0
s_add_u32 s82, s75, 0
buffer_load_dwordx4 v248, s[44:47], s82 offen lds
s_add_u32 m0, s79, 1024
s_add_u32 s82, s73, 8192
buffer_load_dwordx4 v247, s[36:39], s82 offen lds
s_add_u32 m0, s80, 16384
s_add_u32 s82, s75, 0
buffer_load_dwordx4 v248, s[48:51], s82 offen lds
s_add_u32 m0, s79, 2048
s_add_u32 s82, s73, 16384
buffer_load_dwordx4 v246, s[36:39], s82 offen lds
s_add_u32 m0, s80, 1024
s_add_u32 s82, s75, 65536
buffer_load_dwordx4 v249, s[44:47], s82 offen lds
s_add_u32 m0, s79, 3072
s_add_u32 s82, s73, 24576
buffer_load_dwordx4 v247, s[36:39], s82 offen lds
s_add_u32 m0, s80, 17408
s_add_u32 s82, s75, 65536
buffer_load_dwordx4 v249, s[48:51], s82 offen lds
s_add_u32 s86, s86, 1
s_and_b32 s86, s86, 7
s_lshl_b32 s83, s86, 7
s_add_u32 s73, s94, s83
s_lshl_b32 s83, s86, 19
s_add_u32 s75, s96, s83
s_mov_b64 s[40:41], %[ckv]
s_and_b32 s41, s41, 0xffff
s_mov_b32 s42, 0x200000
s_mov_b32 s43, 0x20000
s_mov_b64 s[52:53], %[wuk]
s_add_u32 s52, s52, 128
s_addc_u32 s53, s53, 0
s_and_b32 s53, s53, 0xffff
s_mov_b32 s54, 0x3fff80
s_mov_b32 s55, 0x20000
s_mov_b64 s[56:57], %[wuv]
s_add_u32 s56, s56, 128
s_addc_u32 s57, s57, 0
s_and_b32 s57, s57, 0xffff
s_mov_b32 s58, 0x3fff80
s_mov_b32 s59, 0x20000
v_mov_b32 v250, v248
v_mov_b32 v251, v249
v_lshlrev_b32 v252, 4, v253
s_add_u32 s95, s94, 65536
s_mov_b32 s97, s96
s_mov_b32 s77, 65536
s_mov_b32 s78, 524288
s_mov_b32 s87, 7
s_add_u32 s87, s87, 1
s_and_b32 s87, s87, 7
s_lshl_b32 s83, s87, 7
s_add_u32 s74, s95, s83
s_mul_i32 s83, s87, s78
s_add_u32 s76, s97, s83
s_add_u32 m0, s79, 8192
s_add_u32 s82, s74, 0
buffer_load_dwordx4 v246, s[40:43], s82 offen lds
v_and_b32 v0, 15, v253
v_lshrrev_b32 v1, 4, v253
v_lshrrev_b32 v2, 3, v0
v_lshlrev_b32 v2, 10, v2
v_and_b32 v3, 7, v0
v_lshl_add_u32 v2, v3, 7, v2
v_lshrrev_b32 v3, 1, v0
v_xor_b32 v3, v3, v1
s_add_u32 m0, s80, 8192
s_mov_b32 s82, s76
buffer_load_dwordx4 v250, s[52:55], s82 offen lds
s_lshl_b32 s84, s69, 14
v_add_u32 v2, s84, v2
v_lshl_add_u32 v240, v3, 4, v2
v_xor_b32 v3, 4, v3
v_lshl_add_u32 v241, v3, 4, v2
v_lshlrev_b32 v2, 10, v1
v_bfe_u32 v3, v253, 2, 2
v_lshl_add_u32 v2, v3, 6, v2
v_and_b32 v3, 3, v253
s_add_u32 m0, s79, 9216
s_add_u32 s82, s74, 8192
buffer_load_dwordx4 v247, s[40:43], s82 offen lds
v_lshl_add_u32 v2, v3, 3, v2
v_and_b32 v3, 1, v1
v_lshl_add_u32 v4, v3, 5, v2
v_xor_b32 v3, 1, v3
v_lshl_add_u32 v5, v3, 5, v2
s_lshl_b32 s84, s70, 14
s_add_u32 s84, s84, 65536
v_add_u32 v242, s84, v4
v_add_u32 v243, s84, v5
s_lshl_b32 s84, s70, 9
s_add_u32 s84, s84, 73728
v_add_u32 v244, s84, v4
v_add_u32 v245, s84, v5
s_lshl_b32 s90, s71, 19
s_lshl_b32 s84, s88, 15
s_add_u32 s90, s90, s84
s_cmp_eq_u32 s70, 0
s_cselect_b32 s84, 0x1000000, 0
s_cselect_b32 s85, 0, 0x2000000
s_add_u32 s84, s84, s85
s_add_u32 s90, s90, s84
s_cmp_eq_u32 s70, 0
s_movk_i32 s84, 0x400
s_cselect_b32 s91, 4096, s84
s_cselect_b32 s92, s84, 0x2000
s_add_u32 m0, s80, 24576
s_mov_b32 s82, s76
buffer_load_dwordx4 v250, s[56:59], s82 offen lds
v_mov_b64 v[28:29], 0
v_mov_b64 v[30:31], 0
v_mov_b64 v[32:33], 0
v_mov_b64 v[34:35], 0
v_mov_b64 v[36:37], 0
v_mov_b64 v[38:39], 0
v_mov_b64 v[40:41], 0
v_mov_b64 v[42:43], 0
s_add_u32 m0, s79, 10240
s_add_u32 s82, s74, 16384
buffer_load_dwordx4 v246, s[40:43], s82 offen lds
v_mov_b64 v[44:45], 0
v_mov_b64 v[46:47], 0
v_mov_b64 v[48:49], 0
v_mov_b64 v[50:51], 0
v_mov_b64 v[52:53], 0
v_mov_b64 v[54:55], 0
v_mov_b64 v[56:57], 0
v_mov_b64 v[58:59], 0
s_add_u32 m0, s80, 9216
s_add_u32 s82, s76, s77
buffer_load_dwordx4 v251, s[52:55], s82 offen lds
v_mov_b64 v[60:61], 0
v_mov_b64 v[62:63], 0
v_mov_b64 v[64:65], 0
v_mov_b64 v[66:67], 0
v_mov_b64 v[68:69], 0
v_mov_b64 v[70:71], 0
v_mov_b64 v[72:73], 0
v_mov_b64 v[74:75], 0
s_add_u32 m0, s79, 11264
s_add_u32 s82, s74, 24576
buffer_load_dwordx4 v247, s[40:43], s82 offen lds
v_mov_b64 v[76:77], 0
v_mov_b64 v[78:79], 0
v_mov_b64 v[80:81], 0
v_mov_b64 v[82:83], 0
v_mov_b64 v[84:85], 0
v_mov_b64 v[86:87], 0
v_mov_b64 v[88:89], 0
v_mov_b64 v[90:91], 0
s_add_u32 m0, s80, 25600
s_add_u32 s82, s76, s77
buffer_load_dwordx4 v251, s[56:59], s82 offen lds
v_mov_b64 v[92:93], 0
v_mov_b64 v[94:95], 0
v_mov_b64 v[96:97], 0
v_mov_b64 v[98:99], 0
v_mov_b64 v[100:101], 0
v_mov_b64 v[102:103], 0
v_mov_b64 v[104:105], 0
v_mov_b64 v[106:107], 0
s_add_u32 s87, s87, 1
s_and_b32 s87, s87, 7
s_lshl_b32 s83, s87, 7
s_add_u32 s74, s95, s83
s_mul_i32 s83, s87, s78
s_add_u32 s76, s97, s83
s_mov_b64 s[60:61], %[ws]
s_and_b32 s61, s61, 0xffff
s_mov_b32 s62, 0x5712000
s_mov_b32 s63, 0x20000
v_mov_b32 v231, 0
s_mov_b32 s82, 0x5702000
buffer_load_dwordx2 v[232:233], v231, s[60:63], s82 offen sc1
s_sub_u32 s82, 15, s72
s_cmp_lt_u32 s71, 16
s_cselect_b32 s82, s82, s72
s_lshl_b32 s82, s82, 7
s_and_b32 s83, s71, 15
s_lshl_b32 s83, s83, 3
s_add_u32 s82, s82, s83
s_lshl_b32 s83, s68, 1
s_add_u32 s93, s82, s83
s_mov_b64 s[64:65], %[x]
s_and_b32 s65, s65, 0xffff
s_mov_b32 s66, 0x200000
s_mov_b32 s67, 0x20000
s_lshl_b32 s82, s93, 10
buffer_load_dwordx4 v[16:19], v252, s[64:67], s82 offen
buffer_load_dwordx4 v[20:23], v252, s[64:67], s82 offen offset:1024
s_mov_b64 s[64:65], %[gw]
s_and_b32 s65, s65, 0xffff
s_mov_b32 s66, 0x400
s_mov_b32 s67, 0x20000
buffer_load_dwordx4 v[24:27], v252, s[64:67], 0 offen
s_mov_b64 s[64:65], %[b1]
s_and_b32 s65, s65, 0xffff
s_mov_b32 s66, 0xe00
s_mov_b32 s67, 0x20000
v_lshrrev_b32 v0, 4, v253
v_lshlrev_b32 v0, 3, v0
s_lshl_b32 s82, s71, 7
s_lshl_b32 s83, s70, 6
s_add_u32 s82, s82, s83
buffer_load_dwordx2 a[240:241], v0, s[64:67], s82 offen
buffer_load_dwordx2 a[242:243], v0, s[64:67], s82 offen offset:32
s_add_u32 m0, s79, 32768
s_add_u32 s82, s73, 0
buffer_load_dwordx4 v246, s[36:39], s82 offen lds
v_mov_b64 v[108:109], 0
v_mov_b64 v[110:111], 0
v_mov_b64 v[112:113], 0
v_mov_b64 v[114:115], 0
v_mov_b64 v[116:117], 0
v_mov_b64 v[118:119], 0
v_mov_b64 v[120:121], 0
v_mov_b64 v[122:123], 0
v_mov_b64 v[124:125], 0
s_add_u32 m0, s80, 32768
s_add_u32 s82, s75, 0
buffer_load_dwordx4 v248, s[44:47], s82 offen lds
v_mov_b64 v[126:127], 0
v_mov_b64 v[128:129], 0
v_mov_b64 v[130:131], 0
v_mov_b64 v[132:133], 0
v_mov_b64 v[134:135], 0
v_mov_b64 v[136:137], 0
v_mov_b64 v[138:139], 0
v_mov_b64 v[140:141], 0
v_mov_b64 v[142:143], 0
s_add_u32 m0, s79, 33792
s_add_u32 s82, s73, 8192
buffer_load_dwordx4 v247, s[36:39], s82 offen lds
v_mov_b64 v[144:145], 0
v_mov_b64 v[146:147], 0
v_mov_b64 v[148:149], 0
v_mov_b64 v[150:151], 0
v_mov_b64 v[152:153], 0
v_mov_b64 v[154:155], 0
v_mov_b64 v[156:157], 0
v_mov_b64 v[158:159], 0
v_mov_b64 v[160:161], 0
s_add_u32 m0, s80, 49152
s_add_u32 s82, s75, 0
buffer_load_dwordx4 v248, s[48:51], s82 offen lds
v_mov_b64 v[162:163], 0
v_mov_b64 v[164:165], 0
v_mov_b64 v[166:167], 0
v_mov_b64 v[168:169], 0
v_mov_b64 v[170:171], 0
v_mov_b64 v[172:173], 0
v_mov_b64 v[174:175], 0
v_mov_b64 v[176:177], 0
v_mov_b64 v[178:179], 0
s_add_u32 m0, s79, 34816
s_add_u32 s82, s73, 16384
buffer_load_dwordx4 v246, s[36:39], s82 offen lds
v_mov_b64 v[180:181], 0
v_mov_b64 v[182:183], 0
v_mov_b64 v[184:185], 0
v_mov_b64 v[186:187], 0
v_mov_b64 v[188:189], 0
v_mov_b64 v[190:191], 0
v_mov_b64 v[192:193], 0
v_mov_b64 v[194:195], 0
v_mov_b64 v[196:197], 0
s_add_u32 m0, s80, 33792
s_add_u32 s82, s75, 65536
buffer_load_dwordx4 v249, s[44:47], s82 offen lds
v_mov_b64 v[198:199], 0
v_mov_b64 v[200:201], 0
v_mov_b64 v[202:203], 0
v_mov_b64 v[204:205], 0
v_mov_b64 v[206:207], 0
v_mov_b64 v[208:209], 0
v_mov_b64 v[210:211], 0
v_mov_b64 v[212:213], 0
v_mov_b64 v[214:215], 0
s_add_u32 m0, s79, 35840
s_add_u32 s82, s73, 24576
buffer_load_dwordx4 v247, s[36:39], s82 offen lds
v_mov_b64 v[216:217], 0
v_mov_b64 v[218:219], 0
v_mov_b64 v[220:221], 0
v_mov_b64 v[222:223], 0
v_mov_b64 v[224:225], 0
v_mov_b64 v[226:227], 0
v_mov_b64 v[228:229], 0
v_mov_b64 v[234:235], 0
v_accvgpr_write_b32 a160, 0
s_add_u32 m0, s80, 50176
s_add_u32 s82, s75, 65536
buffer_load_dwordx4 v249, s[48:51], s82 offen lds
v_accvgpr_write_b32 a161, 0
v_accvgpr_write_b32 a162, 0
v_accvgpr_write_b32 a163, 0
v_accvgpr_write_b32 a164, 0
v_accvgpr_write_b32 a165, 0
v_accvgpr_write_b32 a166, 0
v_accvgpr_write_b32 a167, 0
v_accvgpr_write_b32 a168, 0
v_accvgpr_write_b32 a169, 0
s_add_u32 s86, s86, 1
s_and_b32 s86, s86, 7
s_lshl_b32 s83, s86, 7
s_add_u32 s73, s94, s83
s_lshl_b32 s83, s86, 19
s_add_u32 s75, s96, s83
s_add_u32 m0, s79, 40960
s_add_u32 s82, s74, 0
buffer_load_dwordx4 v246, s[40:43], s82 offen lds
v_accvgpr_write_b32 a170, 0
v_accvgpr_write_b32 a171, 0
v_accvgpr_write_b32 a172, 0
v_accvgpr_write_b32 a173, 0
v_accvgpr_write_b32 a174, 0
v_accvgpr_write_b32 a175, 0
s_add_u32 m0, s80, 40960
s_mov_b32 s82, s76
buffer_load_dwordx4 v250, s[52:55], s82 offen lds
s_add_u32 m0, s79, 41984
s_add_u32 s82, s74, 8192
buffer_load_dwordx4 v247, s[40:43], s82 offen lds
s_add_u32 m0, s80, 57344
s_mov_b32 s82, s76
buffer_load_dwordx4 v250, s[56:59], s82 offen lds
s_add_u32 m0, s79, 43008
s_add_u32 s82, s74, 16384
buffer_load_dwordx4 v246, s[40:43], s82 offen lds
s_add_u32 m0, s80, 41984
s_add_u32 s82, s76, s77
buffer_load_dwordx4 v251, s[52:55], s82 offen lds
s_add_u32 m0, s79, 44032
s_add_u32 s82, s74, 24576
buffer_load_dwordx4 v247, s[40:43], s82 offen lds
s_add_u32 m0, s80, 58368
s_add_u32 s82, s76, s77
buffer_load_dwordx4 v251, s[56:59], s82 offen lds
s_add_u32 s87, s87, 1
s_and_b32 s87, s87, 7
s_lshl_b32 s83, s87, 7
s_add_u32 s74, s95, s83
s_mul_i32 s83, s87, s78
s_add_u32 s76, s97, s83
v_mov_b64 v[8:9], 0
s_waitcnt vmcnt(18)
v_dot2c_f32_bf16 v8, v16, v24
v_dot2c_f32_bf16 v9, v20, v24
v_dot2c_f32_bf16 v8, v17, v25
v_dot2c_f32_bf16 v9, v21, v25
v_dot2c_f32_bf16 v8, v18, v26
v_dot2c_f32_bf16 v9, v22, v26
v_dot2c_f32_bf16 v8, v19, v27
v_dot2c_f32_bf16 v9, v23, v27
s_nop 3
v_add_f32_dpp v8, v8, v8 row_ror:8 row_mask:0xf bank_mask:0xf
v_add_f32_dpp v9, v9, v9 row_ror:8 row_mask:0xf bank_mask:0xf
s_nop 0
v_add_f32_dpp v8, v8, v8 row_ror:4 row_mask:0xf bank_mask:0xf
v_add_f32_dpp v9, v9, v9 row_ror:4 row_mask:0xf bank_mask:0xf
s_nop 0
v_add_f32_dpp v8, v8, v8 row_ror:2 row_mask:0xf bank_mask:0xf
v_add_f32_dpp v9, v9, v9 row_ror:2 row_mask:0xf bank_mask:0xf
s_nop 0
v_add_f32_dpp v8, v8, v8 row_ror:1 row_mask:0xf bank_mask:0xf
v_add_f32_dpp v9, v9, v9 row_ror:1 row_mask:0xf bank_mask:0xf
v_mov_b32 v10, v8
v_mov_b32 v11, v9
s_nop 0
v_permlane16_swap_b32 v8, v10
v_permlane16_swap_b32 v9, v11
v_add_f32 v8, v8, v10
v_add_f32 v9, v9, v11
v_mov_b32 v10, v8
v_mov_b32 v11, v9
s_nop 0
v_permlane32_swap_b32 v8, v10
)ASM"
R"ASM(v_permlane32_swap_b32 v9, v11
v_add_f32 v8, v8, v10
v_add_f32 v9, v9, v11
v_mul_f32 v8, 0xbfb8aa3b, v8
v_mul_f32 v9, 0xbfb8aa3b, v9
v_exp_f32 v8, v8
v_exp_f32 v9, v9
v_add_f32 v8, 1.0, v8
v_add_f32 v9, 1.0, v9
v_rcp_f32 v8, v8
v_rcp_f32 v9, v9
v_cmp_eq_u32 vcc, 0, v253
v_mov_b32 v2, 0x7ffffff0
v_mov_b32 v1, 0
v_cndmask_b32 v1, v2, v1, vcc
s_lshl_b32 s82, s93, 2
s_add_u32 s82, s82, 0x4700000
buffer_store_dwordx2 v[8:9], v1, s[60:63], s82 offen sc1
v_readfirstlane_b32 s98, v232
v_readfirstlane_b32 s99, v233
s_nop 2
s_add_u32 s98, s98, 1
s_add_u32 s99, s99, 0x9e3779b9
v_mov_b64 v[0:1], 0
v_mov_b64 v[2:3], 0
v_mov_b64 v[4:5], 0
v_mov_b64 v[6:7], 0
v_mov_b64 v[8:9], 0
v_mov_b64 v[10:11], 0
v_mov_b64 v[12:13], 0
v_mov_b64 v[14:15], 0
v_mov_b64 v[16:17], 0
v_mov_b64 v[18:19], 0
v_mov_b64 v[20:21], 0
v_mov_b64 v[22:23], 0
v_mov_b64 v[24:25], 0
v_mov_b64 v[26:27], 0
v_mov_b64 v[230:231], 0
v_mov_b64 v[232:233], 0
v_mov_b64 v[236:237], 0
v_mov_b64 v[238:239], 0
s_nop 0
s_barrier
ds_read_b64_tr_b16 a[64:65], v242 offset:0
ds_read_b64_tr_b16 a[66:67], v242 offset:256
ds_read_b64_tr_b16 a[68:69], v242 offset:4096
ds_read_b64_tr_b16 a[70:71], v242 offset:4352
ds_read_b128 a[0:3], v240 offset:0
ds_read_b128 a[4:7], v241 offset:0
ds_read_b128 a[8:11], v240 offset:2048
ds_read_b128 a[12:15], v241 offset:2048
ds_read_b128 a[16:19], v240 offset:4096
ds_read_b128 a[20:23], v241 offset:4096
ds_read_b128 a[24:27], v240 offset:6144
ds_read_b128 a[28:31], v241 offset:6144
ds_read_b64_tr_b16 a[72:73], v243 offset:0
ds_read_b64_tr_b16 a[74:75], v243 offset:256
ds_read_b64_tr_b16 a[76:77], v243 offset:4096
ds_read_b64_tr_b16 a[78:79], v243 offset:4352
ds_read_b64_tr_b16 a[80:81], v242 offset:512
ds_read_b64_tr_b16 a[82:83], v242 offset:768
ds_read_b64_tr_b16 a[84:85], v242 offset:4608
ds_read_b64_tr_b16 a[86:87], v242 offset:4864
ds_read_b64_tr_b16 a[88:89], v243 offset:512
ds_read_b64_tr_b16 a[90:91], v243 offset:768
ds_read_b64_tr_b16 a[92:93], v243 offset:4608
ds_read_b64_tr_b16 a[94:95], v243 offset:4864
s_mov_b32 s81, 4
s_cmp_eq_u32 s70, 1
s_cbranch_scc1 mla_fused_a_loopV
mla_fused_a_loopK:
s_waitcnt vmcnt(16) lgkmcnt(0)
s_barrier
s_cmp_lg_u32 s81, 1
s_cbranch_scc1 mla_fused_a_swE_lpK
s_mov_b64 s[36:37], %[x]
s_and_b32 s37, s37, 0xffff
s_mov_b32 s38, 0x200000
s_mov_b32 s39, 0x20000
s_mov_b64 s[44:45], %[wq]
s_and_b32 s45, s45, 0xffff
s_mov_b32 s46, 0x400000
s_mov_b32 s47, 0x20000
s_mov_b64 s[48:49], %[wq]
s_add_u32 s48, s48, 128
s_addc_u32 s49, s49, 0
s_and_b32 s49, s49, 0xffff
s_mov_b32 s50, 0x3fff80
s_mov_b32 s51, 0x20000
s_sub_u32 s82, 15, s72
s_cmp_eq_u32 s69, 0
s_cselect_b32 s88, s82, s72
s_lshl_b32 s73, s88, 17
s_lshl_b32 s82, s70, 15
s_add_u32 s73, s73, s82
s_lshl_b32 s75, s68, 17
s_lshl_b32 s82, s71, 8
s_add_u32 s75, s75, s82
s_mov_b32 s94, s73
s_mov_b32 s96, s75
s_and_b32 s83, s71, 3
s_lshl_b32 s83, s83, 1
s_add_u32 s86, s83, s72
s_add_u32 s86, s86, 7
s_add_u32 s86, s86, 1
s_and_b32 s86, s86, 7
s_lshl_b32 s83, s86, 7
s_add_u32 s73, s94, s83
s_lshl_b32 s83, s86, 19
s_add_u32 s75, s96, s83
mla_fused_a_swE_lpK:
v_mfma_f32_16x16x32_bf16 v[0:3], a[64:67], a[0:3], v[0:3]
ds_read_b64_tr_b16 a[128:129], v242 offset:8192
v_mfma_f32_16x16x32_bf16 v[0:3], a[68:71], a[4:7], v[0:3]
v_mfma_f32_16x16x32_bf16 v[4:7], a[64:67], a[8:11], v[4:7]
ds_read_b64_tr_b16 a[130:131], v242 offset:8448
v_mfma_f32_16x16x32_bf16 v[4:7], a[68:71], a[12:15], v[4:7]
s_add_u32 m0, s79, 0
s_add_u32 s82, s73, 0
v_mfma_f32_16x16x32_bf16 v[8:11], a[64:67], a[16:19], v[8:11]
v_mfma_f32_16x16x32_bf16 v[8:11], a[68:71], a[20:23], v[8:11]
ds_read_b64_tr_b16 a[132:133], v242 offset:12288
v_mfma_f32_16x16x32_bf16 v[12:15], a[64:67], a[24:27], v[12:15]
buffer_load_dwordx4 v246, s[36:39], s82 offen lds
v_mfma_f32_16x16x32_bf16 v[12:15], a[68:71], a[28:31], v[12:15]
ds_read_b64_tr_b16 a[134:135], v242 offset:12544
v_mfma_f32_16x16x32_bf16 v[128:131], a[72:75], a[0:3], v[128:131]
v_mfma_f32_16x16x32_bf16 v[128:131], a[76:79], a[4:7], v[128:131]
v_mfma_f32_16x16x32_bf16 v[132:135], a[72:75], a[8:11], v[132:135]
s_add_u32 m0, s80, 0
s_add_u32 s82, s75, 0
ds_read_b64_tr_b16 a[136:137], v243 offset:8192
v_mfma_f32_16x16x32_bf16 v[132:135], a[76:79], a[12:15], v[132:135]
v_mfma_f32_16x16x32_bf16 v[136:139], a[72:75], a[16:19], v[136:139]
ds_read_b64_tr_b16 a[138:139], v243 offset:8448
v_mfma_f32_16x16x32_bf16 v[136:139], a[76:79], a[20:23], v[136:139]
v_mfma_f32_16x16x32_bf16 v[140:143], a[72:75], a[24:27], v[140:143]
buffer_load_dwordx4 v248, s[44:47], s82 offen lds
v_mfma_f32_16x16x32_bf16 v[140:143], a[76:79], a[28:31], v[140:143]
ds_read_b64_tr_b16 a[140:141], v243 offset:12288
v_mfma_f32_16x16x32_bf16 v[32:35], a[80:83], a[0:3], v[32:35]
v_mfma_f32_16x16x32_bf16 v[32:35], a[84:87], a[4:7], v[32:35]
s_add_u32 m0, s79, 1024
s_add_u32 s82, s73, 8192
ds_read_b64_tr_b16 a[142:143], v243 offset:12544
v_mfma_f32_16x16x32_bf16 v[36:39], a[80:83], a[8:11], v[36:39]
v_mfma_f32_16x16x32_bf16 v[36:39], a[84:87], a[12:15], v[36:39]
v_mfma_f32_16x16x32_bf16 v[40:43], a[80:83], a[16:19], v[40:43]
ds_read_b128 a[32:35], v240 offset:8192
v_mfma_f32_16x16x32_bf16 v[40:43], a[84:87], a[20:23], v[40:43]
buffer_load_dwordx4 v247, s[36:39], s82 offen lds
v_mfma_f32_16x16x32_bf16 v[44:47], a[80:83], a[24:27], v[44:47]
ds_read_b128 a[36:39], v241 offset:8192
v_mfma_f32_16x16x32_bf16 v[44:47], a[84:87], a[28:31], v[44:47]
v_mfma_f32_16x16x32_bf16 v[160:163], a[88:91], a[0:3], v[160:163]
v_mfma_f32_16x16x32_bf16 v[160:163], a[92:95], a[4:7], v[160:163]
s_add_u32 m0, s80, 16384
s_add_u32 s82, s75, 0
ds_read_b64_tr_b16 a[144:145], v242 offset:8704
v_mfma_f32_16x16x32_bf16 v[164:167], a[88:91], a[8:11], v[164:167]
v_mfma_f32_16x16x32_bf16 v[164:167], a[92:95], a[12:15], v[164:167]
ds_read_b64_tr_b16 a[146:147], v242 offset:8960
v_mfma_f32_16x16x32_bf16 v[168:171], a[88:91], a[16:19], v[168:171]
buffer_load_dwordx4 v248, s[48:51], s82 offen lds
v_mfma_f32_16x16x32_bf16 v[168:171], a[92:95], a[20:23], v[168:171]
v_mfma_f32_16x16x32_bf16 v[172:175], a[88:91], a[24:27], v[172:175]
ds_read_b64_tr_b16 a[148:149], v242 offset:12800
v_mfma_f32_16x16x32_bf16 v[172:175], a[92:95], a[28:31], v[172:175]
s_waitcnt lgkmcnt(11)
v_mfma_f32_16x16x32_bf16 v[64:67], a[128:131], a[0:3], v[64:67]
s_add_u32 m0, s79, 2048
s_add_u32 s82, s73, 16384
ds_read_b64_tr_b16 a[150:151], v242 offset:13056
s_waitcnt lgkmcnt(10)
v_mfma_f32_16x16x32_bf16 v[64:67], a[132:135], a[4:7], v[64:67]
v_mfma_f32_16x16x32_bf16 v[68:71], a[128:131], a[8:11], v[68:71]
v_mfma_f32_16x16x32_bf16 v[68:71], a[132:135], a[12:15], v[68:71]
ds_read_b128 a[40:43], v240 offset:10240
v_mfma_f32_16x16x32_bf16 v[72:75], a[128:131], a[16:19], v[72:75]
buffer_load_dwordx4 v246, s[36:39], s82 offen lds
v_mfma_f32_16x16x32_bf16 v[72:75], a[132:135], a[20:23], v[72:75]
ds_read_b128 a[44:47], v241 offset:10240
v_mfma_f32_16x16x32_bf16 v[76:79], a[128:131], a[24:27], v[76:79]
v_mfma_f32_16x16x32_bf16 v[76:79], a[132:135], a[28:31], v[76:79]
s_add_u32 m0, s80, 1024
s_add_u32 s82, s75, 65536
s_waitcnt lgkmcnt(10)
v_mfma_f32_16x16x32_bf16 v[192:195], a[136:139], a[0:3], v[192:195]
ds_read_b64_tr_b16 a[152:153], v243 offset:8704
s_waitcnt lgkmcnt(9)
v_mfma_f32_16x16x32_bf16 v[192:195], a[140:143], a[4:7], v[192:195]
v_mfma_f32_16x16x32_bf16 v[196:199], a[136:139], a[8:11], v[196:199]
ds_read_b64_tr_b16 a[154:155], v243 offset:8960
v_mfma_f32_16x16x32_bf16 v[196:199], a[140:143], a[12:15], v[196:199]
buffer_load_dwordx4 v249, s[44:47], s82 offen lds
v_mfma_f32_16x16x32_bf16 v[200:203], a[136:139], a[16:19], v[200:203]
v_mfma_f32_16x16x32_bf16 v[200:203], a[140:143], a[20:23], v[200:203]
ds_read_b64_tr_b16 a[156:157], v243 offset:12800
v_mfma_f32_16x16x32_bf16 v[204:207], a[136:139], a[24:27], v[204:207]
v_mfma_f32_16x16x32_bf16 v[204:207], a[140:143], a[28:31], v[204:207]
s_add_u32 m0, s79, 3072
s_add_u32 s82, s73, 24576
ds_read_b64_tr_b16 a[158:159], v243 offset:13056
s_waitcnt lgkmcnt(8)
v_mfma_f32_16x16x32_bf16 v[96:99], a[144:147], a[0:3], v[96:99]
s_waitcnt lgkmcnt(6)
v_mfma_f32_16x16x32_bf16 v[96:99], a[148:151], a[4:7], v[96:99]
ds_read_b128 a[48:51], v240 offset:12288
v_mfma_f32_16x16x32_bf16 v[100:103], a[144:147], a[8:11], v[100:103]
buffer_load_dwordx4 v247, s[36:39], s82 offen lds
v_mfma_f32_16x16x32_bf16 v[100:103], a[148:151], a[12:15], v[100:103]
v_mfma_f32_16x16x32_bf16 v[104:107], a[144:147], a[16:19], v[104:107]
ds_read_b128 a[52:55], v241 offset:12288
v_mfma_f32_16x16x32_bf16 v[104:107], a[148:151], a[20:23], v[104:107]
v_mfma_f32_16x16x32_bf16 v[108:111], a[144:147], a[24:27], v[108:111]
s_add_u32 m0, s80, 17408
s_add_u32 s82, s75, 65536
v_mfma_f32_16x16x32_bf16 v[108:111], a[148:151], a[28:31], v[108:111]
ds_read_b128 a[56:59], v240 offset:14336
s_waitcnt lgkmcnt(5)
v_mfma_f32_16x16x32_bf16 v[224:227], a[152:155], a[0:3], v[224:227]
s_waitcnt lgkmcnt(3)
v_mfma_f32_16x16x32_bf16 v[224:227], a[156:159], a[4:7], v[224:227]
ds_read_b128 a[60:63], v241 offset:14336
v_mfma_f32_16x16x32_bf16 v[228:231], a[152:155], a[8:11], v[228:231]
buffer_load_dwordx4 v249, s[48:51], s82 offen lds
s_add_u32 s86, s86, 1
v_mfma_f32_16x16x32_bf16 v[228:231], a[156:159], a[12:15], v[228:231]
s_and_b32 s86, s86, 7
s_lshl_b32 s83, s86, 7
s_add_u32 s73, s94, s83
v_mfma_f32_16x16x32_bf16 v[232:235], a[152:155], a[16:19], v[232:235]
s_lshl_b32 s83, s86, 19
s_add_u32 s75, s96, s83
v_mfma_f32_16x16x32_bf16 v[232:235], a[156:159], a[20:23], v[232:235]
v_mfma_f32_16x16x32_bf16 v[236:239], a[152:155], a[24:27], v[236:239]
v_mfma_f32_16x16x32_bf16 v[236:239], a[156:159], a[28:31], v[236:239]
s_waitcnt vmcnt(16) lgkmcnt(0)
s_barrier
s_cmp_lg_u32 s81, 1
s_cbranch_scc1 mla_fused_a_swL_lpK
s_mov_b64 s[40:41], %[x]
s_and_b32 s41, s41, 0xffff
s_mov_b32 s42, 0x200000
s_mov_b32 s43, 0x20000
s_mov_b64 s[52:53], %[w1]
s_and_b32 s53, s53, 0xffff
s_mov_b32 s54, 0x1c0000
s_mov_b32 s55, 0x20000
s_cmp_ge_u32 s71, 28
s_cselect_b32 s54, 0, s54
s_mov_b32 s58, 0
s_lshl_b32 s74, s88, 17
s_lshl_b32 s82, s70, 15
s_add_u32 s74, s74, s82
s_add_u32 s74, s74, 65536
s_mul_i32 s76, s68, 57344
s_lshl_b32 s82, s71, 7
s_add_u32 s76, s76, s82
s_mov_b32 s77, 28672
s_mov_b32 s78, 229376
s_mov_b32 s95, s74
s_mov_b32 s97, s76
s_and_b32 s83, s71, 3
s_lshl_b32 s83, s83, 1
s_add_u32 s87, s83, s72
s_add_u32 s87, s87, 7
s_add_u32 s87, s87, 1
s_and_b32 s87, s87, 7
s_lshl_b32 s83, s87, 7
s_add_u32 s74, s95, s83
s_mul_i32 s83, s87, s78
s_add_u32 s76, s97, s83
v_and_b32 v253, 63, %[tid]
v_bfe_u32 v254, v253, 2, 3
s_mov_b32 s82, 4608
v_mul_u32_u24 v254, s82, v254
v_sub_u32 v250, v248, v254
v_sub_u32 v251, v249, v254
mla_fused_a_swL_lpK:
v_mfma_f32_16x16x32_bf16 v[80:83], a[128:131], a[32:35], v[80:83]
ds_read_b64_tr_b16 a[96:97], v242 offset:32768
v_mfma_f32_16x16x32_bf16 v[80:83], a[132:135], a[36:39], v[80:83]
v_mfma_f32_16x16x32_bf16 v[84:87], a[128:131], a[40:43], v[84:87]
ds_read_b64_tr_b16 a[98:99], v242 offset:33024
v_mfma_f32_16x16x32_bf16 v[84:87], a[132:135], a[44:47], v[84:87]
s_add_u32 m0, s79, 8192
s_add_u32 s82, s74, 0
v_mfma_f32_16x16x32_bf16 v[88:91], a[128:131], a[48:51], v[88:91]
ds_read_b64_tr_b16 a[100:101], v242 offset:36864
v_mfma_f32_16x16x32_bf16 v[88:91], a[132:135], a[52:55], v[88:91]
v_mfma_f32_16x16x32_bf16 v[92:95], a[128:131], a[56:59], v[92:95]
buffer_load_dwordx4 v246, s[40:43], s82 offen lds
v_mfma_f32_16x16x32_bf16 v[92:95], a[132:135], a[60:63], v[92:95]
ds_read_b64_tr_b16 a[102:103], v242 offset:37120
v_mfma_f32_16x16x32_bf16 v[208:211], a[136:139], a[32:35], v[208:211]
v_mfma_f32_16x16x32_bf16 v[208:211], a[140:143], a[36:39], v[208:211]
ds_read_b128 a[0:3], v240 offset:32768
v_mfma_f32_16x16x32_bf16 v[212:215], a[136:139], a[40:43], v[212:215]
s_add_u32 m0, s80, 8192
s_mov_b32 s82, s76
v_mfma_f32_16x16x32_bf16 v[212:215], a[140:143], a[44:47], v[212:215]
v_mfma_f32_16x16x32_bf16 v[216:219], a[136:139], a[48:51], v[216:219]
ds_read_b128 a[4:7], v241 offset:32768
v_mfma_f32_16x16x32_bf16 v[216:219], a[140:143], a[52:55], v[216:219]
ds_read_b128 a[8:11], v240 offset:34816
v_mfma_f32_16x16x32_bf16 v[220:223], a[136:139], a[56:59], v[220:223]
buffer_load_dwordx4 v250, s[52:55], s82 offen lds
)ASM"
R"ASM(v_mfma_f32_16x16x32_bf16 v[220:223], a[140:143], a[60:63], v[220:223]
v_mfma_f32_16x16x32_bf16 v[112:115], a[144:147], a[32:35], v[112:115]
v_mfma_f32_16x16x32_bf16 v[112:115], a[148:151], a[36:39], v[112:115]
s_add_u32 m0, s79, 9216
s_add_u32 s82, s74, 8192
ds_read_b128 a[12:15], v241 offset:34816
v_mfma_f32_16x16x32_bf16 v[116:119], a[144:147], a[40:43], v[116:119]
v_mfma_f32_16x16x32_bf16 v[116:119], a[148:151], a[44:47], v[116:119]
ds_read_b128 a[16:19], v240 offset:36864
v_mfma_f32_16x16x32_bf16 v[120:123], a[144:147], a[48:51], v[120:123]
v_mfma_f32_16x16x32_bf16 v[120:123], a[148:151], a[52:55], v[120:123]
buffer_load_dwordx4 v247, s[40:43], s82 offen lds
v_mfma_f32_16x16x32_bf16 v[124:127], a[144:147], a[56:59], v[124:127]
ds_read_b128 a[20:23], v241 offset:36864
v_mfma_f32_16x16x32_bf16 v[124:127], a[148:151], a[60:63], v[124:127]
v_mfma_f32_16x16x32_bf16 a[160:163], a[152:155], a[32:35], a[160:163]
ds_read_b128 a[24:27], v240 offset:38912
v_mfma_f32_16x16x32_bf16 a[160:163], a[156:159], a[36:39], a[160:163]
s_add_u32 m0, s80, 24576
s_mov_b32 s82, s76
v_mfma_f32_16x16x32_bf16 a[164:167], a[152:155], a[40:43], a[164:167]
v_mfma_f32_16x16x32_bf16 a[164:167], a[156:159], a[44:47], a[164:167]
ds_read_b128 a[28:31], v241 offset:38912
v_mfma_f32_16x16x32_bf16 a[168:171], a[152:155], a[48:51], a[168:171]
buffer_load_dwordx4 v250, s[56:59], s82 offen lds
v_mfma_f32_16x16x32_bf16 a[168:171], a[156:159], a[52:55], a[168:171]
ds_read_b64_tr_b16 a[104:105], v243 offset:32768
v_mfma_f32_16x16x32_bf16 a[172:175], a[152:155], a[56:59], a[172:175]
v_mfma_f32_16x16x32_bf16 a[172:175], a[156:159], a[60:63], a[172:175]
v_mfma_f32_16x16x32_bf16 v[16:19], a[64:67], a[32:35], v[16:19]
s_add_u32 m0, s79, 10240
s_add_u32 s82, s74, 16384
ds_read_b64_tr_b16 a[106:107], v243 offset:33024
v_mfma_f32_16x16x32_bf16 v[16:19], a[68:71], a[36:39], v[16:19]
v_mfma_f32_16x16x32_bf16 v[20:23], a[64:67], a[40:43], v[20:23]
ds_read_b64_tr_b16 a[108:109], v243 offset:36864
v_mfma_f32_16x16x32_bf16 v[20:23], a[68:71], a[44:47], v[20:23]
v_mfma_f32_16x16x32_bf16 v[24:27], a[64:67], a[48:51], v[24:27]
buffer_load_dwordx4 v246, s[40:43], s82 offen lds
v_mfma_f32_16x16x32_bf16 v[24:27], a[68:71], a[52:55], v[24:27]
ds_read_b64_tr_b16 a[110:111], v243 offset:37120
v_mfma_f32_16x16x32_bf16 v[28:31], a[64:67], a[56:59], v[28:31]
v_mfma_f32_16x16x32_bf16 v[28:31], a[68:71], a[60:63], v[28:31]
s_add_u32 m0, s80, 9216
s_add_u32 s82, s76, s77
ds_read_b64_tr_b16 a[112:113], v242 offset:33280
v_mfma_f32_16x16x32_bf16 v[144:147], a[72:75], a[32:35], v[144:147]
v_mfma_f32_16x16x32_bf16 v[144:147], a[76:79], a[36:39], v[144:147]
v_mfma_f32_16x16x32_bf16 v[148:151], a[72:75], a[40:43], v[148:151]
ds_read_b64_tr_b16 a[114:115], v242 offset:33536
v_mfma_f32_16x16x32_bf16 v[148:151], a[76:79], a[44:47], v[148:151]
buffer_load_dwordx4 v251, s[52:55], s82 offen lds
v_mfma_f32_16x16x32_bf16 v[152:155], a[72:75], a[48:51], v[152:155]
ds_read_b64_tr_b16 a[116:117], v242 offset:37376
v_mfma_f32_16x16x32_bf16 v[152:155], a[76:79], a[52:55], v[152:155]
v_mfma_f32_16x16x32_bf16 v[156:159], a[72:75], a[56:59], v[156:159]
v_mfma_f32_16x16x32_bf16 v[156:159], a[76:79], a[60:63], v[156:159]
s_add_u32 m0, s79, 11264
s_add_u32 s82, s74, 24576
ds_read_b64_tr_b16 a[118:119], v242 offset:37632
v_mfma_f32_16x16x32_bf16 v[48:51], a[80:83], a[32:35], v[48:51]
v_mfma_f32_16x16x32_bf16 v[48:51], a[84:87], a[36:39], v[48:51]
ds_read_b64_tr_b16 a[120:121], v243 offset:33280
v_mfma_f32_16x16x32_bf16 v[52:55], a[80:83], a[40:43], v[52:55]
buffer_load_dwordx4 v247, s[40:43], s82 offen lds
v_mfma_f32_16x16x32_bf16 v[52:55], a[84:87], a[44:47], v[52:55]
v_mfma_f32_16x16x32_bf16 v[56:59], a[80:83], a[48:51], v[56:59]
ds_read_b64_tr_b16 a[122:123], v243 offset:33536
v_mfma_f32_16x16x32_bf16 v[56:59], a[84:87], a[52:55], v[56:59]
v_mfma_f32_16x16x32_bf16 v[60:63], a[80:83], a[56:59], v[60:63]
s_add_u32 m0, s80, 25600
s_add_u32 s82, s76, s77
ds_read_b64_tr_b16 a[124:125], v243 offset:37376
v_mfma_f32_16x16x32_bf16 v[60:63], a[84:87], a[60:63], v[60:63]
v_mfma_f32_16x16x32_bf16 v[176:179], a[88:91], a[32:35], v[176:179]
v_mfma_f32_16x16x32_bf16 v[176:179], a[92:95], a[36:39], v[176:179]
ds_read_b64_tr_b16 a[126:127], v243 offset:37632
v_mfma_f32_16x16x32_bf16 v[180:183], a[88:91], a[40:43], v[180:183]
buffer_load_dwordx4 v251, s[56:59], s82 offen lds
s_add_u32 s87, s87, 1
v_mfma_f32_16x16x32_bf16 v[180:183], a[92:95], a[44:47], v[180:183]
s_and_b32 s87, s87, 7
s_lshl_b32 s83, s87, 7
s_add_u32 s74, s95, s83
v_mfma_f32_16x16x32_bf16 v[184:187], a[88:91], a[48:51], v[184:187]
s_mul_i32 s83, s87, s78
s_add_u32 s76, s97, s83
v_mfma_f32_16x16x32_bf16 v[184:187], a[92:95], a[52:55], v[184:187]
v_mfma_f32_16x16x32_bf16 v[188:191], a[88:91], a[56:59], v[188:191]
v_mfma_f32_16x16x32_bf16 v[188:191], a[92:95], a[60:63], v[188:191]
s_waitcnt vmcnt(16) lgkmcnt(0)
s_barrier
v_mfma_f32_16x16x32_bf16 v[0:3], a[96:99], a[0:3], v[0:3]
ds_read_b64_tr_b16 a[128:129], v242 offset:40960
v_mfma_f32_16x16x32_bf16 v[0:3], a[100:103], a[4:7], v[0:3]
v_mfma_f32_16x16x32_bf16 v[4:7], a[96:99], a[8:11], v[4:7]
ds_read_b64_tr_b16 a[130:131], v242 offset:41216
v_mfma_f32_16x16x32_bf16 v[4:7], a[100:103], a[12:15], v[4:7]
s_add_u32 m0, s79, 32768
s_add_u32 s82, s73, 0
v_mfma_f32_16x16x32_bf16 v[8:11], a[96:99], a[16:19], v[8:11]
v_mfma_f32_16x16x32_bf16 v[8:11], a[100:103], a[20:23], v[8:11]
ds_read_b64_tr_b16 a[132:133], v242 offset:45056
v_mfma_f32_16x16x32_bf16 v[12:15], a[96:99], a[24:27], v[12:15]
buffer_load_dwordx4 v246, s[36:39], s82 offen lds
v_mfma_f32_16x16x32_bf16 v[12:15], a[100:103], a[28:31], v[12:15]
ds_read_b64_tr_b16 a[134:135], v242 offset:45312
v_mfma_f32_16x16x32_bf16 v[128:131], a[104:107], a[0:3], v[128:131]
v_mfma_f32_16x16x32_bf16 v[128:131], a[108:111], a[4:7], v[128:131]
v_mfma_f32_16x16x32_bf16 v[132:135], a[104:107], a[8:11], v[132:135]
s_add_u32 m0, s80, 32768
s_add_u32 s82, s75, 0
ds_read_b64_tr_b16 a[136:137], v243 offset:40960
v_mfma_f32_16x16x32_bf16 v[132:135], a[108:111], a[12:15], v[132:135]
v_mfma_f32_16x16x32_bf16 v[136:139], a[104:107], a[16:19], v[136:139]
ds_read_b64_tr_b16 a[138:139], v243 offset:41216
v_mfma_f32_16x16x32_bf16 v[136:139], a[108:111], a[20:23], v[136:139]
v_mfma_f32_16x16x32_bf16 v[140:143], a[104:107], a[24:27], v[140:143]
buffer_load_dwordx4 v248, s[44:47], s82 offen lds
v_mfma_f32_16x16x32_bf16 v[140:143], a[108:111], a[28:31], v[140:143]
ds_read_b64_tr_b16 a[140:141], v243 offset:45056
v_mfma_f32_16x16x32_bf16 v[32:35], a[112:115], a[0:3], v[32:35]
v_mfma_f32_16x16x32_bf16 v[32:35], a[116:119], a[4:7], v[32:35]
s_add_u32 m0, s79, 33792
s_add_u32 s82, s73, 8192
ds_read_b64_tr_b16 a[142:143], v243 offset:45312
v_mfma_f32_16x16x32_bf16 v[36:39], a[112:115], a[8:11], v[36:39]
v_mfma_f32_16x16x32_bf16 v[36:39], a[116:119], a[12:15], v[36:39]
v_mfma_f32_16x16x32_bf16 v[40:43], a[112:115], a[16:19], v[40:43]
ds_read_b128 a[32:35], v240 offset:40960
v_mfma_f32_16x16x32_bf16 v[40:43], a[116:119], a[20:23], v[40:43]
buffer_load_dwordx4 v247, s[36:39], s82 offen lds
v_mfma_f32_16x16x32_bf16 v[44:47], a[112:115], a[24:27], v[44:47]
ds_read_b128 a[36:39], v241 offset:40960
v_mfma_f32_16x16x32_bf16 v[44:47], a[116:119], a[28:31], v[44:47]
v_mfma_f32_16x16x32_bf16 v[160:163], a[120:123], a[0:3], v[160:163]
v_mfma_f32_16x16x32_bf16 v[160:163], a[124:127], a[4:7], v[160:163]
s_add_u32 m0, s80, 49152
s_add_u32 s82, s75, 0
ds_read_b64_tr_b16 a[144:145], v242 offset:41472
v_mfma_f32_16x16x32_bf16 v[164:167], a[120:123], a[8:11], v[164:167]
v_mfma_f32_16x16x32_bf16 v[164:167], a[124:127], a[12:15], v[164:167]
ds_read_b64_tr_b16 a[146:147], v242 offset:41728
v_mfma_f32_16x16x32_bf16 v[168:171], a[120:123], a[16:19], v[168:171]
buffer_load_dwordx4 v248, s[48:51], s82 offen lds
v_mfma_f32_16x16x32_bf16 v[168:171], a[124:127], a[20:23], v[168:171]
v_mfma_f32_16x16x32_bf16 v[172:175], a[120:123], a[24:27], v[172:175]
ds_read_b64_tr_b16 a[148:149], v242 offset:45568
v_mfma_f32_16x16x32_bf16 v[172:175], a[124:127], a[28:31], v[172:175]
s_waitcnt lgkmcnt(11)
v_mfma_f32_16x16x32_bf16 v[64:67], a[128:131], a[0:3], v[64:67]
s_add_u32 m0, s79, 34816
s_add_u32 s82, s73, 16384
ds_read_b64_tr_b16 a[150:151], v242 offset:45824
s_waitcnt lgkmcnt(10)
v_mfma_f32_16x16x32_bf16 v[64:67], a[132:135], a[4:7], v[64:67]
v_mfma_f32_16x16x32_bf16 v[68:71], a[128:131], a[8:11], v[68:71]
v_mfma_f32_16x16x32_bf16 v[68:71], a[132:135], a[12:15], v[68:71]
ds_read_b128 a[40:43], v240 offset:43008
v_mfma_f32_16x16x32_bf16 v[72:75], a[128:131], a[16:19], v[72:75]
buffer_load_dwordx4 v246, s[36:39], s82 offen lds
v_mfma_f32_16x16x32_bf16 v[72:75], a[132:135], a[20:23], v[72:75]
ds_read_b128 a[44:47], v241 offset:43008
v_mfma_f32_16x16x32_bf16 v[76:79], a[128:131], a[24:27], v[76:79]
v_mfma_f32_16x16x32_bf16 v[76:79], a[132:135], a[28:31], v[76:79]
s_add_u32 m0, s80, 33792
s_add_u32 s82, s75, 65536
s_waitcnt lgkmcnt(10)
v_mfma_f32_16x16x32_bf16 v[192:195], a[136:139], a[0:3], v[192:195]
ds_read_b64_tr_b16 a[152:153], v243 offset:41472
s_waitcnt lgkmcnt(9)
v_mfma_f32_16x16x32_bf16 v[192:195], a[140:143], a[4:7], v[192:195]
v_mfma_f32_16x16x32_bf16 v[196:199], a[136:139], a[8:11], v[196:199]
ds_read_b64_tr_b16 a[154:155], v243 offset:41728
v_mfma_f32_16x16x32_bf16 v[196:199], a[140:143], a[12:15], v[196:199]
buffer_load_dwordx4 v249, s[44:47], s82 offen lds
v_mfma_f32_16x16x32_bf16 v[200:203], a[136:139], a[16:19], v[200:203]
v_mfma_f32_16x16x32_bf16 v[200:203], a[140:143], a[20:23], v[200:203]
ds_read_b64_tr_b16 a[156:157], v243 offset:45568
v_mfma_f32_16x16x32_bf16 v[204:207], a[136:139], a[24:27], v[204:207]
v_mfma_f32_16x16x32_bf16 v[204:207], a[140:143], a[28:31], v[204:207]
s_add_u32 m0, s79, 35840
s_add_u32 s82, s73, 24576
ds_read_b64_tr_b16 a[158:159], v243 offset:45824
s_waitcnt lgkmcnt(8)
v_mfma_f32_16x16x32_bf16 v[96:99], a[144:147], a[0:3], v[96:99]
s_waitcnt lgkmcnt(6)
v_mfma_f32_16x16x32_bf16 v[96:99], a[148:151], a[4:7], v[96:99]
ds_read_b128 a[48:51], v240 offset:45056
v_mfma_f32_16x16x32_bf16 v[100:103], a[144:147], a[8:11], v[100:103]
buffer_load_dwordx4 v247, s[36:39], s82 offen lds
v_mfma_f32_16x16x32_bf16 v[100:103], a[148:151], a[12:15], v[100:103]
v_mfma_f32_16x16x32_bf16 v[104:107], a[144:147], a[16:19], v[104:107]
ds_read_b128 a[52:55], v241 offset:45056
v_mfma_f32_16x16x32_bf16 v[104:107], a[148:151], a[20:23], v[104:107]
v_mfma_f32_16x16x32_bf16 v[108:111], a[144:147], a[24:27], v[108:111]
s_add_u32 m0, s80, 50176
s_add_u32 s82, s75, 65536
v_mfma_f32_16x16x32_bf16 v[108:111], a[148:151], a[28:31], v[108:111]
ds_read_b128 a[56:59], v240 offset:47104
s_waitcnt lgkmcnt(5)
v_mfma_f32_16x16x32_bf16 v[224:227], a[152:155], a[0:3], v[224:227]
s_waitcnt lgkmcnt(3)
v_mfma_f32_16x16x32_bf16 v[224:227], a[156:159], a[4:7], v[224:227]
ds_read_b128 a[60:63], v241 offset:47104
v_mfma_f32_16x16x32_bf16 v[228:231], a[152:155], a[8:11], v[228:231]
buffer_load_dwordx4 v249, s[48:51], s82 offen lds
s_add_u32 s86, s86, 1
v_mfma_f32_16x16x32_bf16 v[228:231], a[156:159], a[12:15], v[228:231]
s_and_b32 s86, s86, 7
s_lshl_b32 s83, s86, 7
s_add_u32 s73, s94, s83
v_mfma_f32_16x16x32_bf16 v[232:235], a[152:155], a[16:19], v[232:235]
s_lshl_b32 s83, s86, 19
s_add_u32 s75, s96, s83
v_mfma_f32_16x16x32_bf16 v[232:235], a[156:159], a[20:23], v[232:235]
v_mfma_f32_16x16x32_bf16 v[236:239], a[152:155], a[24:27], v[236:239]
v_mfma_f32_16x16x32_bf16 v[236:239], a[156:159], a[28:31], v[236:239]
s_waitcnt vmcnt(16) lgkmcnt(0)
s_barrier
v_mfma_f32_16x16x32_bf16 v[80:83], a[128:131], a[32:35], v[80:83]
ds_read_b64_tr_b16 a[64:65], v242 offset:0
v_mfma_f32_16x16x32_bf16 v[80:83], a[132:135], a[36:39], v[80:83]
v_mfma_f32_16x16x32_bf16 v[84:87], a[128:131], a[40:43], v[84:87]
ds_read_b64_tr_b16 a[66:67], v242 offset:256
v_mfma_f32_16x16x32_bf16 v[84:87], a[132:135], a[44:47], v[84:87]
s_add_u32 m0, s79, 40960
s_add_u32 s82, s74, 0
v_mfma_f32_16x16x32_bf16 v[88:91], a[128:131], a[48:51], v[88:91]
ds_read_b64_tr_b16 a[68:69], v242 offset:4096
v_mfma_f32_16x16x32_bf16 v[88:91], a[132:135], a[52:55], v[88:91]
)ASM"
R"ASM(v_mfma_f32_16x16x32_bf16 v[92:95], a[128:131], a[56:59], v[92:95]
buffer_load_dwordx4 v246, s[40:43], s82 offen lds
v_mfma_f32_16x16x32_bf16 v[92:95], a[132:135], a[60:63], v[92:95]
ds_read_b64_tr_b16 a[70:71], v242 offset:4352
v_mfma_f32_16x16x32_bf16 v[208:211], a[136:139], a[32:35], v[208:211]
v_mfma_f32_16x16x32_bf16 v[208:211], a[140:143], a[36:39], v[208:211]
ds_read_b128 a[0:3], v240 offset:0
v_mfma_f32_16x16x32_bf16 v[212:215], a[136:139], a[40:43], v[212:215]
s_add_u32 m0, s80, 40960
s_mov_b32 s82, s76
v_mfma_f32_16x16x32_bf16 v[212:215], a[140:143], a[44:47], v[212:215]
v_mfma_f32_16x16x32_bf16 v[216:219], a[136:139], a[48:51], v[216:219]
ds_read_b128 a[4:7], v241 offset:0
v_mfma_f32_16x16x32_bf16 v[216:219], a[140:143], a[52:55], v[216:219]
ds_read_b128 a[8:11], v240 offset:2048
v_mfma_f32_16x16x32_bf16 v[220:223], a[136:139], a[56:59], v[220:223]
buffer_load_dwordx4 v250, s[52:55], s82 offen lds
v_mfma_f32_16x16x32_bf16 v[220:223], a[140:143], a[60:63], v[220:223]
v_mfma_f32_16x16x32_bf16 v[112:115], a[144:147], a[32:35], v[112:115]
v_mfma_f32_16x16x32_bf16 v[112:115], a[148:151], a[36:39], v[112:115]
s_add_u32 m0, s79, 41984
s_add_u32 s82, s74, 8192
ds_read_b128 a[12:15], v241 offset:2048
v_mfma_f32_16x16x32_bf16 v[116:119], a[144:147], a[40:43], v[116:119]
v_mfma_f32_16x16x32_bf16 v[116:119], a[148:151], a[44:47], v[116:119]
ds_read_b128 a[16:19], v240 offset:4096
v_mfma_f32_16x16x32_bf16 v[120:123], a[144:147], a[48:51], v[120:123]
v_mfma_f32_16x16x32_bf16 v[120:123], a[148:151], a[52:55], v[120:123]
buffer_load_dwordx4 v247, s[40:43], s82 offen lds
v_mfma_f32_16x16x32_bf16 v[124:127], a[144:147], a[56:59], v[124:127]
ds_read_b128 a[20:23], v241 offset:4096
v_mfma_f32_16x16x32_bf16 v[124:127], a[148:151], a[60:63], v[124:127]
v_mfma_f32_16x16x32_bf16 a[160:163], a[152:155], a[32:35], a[160:163]
ds_read_b128 a[24:27], v240 offset:6144
v_mfma_f32_16x16x32_bf16 a[160:163], a[156:159], a[36:39], a[160:163]
s_add_u32 m0, s80, 57344
s_mov_b32 s82, s76
v_mfma_f32_16x16x32_bf16 a[164:167], a[152:155], a[40:43], a[164:167]
v_mfma_f32_16x16x32_bf16 a[164:167], a[156:159], a[44:47], a[164:167]
ds_read_b128 a[28:31], v241 offset:6144
v_mfma_f32_16x16x32_bf16 a[168:171], a[152:155], a[48:51], a[168:171]
buffer_load_dwordx4 v250, s[56:59], s82 offen lds
v_mfma_f32_16x16x32_bf16 a[168:171], a[156:159], a[52:55], a[168:171]
ds_read_b64_tr_b16 a[72:73], v243 offset:0
v_mfma_f32_16x16x32_bf16 a[172:175], a[152:155], a[56:59], a[172:175]
v_mfma_f32_16x16x32_bf16 a[172:175], a[156:159], a[60:63], a[172:175]
v_mfma_f32_16x16x32_bf16 v[16:19], a[96:99], a[32:35], v[16:19]
s_add_u32 m0, s79, 43008
s_add_u32 s82, s74, 16384
ds_read_b64_tr_b16 a[74:75], v243 offset:256
v_mfma_f32_16x16x32_bf16 v[16:19], a[100:103], a[36:39], v[16:19]
v_mfma_f32_16x16x32_bf16 v[20:23], a[96:99], a[40:43], v[20:23]
ds_read_b64_tr_b16 a[76:77], v243 offset:4096
v_mfma_f32_16x16x32_bf16 v[20:23], a[100:103], a[44:47], v[20:23]
v_mfma_f32_16x16x32_bf16 v[24:27], a[96:99], a[48:51], v[24:27]
buffer_load_dwordx4 v246, s[40:43], s82 offen lds
v_mfma_f32_16x16x32_bf16 v[24:27], a[100:103], a[52:55], v[24:27]
ds_read_b64_tr_b16 a[78:79], v243 offset:4352
v_mfma_f32_16x16x32_bf16 v[28:31], a[96:99], a[56:59], v[28:31]
v_mfma_f32_16x16x32_bf16 v[28:31], a[100:103], a[60:63], v[28:31]
s_add_u32 m0, s80, 41984
s_add_u32 s82, s76, s77
ds_read_b64_tr_b16 a[80:81], v242 offset:512
v_mfma_f32_16x16x32_bf16 v[144:147], a[104:107], a[32:35], v[144:147]
v_mfma_f32_16x16x32_bf16 v[144:147], a[108:111], a[36:39], v[144:147]
v_mfma_f32_16x16x32_bf16 v[148:151], a[104:107], a[40:43], v[148:151]
ds_read_b64_tr_b16 a[82:83], v242 offset:768
v_mfma_f32_16x16x32_bf16 v[148:151], a[108:111], a[44:47], v[148:151]
buffer_load_dwordx4 v251, s[52:55], s82 offen lds
v_mfma_f32_16x16x32_bf16 v[152:155], a[104:107], a[48:51], v[152:155]
ds_read_b64_tr_b16 a[84:85], v242 offset:4608
v_mfma_f32_16x16x32_bf16 v[152:155], a[108:111], a[52:55], v[152:155]
v_mfma_f32_16x16x32_bf16 v[156:159], a[104:107], a[56:59], v[156:159]
v_mfma_f32_16x16x32_bf16 v[156:159], a[108:111], a[60:63], v[156:159]
s_add_u32 m0, s79, 44032
s_add_u32 s82, s74, 24576
ds_read_b64_tr_b16 a[86:87], v242 offset:4864
v_mfma_f32_16x16x32_bf16 v[48:51], a[112:115], a[32:35], v[48:51]
v_mfma_f32_16x16x32_bf16 v[48:51], a[116:119], a[36:39], v[48:51]
ds_read_b64_tr_b16 a[88:89], v243 offset:512
v_mfma_f32_16x16x32_bf16 v[52:55], a[112:115], a[40:43], v[52:55]
buffer_load_dwordx4 v247, s[40:43], s82 offen lds
v_mfma_f32_16x16x32_bf16 v[52:55], a[116:119], a[44:47], v[52:55]
v_mfma_f32_16x16x32_bf16 v[56:59], a[112:115], a[48:51], v[56:59]
ds_read_b64_tr_b16 a[90:91], v243 offset:768
v_mfma_f32_16x16x32_bf16 v[56:59], a[116:119], a[52:55], v[56:59]
v_mfma_f32_16x16x32_bf16 v[60:63], a[112:115], a[56:59], v[60:63]
s_add_u32 m0, s80, 58368
s_add_u32 s82, s76, s77
ds_read_b64_tr_b16 a[92:93], v243 offset:4608
v_mfma_f32_16x16x32_bf16 v[60:63], a[116:119], a[60:63], v[60:63]
v_mfma_f32_16x16x32_bf16 v[176:179], a[120:123], a[32:35], v[176:179]
v_mfma_f32_16x16x32_bf16 v[176:179], a[124:127], a[36:39], v[176:179]
ds_read_b64_tr_b16 a[94:95], v243 offset:4864
v_mfma_f32_16x16x32_bf16 v[180:183], a[120:123], a[40:43], v[180:183]
buffer_load_dwordx4 v251, s[56:59], s82 offen lds
s_add_u32 s87, s87, 1
v_mfma_f32_16x16x32_bf16 v[180:183], a[124:127], a[44:47], v[180:183]
s_and_b32 s87, s87, 7
s_lshl_b32 s83, s87, 7
s_add_u32 s74, s95, s83
v_mfma_f32_16x16x32_bf16 v[184:187], a[120:123], a[48:51], v[184:187]
s_mul_i32 s83, s87, s78
s_add_u32 s76, s97, s83
v_mfma_f32_16x16x32_bf16 v[184:187], a[124:127], a[52:55], v[184:187]
v_mfma_f32_16x16x32_bf16 v[188:191], a[120:123], a[56:59], v[188:191]
v_mfma_f32_16x16x32_bf16 v[188:191], a[124:127], a[60:63], v[188:191]
s_sub_u32 s81, s81, 1
s_cmp_lg_u32 s81, 0
s_cbranch_scc1 mla_fused_a_loopK
s_branch mla_fused_a_park
mla_fused_a_loopV:
s_waitcnt vmcnt(16) lgkmcnt(0)
s_barrier
s_cmp_lg_u32 s81, 1
s_cbranch_scc1 mla_fused_a_swE_lpV
s_mov_b64 s[36:37], %[x]
s_and_b32 s37, s37, 0xffff
s_mov_b32 s38, 0x200000
s_mov_b32 s39, 0x20000
s_mov_b64 s[44:45], %[wq]
s_and_b32 s45, s45, 0xffff
s_mov_b32 s46, 0x400000
s_mov_b32 s47, 0x20000
s_mov_b64 s[48:49], %[wq]
s_add_u32 s48, s48, 128
s_addc_u32 s49, s49, 0
s_and_b32 s49, s49, 0xffff
s_mov_b32 s50, 0x3fff80
s_mov_b32 s51, 0x20000
s_sub_u32 s82, 15, s72
s_cmp_eq_u32 s69, 0
s_cselect_b32 s88, s82, s72
s_lshl_b32 s73, s88, 17
s_lshl_b32 s82, s70, 15
s_add_u32 s73, s73, s82
s_lshl_b32 s75, s68, 17
s_lshl_b32 s82, s71, 8
s_add_u32 s75, s75, s82
s_mov_b32 s94, s73
s_mov_b32 s96, s75
s_and_b32 s83, s71, 3
s_lshl_b32 s83, s83, 1
s_add_u32 s86, s83, s72
s_add_u32 s86, s86, 7
s_add_u32 s86, s86, 1
s_and_b32 s86, s86, 7
s_lshl_b32 s83, s86, 7
s_add_u32 s73, s94, s83
s_lshl_b32 s83, s86, 19
s_add_u32 s75, s96, s83
mla_fused_a_swE_lpV:
v_mfma_f32_16x16x32_bf16 v[0:3], a[0:3], a[64:67], v[0:3]
ds_read_b64_tr_b16 a[128:129], v242 offset:8192
v_mfma_f32_16x16x32_bf16 v[0:3], a[4:7], a[68:71], v[0:3]
v_mfma_f32_16x16x32_bf16 v[128:131], a[8:11], a[64:67], v[128:131]
ds_read_b64_tr_b16 a[130:131], v242 offset:8448
v_mfma_f32_16x16x32_bf16 v[128:131], a[12:15], a[68:71], v[128:131]
s_add_u32 m0, s79, 0
s_add_u32 s82, s73, 0
v_mfma_f32_16x16x32_bf16 v[32:35], a[16:19], a[64:67], v[32:35]
v_mfma_f32_16x16x32_bf16 v[32:35], a[20:23], a[68:71], v[32:35]
ds_read_b64_tr_b16 a[132:133], v242 offset:12288
v_mfma_f32_16x16x32_bf16 v[160:163], a[24:27], a[64:67], v[160:163]
buffer_load_dwordx4 v246, s[36:39], s82 offen lds
v_mfma_f32_16x16x32_bf16 v[160:163], a[28:31], a[68:71], v[160:163]
ds_read_b64_tr_b16 a[134:135], v242 offset:12544
v_mfma_f32_16x16x32_bf16 v[4:7], a[0:3], a[72:75], v[4:7]
v_mfma_f32_16x16x32_bf16 v[4:7], a[4:7], a[76:79], v[4:7]
v_mfma_f32_16x16x32_bf16 v[132:135], a[8:11], a[72:75], v[132:135]
s_add_u32 m0, s80, 0
s_add_u32 s82, s75, 0
ds_read_b64_tr_b16 a[136:137], v243 offset:8192
v_mfma_f32_16x16x32_bf16 v[132:135], a[12:15], a[76:79], v[132:135]
v_mfma_f32_16x16x32_bf16 v[36:39], a[16:19], a[72:75], v[36:39]
ds_read_b64_tr_b16 a[138:139], v243 offset:8448
v_mfma_f32_16x16x32_bf16 v[36:39], a[20:23], a[76:79], v[36:39]
v_mfma_f32_16x16x32_bf16 v[164:167], a[24:27], a[72:75], v[164:167]
buffer_load_dwordx4 v248, s[44:47], s82 offen lds
v_mfma_f32_16x16x32_bf16 v[164:167], a[28:31], a[76:79], v[164:167]
ds_read_b64_tr_b16 a[140:141], v243 offset:12288
v_mfma_f32_16x16x32_bf16 v[8:11], a[0:3], a[80:83], v[8:11]
v_mfma_f32_16x16x32_bf16 v[8:11], a[4:7], a[84:87], v[8:11]
s_add_u32 m0, s79, 1024
s_add_u32 s82, s73, 8192
ds_read_b64_tr_b16 a[142:143], v243 offset:12544
v_mfma_f32_16x16x32_bf16 v[136:139], a[8:11], a[80:83], v[136:139]
v_mfma_f32_16x16x32_bf16 v[136:139], a[12:15], a[84:87], v[136:139]
v_mfma_f32_16x16x32_bf16 v[40:43], a[16:19], a[80:83], v[40:43]
ds_read_b128 a[32:35], v240 offset:8192
v_mfma_f32_16x16x32_bf16 v[40:43], a[20:23], a[84:87], v[40:43]
buffer_load_dwordx4 v247, s[36:39], s82 offen lds
v_mfma_f32_16x16x32_bf16 v[168:171], a[24:27], a[80:83], v[168:171]
ds_read_b128 a[36:39], v241 offset:8192
v_mfma_f32_16x16x32_bf16 v[168:171], a[28:31], a[84:87], v[168:171]
v_mfma_f32_16x16x32_bf16 v[12:15], a[0:3], a[88:91], v[12:15]
v_mfma_f32_16x16x32_bf16 v[12:15], a[4:7], a[92:95], v[12:15]
s_add_u32 m0, s80, 16384
s_add_u32 s82, s75, 0
ds_read_b64_tr_b16 a[144:145], v242 offset:8704
v_mfma_f32_16x16x32_bf16 v[140:143], a[8:11], a[88:91], v[140:143]
v_mfma_f32_16x16x32_bf16 v[140:143], a[12:15], a[92:95], v[140:143]
ds_read_b64_tr_b16 a[146:147], v242 offset:8960
v_mfma_f32_16x16x32_bf16 v[44:47], a[16:19], a[88:91], v[44:47]
buffer_load_dwordx4 v248, s[48:51], s82 offen lds
v_mfma_f32_16x16x32_bf16 v[44:47], a[20:23], a[92:95], v[44:47]
v_mfma_f32_16x16x32_bf16 v[172:175], a[24:27], a[88:91], v[172:175]
ds_read_b64_tr_b16 a[148:149], v242 offset:12800
v_mfma_f32_16x16x32_bf16 v[172:175], a[28:31], a[92:95], v[172:175]
s_waitcnt lgkmcnt(11)
v_mfma_f32_16x16x32_bf16 v[16:19], a[0:3], a[128:131], v[16:19]
s_add_u32 m0, s79, 2048
s_add_u32 s82, s73, 16384
ds_read_b64_tr_b16 a[150:151], v242 offset:13056
s_waitcnt lgkmcnt(10)
v_mfma_f32_16x16x32_bf16 v[16:19], a[4:7], a[132:135], v[16:19]
v_mfma_f32_16x16x32_bf16 v[144:147], a[8:11], a[128:131], v[144:147]
v_mfma_f32_16x16x32_bf16 v[144:147], a[12:15], a[132:135], v[144:147]
ds_read_b128 a[40:43], v240 offset:10240
v_mfma_f32_16x16x32_bf16 v[48:51], a[16:19], a[128:131], v[48:51]
buffer_load_dwordx4 v246, s[36:39], s82 offen lds
v_mfma_f32_16x16x32_bf16 v[48:51], a[20:23], a[132:135], v[48:51]
ds_read_b128 a[44:47], v241 offset:10240
v_mfma_f32_16x16x32_bf16 v[176:179], a[24:27], a[128:131], v[176:179]
v_mfma_f32_16x16x32_bf16 v[176:179], a[28:31], a[132:135], v[176:179]
s_add_u32 m0, s80, 1024
s_add_u32 s82, s75, 65536
s_waitcnt lgkmcnt(10)
v_mfma_f32_16x16x32_bf16 v[20:23], a[0:3], a[136:139], v[20:23]
ds_read_b64_tr_b16 a[152:153], v243 offset:8704
s_waitcnt lgkmcnt(9)
v_mfma_f32_16x16x32_bf16 v[20:23], a[4:7], a[140:143], v[20:23]
v_mfma_f32_16x16x32_bf16 v[148:151], a[8:11], a[136:139], v[148:151]
ds_read_b64_tr_b16 a[154:155], v243 offset:8960
v_mfma_f32_16x16x32_bf16 v[148:151], a[12:15], a[140:143], v[148:151]
buffer_load_dwordx4 v249, s[44:47], s82 offen lds
v_mfma_f32_16x16x32_bf16 v[52:55], a[16:19], a[136:139], v[52:55]
v_mfma_f32_16x16x32_bf16 v[52:55], a[20:23], a[140:143], v[52:55]
ds_read_b64_tr_b16 a[156:157], v243 offset:12800
v_mfma_f32_16x16x32_bf16 v[180:183], a[24:27], a[136:139], v[180:183]
v_mfma_f32_16x16x32_bf16 v[180:183], a[28:31], a[140:143], v[180:183]
s_add_u32 m0, s79, 3072
s_add_u32 s82, s73, 24576
ds_read_b64_tr_b16 a[158:159], v243 offset:13056
s_waitcnt lgkmcnt(8)
v_mfma_f32_16x16x32_bf16 v[24:27], a[0:3], a[144:147], v[24:27]
s_waitcnt lgkmcnt(6)
v_mfma_f32_16x16x32_bf16 v[24:27], a[4:7], a[148:151], v[24:27]
ds_read_b128 a[48:51], v240 offset:12288
v_mfma_f32_16x16x32_bf16 v[152:155], a[8:11], a[144:147], v[152:155]
buffer_load_dwordx4 v247, s[36:39], s82 offen lds
v_mfma_f32_16x16x32_bf16 v[152:155], a[12:15], a[148:151], v[152:155]
)ASM"
R"ASM(v_mfma_f32_16x16x32_bf16 v[56:59], a[16:19], a[144:147], v[56:59]
ds_read_b128 a[52:55], v241 offset:12288
v_mfma_f32_16x16x32_bf16 v[56:59], a[20:23], a[148:151], v[56:59]
v_mfma_f32_16x16x32_bf16 v[184:187], a[24:27], a[144:147], v[184:187]
s_add_u32 m0, s80, 17408
s_add_u32 s82, s75, 65536
v_mfma_f32_16x16x32_bf16 v[184:187], a[28:31], a[148:151], v[184:187]
ds_read_b128 a[56:59], v240 offset:14336
s_waitcnt lgkmcnt(5)
v_mfma_f32_16x16x32_bf16 v[28:31], a[0:3], a[152:155], v[28:31]
s_waitcnt lgkmcnt(3)
v_mfma_f32_16x16x32_bf16 v[28:31], a[4:7], a[156:159], v[28:31]
ds_read_b128 a[60:63], v241 offset:14336
v_mfma_f32_16x16x32_bf16 v[156:159], a[8:11], a[152:155], v[156:159]
buffer_load_dwordx4 v249, s[48:51], s82 offen lds
s_add_u32 s86, s86, 1
v_mfma_f32_16x16x32_bf16 v[156:159], a[12:15], a[156:159], v[156:159]
s_and_b32 s86, s86, 7
s_lshl_b32 s83, s86, 7
s_add_u32 s73, s94, s83
v_mfma_f32_16x16x32_bf16 v[60:63], a[16:19], a[152:155], v[60:63]
s_lshl_b32 s83, s86, 19
s_add_u32 s75, s96, s83
v_mfma_f32_16x16x32_bf16 v[60:63], a[20:23], a[156:159], v[60:63]
v_mfma_f32_16x16x32_bf16 v[188:191], a[24:27], a[152:155], v[188:191]
v_mfma_f32_16x16x32_bf16 v[188:191], a[28:31], a[156:159], v[188:191]
s_waitcnt vmcnt(16) lgkmcnt(0)
s_barrier
s_cmp_lg_u32 s81, 1
s_cbranch_scc1 mla_fused_a_swL_lpV
s_mov_b64 s[40:41], %[x]
s_and_b32 s41, s41, 0xffff
s_mov_b32 s42, 0x200000
s_mov_b32 s43, 0x20000
s_mov_b64 s[52:53], %[w1]
s_and_b32 s53, s53, 0xffff
s_mov_b32 s54, 0x1c0000
s_mov_b32 s55, 0x20000
s_cmp_ge_u32 s71, 28
s_cselect_b32 s54, 0, s54
s_mov_b32 s58, 0
s_lshl_b32 s74, s88, 17
s_lshl_b32 s82, s70, 15
s_add_u32 s74, s74, s82
s_add_u32 s74, s74, 65536
s_mul_i32 s76, s68, 57344
s_lshl_b32 s82, s71, 7
s_add_u32 s76, s76, s82
s_mov_b32 s77, 28672
s_mov_b32 s78, 229376
s_mov_b32 s95, s74
s_mov_b32 s97, s76
s_and_b32 s83, s71, 3
s_lshl_b32 s83, s83, 1
s_add_u32 s87, s83, s72
s_add_u32 s87, s87, 7
s_add_u32 s87, s87, 1
s_and_b32 s87, s87, 7
s_lshl_b32 s83, s87, 7
s_add_u32 s74, s95, s83
s_mul_i32 s83, s87, s78
s_add_u32 s76, s97, s83
v_and_b32 v253, 63, %[tid]
v_bfe_u32 v254, v253, 2, 3
s_mov_b32 s82, 4608
v_mul_u32_u24 v254, s82, v254
v_sub_u32 v250, v248, v254
v_sub_u32 v251, v249, v254
mla_fused_a_swL_lpV:
v_mfma_f32_16x16x32_bf16 v[80:83], a[32:35], a[128:131], v[80:83]
ds_read_b64_tr_b16 a[96:97], v242 offset:32768
v_mfma_f32_16x16x32_bf16 v[80:83], a[36:39], a[132:135], v[80:83]
v_mfma_f32_16x16x32_bf16 v[208:211], a[40:43], a[128:131], v[208:211]
ds_read_b64_tr_b16 a[98:99], v242 offset:33024
v_mfma_f32_16x16x32_bf16 v[208:211], a[44:47], a[132:135], v[208:211]
s_add_u32 m0, s79, 8192
s_add_u32 s82, s74, 0
v_mfma_f32_16x16x32_bf16 v[112:115], a[48:51], a[128:131], v[112:115]
ds_read_b64_tr_b16 a[100:101], v242 offset:36864
v_mfma_f32_16x16x32_bf16 v[112:115], a[52:55], a[132:135], v[112:115]
v_mfma_f32_16x16x32_bf16 a[160:163], a[56:59], a[128:131], a[160:163]
buffer_load_dwordx4 v246, s[40:43], s82 offen lds
v_mfma_f32_16x16x32_bf16 a[160:163], a[60:63], a[132:135], a[160:163]
ds_read_b64_tr_b16 a[102:103], v242 offset:37120
v_mfma_f32_16x16x32_bf16 v[84:87], a[32:35], a[136:139], v[84:87]
v_mfma_f32_16x16x32_bf16 v[84:87], a[36:39], a[140:143], v[84:87]
ds_read_b128 a[0:3], v240 offset:32768
v_mfma_f32_16x16x32_bf16 v[212:215], a[40:43], a[136:139], v[212:215]
s_add_u32 m0, s80, 8192
s_mov_b32 s82, s76
v_mfma_f32_16x16x32_bf16 v[212:215], a[44:47], a[140:143], v[212:215]
v_mfma_f32_16x16x32_bf16 v[116:119], a[48:51], a[136:139], v[116:119]
ds_read_b128 a[4:7], v241 offset:32768
v_mfma_f32_16x16x32_bf16 v[116:119], a[52:55], a[140:143], v[116:119]
ds_read_b128 a[8:11], v240 offset:34816
v_mfma_f32_16x16x32_bf16 a[164:167], a[56:59], a[136:139], a[164:167]
buffer_load_dwordx4 v250, s[52:55], s82 offen lds
v_mfma_f32_16x16x32_bf16 a[164:167], a[60:63], a[140:143], a[164:167]
v_mfma_f32_16x16x32_bf16 v[88:91], a[32:35], a[144:147], v[88:91]
v_mfma_f32_16x16x32_bf16 v[88:91], a[36:39], a[148:151], v[88:91]
s_add_u32 m0, s79, 9216
s_add_u32 s82, s74, 8192
ds_read_b128 a[12:15], v241 offset:34816
v_mfma_f32_16x16x32_bf16 v[216:219], a[40:43], a[144:147], v[216:219]
v_mfma_f32_16x16x32_bf16 v[216:219], a[44:47], a[148:151], v[216:219]
ds_read_b128 a[16:19], v240 offset:36864
v_mfma_f32_16x16x32_bf16 v[120:123], a[48:51], a[144:147], v[120:123]
v_mfma_f32_16x16x32_bf16 v[120:123], a[52:55], a[148:151], v[120:123]
buffer_load_dwordx4 v247, s[40:43], s82 offen lds
v_mfma_f32_16x16x32_bf16 a[168:171], a[56:59], a[144:147], a[168:171]
ds_read_b128 a[20:23], v241 offset:36864
v_mfma_f32_16x16x32_bf16 a[168:171], a[60:63], a[148:151], a[168:171]
v_mfma_f32_16x16x32_bf16 v[92:95], a[32:35], a[152:155], v[92:95]
ds_read_b128 a[24:27], v240 offset:38912
v_mfma_f32_16x16x32_bf16 v[92:95], a[36:39], a[156:159], v[92:95]
s_add_u32 m0, s80, 24576
s_mov_b32 s82, s76
v_mfma_f32_16x16x32_bf16 v[220:223], a[40:43], a[152:155], v[220:223]
v_mfma_f32_16x16x32_bf16 v[220:223], a[44:47], a[156:159], v[220:223]
ds_read_b128 a[28:31], v241 offset:38912
v_mfma_f32_16x16x32_bf16 v[124:127], a[48:51], a[152:155], v[124:127]
buffer_load_dwordx4 v250, s[56:59], s82 offen lds
v_mfma_f32_16x16x32_bf16 v[124:127], a[52:55], a[156:159], v[124:127]
ds_read_b64_tr_b16 a[104:105], v243 offset:32768
v_mfma_f32_16x16x32_bf16 a[172:175], a[56:59], a[152:155], a[172:175]
v_mfma_f32_16x16x32_bf16 a[172:175], a[60:63], a[156:159], a[172:175]
v_mfma_f32_16x16x32_bf16 v[64:67], a[32:35], a[64:67], v[64:67]
s_add_u32 m0, s79, 10240
s_add_u32 s82, s74, 16384
ds_read_b64_tr_b16 a[106:107], v243 offset:33024
v_mfma_f32_16x16x32_bf16 v[64:67], a[36:39], a[68:71], v[64:67]
v_mfma_f32_16x16x32_bf16 v[192:195], a[40:43], a[64:67], v[192:195]
ds_read_b64_tr_b16 a[108:109], v243 offset:36864
v_mfma_f32_16x16x32_bf16 v[192:195], a[44:47], a[68:71], v[192:195]
v_mfma_f32_16x16x32_bf16 v[96:99], a[48:51], a[64:67], v[96:99]
buffer_load_dwordx4 v246, s[40:43], s82 offen lds
v_mfma_f32_16x16x32_bf16 v[96:99], a[52:55], a[68:71], v[96:99]
ds_read_b64_tr_b16 a[110:111], v243 offset:37120
v_mfma_f32_16x16x32_bf16 v[224:227], a[56:59], a[64:67], v[224:227]
v_mfma_f32_16x16x32_bf16 v[224:227], a[60:63], a[68:71], v[224:227]
s_add_u32 m0, s80, 9216
s_add_u32 s82, s76, s77
ds_read_b64_tr_b16 a[112:113], v242 offset:33280
v_mfma_f32_16x16x32_bf16 v[68:71], a[32:35], a[72:75], v[68:71]
v_mfma_f32_16x16x32_bf16 v[68:71], a[36:39], a[76:79], v[68:71]
v_mfma_f32_16x16x32_bf16 v[196:199], a[40:43], a[72:75], v[196:199]
ds_read_b64_tr_b16 a[114:115], v242 offset:33536
v_mfma_f32_16x16x32_bf16 v[196:199], a[44:47], a[76:79], v[196:199]
buffer_load_dwordx4 v251, s[52:55], s82 offen lds
v_mfma_f32_16x16x32_bf16 v[100:103], a[48:51], a[72:75], v[100:103]
ds_read_b64_tr_b16 a[116:117], v242 offset:37376
v_mfma_f32_16x16x32_bf16 v[100:103], a[52:55], a[76:79], v[100:103]
v_mfma_f32_16x16x32_bf16 v[228:231], a[56:59], a[72:75], v[228:231]
v_mfma_f32_16x16x32_bf16 v[228:231], a[60:63], a[76:79], v[228:231]
s_add_u32 m0, s79, 11264
s_add_u32 s82, s74, 24576
ds_read_b64_tr_b16 a[118:119], v242 offset:37632
v_mfma_f32_16x16x32_bf16 v[72:75], a[32:35], a[80:83], v[72:75]
v_mfma_f32_16x16x32_bf16 v[72:75], a[36:39], a[84:87], v[72:75]
ds_read_b64_tr_b16 a[120:121], v243 offset:33280
v_mfma_f32_16x16x32_bf16 v[200:203], a[40:43], a[80:83], v[200:203]
buffer_load_dwordx4 v247, s[40:43], s82 offen lds
v_mfma_f32_16x16x32_bf16 v[200:203], a[44:47], a[84:87], v[200:203]
v_mfma_f32_16x16x32_bf16 v[104:107], a[48:51], a[80:83], v[104:107]
ds_read_b64_tr_b16 a[122:123], v243 offset:33536
v_mfma_f32_16x16x32_bf16 v[104:107], a[52:55], a[84:87], v[104:107]
v_mfma_f32_16x16x32_bf16 v[232:235], a[56:59], a[80:83], v[232:235]
s_add_u32 m0, s80, 25600
s_add_u32 s82, s76, s77
ds_read_b64_tr_b16 a[124:125], v243 offset:37376
v_mfma_f32_16x16x32_bf16 v[232:235], a[60:63], a[84:87], v[232:235]
v_mfma_f32_16x16x32_bf16 v[76:79], a[32:35], a[88:91], v[76:79]
v_mfma_f32_16x16x32_bf16 v[76:79], a[36:39], a[92:95], v[76:79]
ds_read_b64_tr_b16 a[126:127], v243 offset:37632
v_mfma_f32_16x16x32_bf16 v[204:207], a[40:43], a[88:91], v[204:207]
buffer_load_dwordx4 v251, s[56:59], s82 offen lds
s_add_u32 s87, s87, 1
v_mfma_f32_16x16x32_bf16 v[204:207], a[44:47], a[92:95], v[204:207]
s_and_b32 s87, s87, 7
s_lshl_b32 s83, s87, 7
s_add_u32 s74, s95, s83
v_mfma_f32_16x16x32_bf16 v[108:111], a[48:51], a[88:91], v[108:111]
s_mul_i32 s83, s87, s78
s_add_u32 s76, s97, s83
v_mfma_f32_16x16x32_bf16 v[108:111], a[52:55], a[92:95], v[108:111]
v_mfma_f32_16x16x32_bf16 v[236:239], a[56:59], a[88:91], v[236:239]
v_mfma_f32_16x16x32_bf16 v[236:239], a[60:63], a[92:95], v[236:239]
s_waitcnt vmcnt(16) lgkmcnt(0)
s_barrier
v_mfma_f32_16x16x32_bf16 v[0:3], a[0:3], a[96:99], v[0:3]
ds_read_b64_tr_b16 a[128:129], v242 offset:40960
v_mfma_f32_16x16x32_bf16 v[0:3], a[4:7], a[100:103], v[0:3]
v_mfma_f32_16x16x32_bf16 v[128:131], a[8:11], a[96:99], v[128:131]
ds_read_b64_tr_b16 a[130:131], v242 offset:41216
v_mfma_f32_16x16x32_bf16 v[128:131], a[12:15], a[100:103], v[128:131]
s_add_u32 m0, s79, 32768
s_add_u32 s82, s73, 0
v_mfma_f32_16x16x32_bf16 v[32:35], a[16:19], a[96:99], v[32:35]
v_mfma_f32_16x16x32_bf16 v[32:35], a[20:23], a[100:103], v[32:35]
ds_read_b64_tr_b16 a[132:133], v242 offset:45056
v_mfma_f32_16x16x32_bf16 v[160:163], a[24:27], a[96:99], v[160:163]
buffer_load_dwordx4 v246, s[36:39], s82 offen lds
v_mfma_f32_16x16x32_bf16 v[160:163], a[28:31], a[100:103], v[160:163]
ds_read_b64_tr_b16 a[134:135], v242 offset:45312
v_mfma_f32_16x16x32_bf16 v[4:7], a[0:3], a[104:107], v[4:7]
v_mfma_f32_16x16x32_bf16 v[4:7], a[4:7], a[108:111], v[4:7]
v_mfma_f32_16x16x32_bf16 v[132:135], a[8:11], a[104:107], v[132:135]
s_add_u32 m0, s80, 32768
s_add_u32 s82, s75, 0
ds_read_b64_tr_b16 a[136:137], v243 offset:40960
v_mfma_f32_16x16x32_bf16 v[132:135], a[12:15], a[108:111], v[132:135]
v_mfma_f32_16x16x32_bf16 v[36:39], a[16:19], a[104:107], v[36:39]
ds_read_b64_tr_b16 a[138:139], v243 offset:41216
v_mfma_f32_16x16x32_bf16 v[36:39], a[20:23], a[108:111], v[36:39]
v_mfma_f32_16x16x32_bf16 v[164:167], a[24:27], a[104:107], v[164:167]
buffer_load_dwordx4 v248, s[44:47], s82 offen lds
v_mfma_f32_16x16x32_bf16 v[164:167], a[28:31], a[108:111], v[164:167]
ds_read_b64_tr_b16 a[140:141], v243 offset:45056
v_mfma_f32_16x16x32_bf16 v[8:11], a[0:3], a[112:115], v[8:11]
v_mfma_f32_16x16x32_bf16 v[8:11], a[4:7], a[116:119], v[8:11]
s_add_u32 m0, s79, 33792
s_add_u32 s82, s73, 8192
ds_read_b64_tr_b16 a[142:143], v243 offset:45312
v_mfma_f32_16x16x32_bf16 v[136:139], a[8:11], a[112:115], v[136:139]
v_mfma_f32_16x16x32_bf16 v[136:139], a[12:15], a[116:119], v[136:139]
v_mfma_f32_16x16x32_bf16 v[40:43], a[16:19], a[112:115], v[40:43]
ds_read_b128 a[32:35], v240 offset:40960
v_mfma_f32_16x16x32_bf16 v[40:43], a[20:23], a[116:119], v[40:43]
buffer_load_dwordx4 v247, s[36:39], s82 offen lds
v_mfma_f32_16x16x32_bf16 v[168:171], a[24:27], a[112:115], v[168:171]
ds_read_b128 a[36:39], v241 offset:40960
v_mfma_f32_16x16x32_bf16 v[168:171], a[28:31], a[116:119], v[168:171]
v_mfma_f32_16x16x32_bf16 v[12:15], a[0:3], a[120:123], v[12:15]
v_mfma_f32_16x16x32_bf16 v[12:15], a[4:7], a[124:127], v[12:15]
s_add_u32 m0, s80, 49152
s_add_u32 s82, s75, 0
ds_read_b64_tr_b16 a[144:145], v242 offset:41472
v_mfma_f32_16x16x32_bf16 v[140:143], a[8:11], a[120:123], v[140:143]
v_mfma_f32_16x16x32_bf16 v[140:143], a[12:15], a[124:127], v[140:143]
ds_read_b64_tr_b16 a[146:147], v242 offset:41728
v_mfma_f32_16x16x32_bf16 v[44:47], a[16:19], a[120:123], v[44:47]
buffer_load_dwordx4 v248, s[48:51], s82 offen lds
v_mfma_f32_16x16x32_bf16 v[44:47], a[20:23], a[124:127], v[44:47]
v_mfma_f32_16x16x32_bf16 v[172:175], a[24:27], a[120:123], v[172:175]
ds_read_b64_tr_b16 a[148:149], v242 offset:45568
v_mfma_f32_16x16x32_bf16 v[172:175], a[28:31], a[124:127], v[172:175]
s_waitcnt lgkmcnt(11)
v_mfma_f32_16x16x32_bf16 v[16:19], a[0:3], a[128:131], v[16:19]
s_add_u32 m0, s79, 34816
s_add_u32 s82, s73, 16384
ds_read_b64_tr_b16 a[150:151], v242 offset:45824
s_waitcnt lgkmcnt(10)
v_mfma_f32_16x16x32_bf16 v[16:19], a[4:7], a[132:135], v[16:19]
)ASM"
R"ASM(v_mfma_f32_16x16x32_bf16 v[144:147], a[8:11], a[128:131], v[144:147]
v_mfma_f32_16x16x32_bf16 v[144:147], a[12:15], a[132:135], v[144:147]
ds_read_b128 a[40:43], v240 offset:43008
v_mfma_f32_16x16x32_bf16 v[48:51], a[16:19], a[128:131], v[48:51]
buffer_load_dwordx4 v246, s[36:39], s82 offen lds
v_mfma_f32_16x16x32_bf16 v[48:51], a[20:23], a[132:135], v[48:51]
ds_read_b128 a[44:47], v241 offset:43008
v_mfma_f32_16x16x32_bf16 v[176:179], a[24:27], a[128:131], v[176:179]
v_mfma_f32_16x16x32_bf16 v[176:179], a[28:31], a[132:135], v[176:179]
s_add_u32 m0, s80, 33792
s_add_u32 s82, s75, 65536
s_waitcnt lgkmcnt(10)
v_mfma_f32_16x16x32_bf16 v[20:23], a[0:3], a[136:139], v[20:23]
ds_read_b64_tr_b16 a[152:153], v243 offset:41472
s_waitcnt lgkmcnt(9)
v_mfma_f32_16x16x32_bf16 v[20:23], a[4:7], a[140:143], v[20:23]
v_mfma_f32_16x16x32_bf16 v[148:151], a[8:11], a[136:139], v[148:151]
ds_read_b64_tr_b16 a[154:155], v243 offset:41728
v_mfma_f32_16x16x32_bf16 v[148:151], a[12:15], a[140:143], v[148:151]
buffer_load_dwordx4 v249, s[44:47], s82 offen lds
v_mfma_f32_16x16x32_bf16 v[52:55], a[16:19], a[136:139], v[52:55]
v_mfma_f32_16x16x32_bf16 v[52:55], a[20:23], a[140:143], v[52:55]
ds_read_b64_tr_b16 a[156:157], v243 offset:45568
v_mfma_f32_16x16x32_bf16 v[180:183], a[24:27], a[136:139], v[180:183]
v_mfma_f32_16x16x32_bf16 v[180:183], a[28:31], a[140:143], v[180:183]
s_add_u32 m0, s79, 35840
s_add_u32 s82, s73, 24576
ds_read_b64_tr_b16 a[158:159], v243 offset:45824
s_waitcnt lgkmcnt(8)
v_mfma_f32_16x16x32_bf16 v[24:27], a[0:3], a[144:147], v[24:27]
s_waitcnt lgkmcnt(6)
v_mfma_f32_16x16x32_bf16 v[24:27], a[4:7], a[148:151], v[24:27]
ds_read_b128 a[48:51], v240 offset:45056
v_mfma_f32_16x16x32_bf16 v[152:155], a[8:11], a[144:147], v[152:155]
buffer_load_dwordx4 v247, s[36:39], s82 offen lds
v_mfma_f32_16x16x32_bf16 v[152:155], a[12:15], a[148:151], v[152:155]
v_mfma_f32_16x16x32_bf16 v[56:59], a[16:19], a[144:147], v[56:59]
ds_read_b128 a[52:55], v241 offset:45056
v_mfma_f32_16x16x32_bf16 v[56:59], a[20:23], a[148:151], v[56:59]
v_mfma_f32_16x16x32_bf16 v[184:187], a[24:27], a[144:147], v[184:187]
s_add_u32 m0, s80, 50176
s_add_u32 s82, s75, 65536
v_mfma_f32_16x16x32_bf16 v[184:187], a[28:31], a[148:151], v[184:187]
ds_read_b128 a[56:59], v240 offset:47104
s_waitcnt lgkmcnt(5)
v_mfma_f32_16x16x32_bf16 v[28:31], a[0:3], a[152:155], v[28:31]
s_waitcnt lgkmcnt(3)
v_mfma_f32_16x16x32_bf16 v[28:31], a[4:7], a[156:159], v[28:31]
ds_read_b128 a[60:63], v241 offset:47104
v_mfma_f32_16x16x32_bf16 v[156:159], a[8:11], a[152:155], v[156:159]
buffer_load_dwordx4 v249, s[48:51], s82 offen lds
s_add_u32 s86, s86, 1
v_mfma_f32_16x16x32_bf16 v[156:159], a[12:15], a[156:159], v[156:159]
s_and_b32 s86, s86, 7
s_lshl_b32 s83, s86, 7
s_add_u32 s73, s94, s83
v_mfma_f32_16x16x32_bf16 v[60:63], a[16:19], a[152:155], v[60:63]
s_lshl_b32 s83, s86, 19
s_add_u32 s75, s96, s83
v_mfma_f32_16x16x32_bf16 v[60:63], a[20:23], a[156:159], v[60:63]
v_mfma_f32_16x16x32_bf16 v[188:191], a[24:27], a[152:155], v[188:191]
v_mfma_f32_16x16x32_bf16 v[188:191], a[28:31], a[156:159], v[188:191]
s_waitcnt vmcnt(16) lgkmcnt(0)
s_barrier
v_mfma_f32_16x16x32_bf16 v[80:83], a[32:35], a[128:131], v[80:83]
ds_read_b64_tr_b16 a[64:65], v242 offset:0
v_mfma_f32_16x16x32_bf16 v[80:83], a[36:39], a[132:135], v[80:83]
v_mfma_f32_16x16x32_bf16 v[208:211], a[40:43], a[128:131], v[208:211]
ds_read_b64_tr_b16 a[66:67], v242 offset:256
v_mfma_f32_16x16x32_bf16 v[208:211], a[44:47], a[132:135], v[208:211]
s_add_u32 m0, s79, 40960
s_add_u32 s82, s74, 0
v_mfma_f32_16x16x32_bf16 v[112:115], a[48:51], a[128:131], v[112:115]
ds_read_b64_tr_b16 a[68:69], v242 offset:4096
v_mfma_f32_16x16x32_bf16 v[112:115], a[52:55], a[132:135], v[112:115]
v_mfma_f32_16x16x32_bf16 a[160:163], a[56:59], a[128:131], a[160:163]
buffer_load_dwordx4 v246, s[40:43], s82 offen lds
v_mfma_f32_16x16x32_bf16 a[160:163], a[60:63], a[132:135], a[160:163]
ds_read_b64_tr_b16 a[70:71], v242 offset:4352
v_mfma_f32_16x16x32_bf16 v[84:87], a[32:35], a[136:139], v[84:87]
v_mfma_f32_16x16x32_bf16 v[84:87], a[36:39], a[140:143], v[84:87]
ds_read_b128 a[0:3], v240 offset:0
v_mfma_f32_16x16x32_bf16 v[212:215], a[40:43], a[136:139], v[212:215]
s_add_u32 m0, s80, 40960
s_mov_b32 s82, s76
v_mfma_f32_16x16x32_bf16 v[212:215], a[44:47], a[140:143], v[212:215]
v_mfma_f32_16x16x32_bf16 v[116:119], a[48:51], a[136:139], v[116:119]
ds_read_b128 a[4:7], v241 offset:0
v_mfma_f32_16x16x32_bf16 v[116:119], a[52:55], a[140:143], v[116:119]
ds_read_b128 a[8:11], v240 offset:2048
v_mfma_f32_16x16x32_bf16 a[164:167], a[56:59], a[136:139], a[164:167]
buffer_load_dwordx4 v250, s[52:55], s82 offen lds
v_mfma_f32_16x16x32_bf16 a[164:167], a[60:63], a[140:143], a[164:167]
v_mfma_f32_16x16x32_bf16 v[88:91], a[32:35], a[144:147], v[88:91]
v_mfma_f32_16x16x32_bf16 v[88:91], a[36:39], a[148:151], v[88:91]
s_add_u32 m0, s79, 41984
s_add_u32 s82, s74, 8192
ds_read_b128 a[12:15], v241 offset:2048
v_mfma_f32_16x16x32_bf16 v[216:219], a[40:43], a[144:147], v[216:219]
v_mfma_f32_16x16x32_bf16 v[216:219], a[44:47], a[148:151], v[216:219]
ds_read_b128 a[16:19], v240 offset:4096
v_mfma_f32_16x16x32_bf16 v[120:123], a[48:51], a[144:147], v[120:123]
v_mfma_f32_16x16x32_bf16 v[120:123], a[52:55], a[148:151], v[120:123]
buffer_load_dwordx4 v247, s[40:43], s82 offen lds
v_mfma_f32_16x16x32_bf16 a[168:171], a[56:59], a[144:147], a[168:171]
ds_read_b128 a[20:23], v241 offset:4096
v_mfma_f32_16x16x32_bf16 a[168:171], a[60:63], a[148:151], a[168:171]
v_mfma_f32_16x16x32_bf16 v[92:95], a[32:35], a[152:155], v[92:95]
ds_read_b128 a[24:27], v240 offset:6144
v_mfma_f32_16x16x32_bf16 v[92:95], a[36:39], a[156:159], v[92:95]
s_add_u32 m0, s80, 57344
s_mov_b32 s82, s76
v_mfma_f32_16x16x32_bf16 v[220:223], a[40:43], a[152:155], v[220:223]
v_mfma_f32_16x16x32_bf16 v[220:223], a[44:47], a[156:159], v[220:223]
ds_read_b128 a[28:31], v241 offset:6144
v_mfma_f32_16x16x32_bf16 v[124:127], a[48:51], a[152:155], v[124:127]
buffer_load_dwordx4 v250, s[56:59], s82 offen lds
v_mfma_f32_16x16x32_bf16 v[124:127], a[52:55], a[156:159], v[124:127]
ds_read_b64_tr_b16 a[72:73], v243 offset:0
v_mfma_f32_16x16x32_bf16 a[172:175], a[56:59], a[152:155], a[172:175]
v_mfma_f32_16x16x32_bf16 a[172:175], a[60:63], a[156:159], a[172:175]
v_mfma_f32_16x16x32_bf16 v[64:67], a[32:35], a[96:99], v[64:67]
s_add_u32 m0, s79, 43008
s_add_u32 s82, s74, 16384
ds_read_b64_tr_b16 a[74:75], v243 offset:256
v_mfma_f32_16x16x32_bf16 v[64:67], a[36:39], a[100:103], v[64:67]
v_mfma_f32_16x16x32_bf16 v[192:195], a[40:43], a[96:99], v[192:195]
ds_read_b64_tr_b16 a[76:77], v243 offset:4096
v_mfma_f32_16x16x32_bf16 v[192:195], a[44:47], a[100:103], v[192:195]
v_mfma_f32_16x16x32_bf16 v[96:99], a[48:51], a[96:99], v[96:99]
buffer_load_dwordx4 v246, s[40:43], s82 offen lds
v_mfma_f32_16x16x32_bf16 v[96:99], a[52:55], a[100:103], v[96:99]
ds_read_b64_tr_b16 a[78:79], v243 offset:4352
v_mfma_f32_16x16x32_bf16 v[224:227], a[56:59], a[96:99], v[224:227]
v_mfma_f32_16x16x32_bf16 v[224:227], a[60:63], a[100:103], v[224:227]
s_add_u32 m0, s80, 41984
s_add_u32 s82, s76, s77
ds_read_b64_tr_b16 a[80:81], v242 offset:512
v_mfma_f32_16x16x32_bf16 v[68:71], a[32:35], a[104:107], v[68:71]
v_mfma_f32_16x16x32_bf16 v[68:71], a[36:39], a[108:111], v[68:71]
v_mfma_f32_16x16x32_bf16 v[196:199], a[40:43], a[104:107], v[196:199]
ds_read_b64_tr_b16 a[82:83], v242 offset:768
v_mfma_f32_16x16x32_bf16 v[196:199], a[44:47], a[108:111], v[196:199]
buffer_load_dwordx4 v251, s[52:55], s82 offen lds
v_mfma_f32_16x16x32_bf16 v[100:103], a[48:51], a[104:107], v[100:103]
ds_read_b64_tr_b16 a[84:85], v242 offset:4608
v_mfma_f32_16x16x32_bf16 v[100:103], a[52:55], a[108:111], v[100:103]
v_mfma_f32_16x16x32_bf16 v[228:231], a[56:59], a[104:107], v[228:231]
v_mfma_f32_16x16x32_bf16 v[228:231], a[60:63], a[108:111], v[228:231]
s_add_u32 m0, s79, 44032
s_add_u32 s82, s74, 24576
ds_read_b64_tr_b16 a[86:87], v242 offset:4864
v_mfma_f32_16x16x32_bf16 v[72:75], a[32:35], a[112:115], v[72:75]
v_mfma_f32_16x16x32_bf16 v[72:75], a[36:39], a[116:119], v[72:75]
ds_read_b64_tr_b16 a[88:89], v243 offset:512
v_mfma_f32_16x16x32_bf16 v[200:203], a[40:43], a[112:115], v[200:203]
buffer_load_dwordx4 v247, s[40:43], s82 offen lds
v_mfma_f32_16x16x32_bf16 v[200:203], a[44:47], a[116:119], v[200:203]
v_mfma_f32_16x16x32_bf16 v[104:107], a[48:51], a[112:115], v[104:107]
ds_read_b64_tr_b16 a[90:91], v243 offset:768
v_mfma_f32_16x16x32_bf16 v[104:107], a[52:55], a[116:119], v[104:107]
v_mfma_f32_16x16x32_bf16 v[232:235], a[56:59], a[112:115], v[232:235]
s_add_u32 m0, s80, 58368
s_add_u32 s82, s76, s77
ds_read_b64_tr_b16 a[92:93], v243 offset:4608
v_mfma_f32_16x16x32_bf16 v[232:235], a[60:63], a[116:119], v[232:235]
v_mfma_f32_16x16x32_bf16 v[76:79], a[32:35], a[120:123], v[76:79]
v_mfma_f32_16x16x32_bf16 v[76:79], a[36:39], a[124:127], v[76:79]
ds_read_b64_tr_b16 a[94:95], v243 offset:4864
v_mfma_f32_16x16x32_bf16 v[204:207], a[40:43], a[120:123], v[204:207]
buffer_load_dwordx4 v251, s[56:59], s82 offen lds
s_add_u32 s87, s87, 1
v_mfma_f32_16x16x32_bf16 v[204:207], a[44:47], a[124:127], v[204:207]
s_and_b32 s87, s87, 7
s_lshl_b32 s83, s87, 7
s_add_u32 s74, s95, s83
v_mfma_f32_16x16x32_bf16 v[108:111], a[48:51], a[120:123], v[108:111]
s_mul_i32 s83, s87, s78
s_add_u32 s76, s97, s83
v_mfma_f32_16x16x32_bf16 v[108:111], a[52:55], a[124:127], v[108:111]
v_mfma_f32_16x16x32_bf16 v[236:239], a[56:59], a[120:123], v[236:239]
v_mfma_f32_16x16x32_bf16 v[236:239], a[60:63], a[124:127], v[236:239]
s_sub_u32 s81, s81, 1
s_cmp_lg_u32 s81, 0
s_cbranch_scc1 mla_fused_a_loopV
mla_fused_a_park:
v_cvt_pk_bf16_f32 v0, v0, v1
v_cvt_pk_bf16_f32 v1, v2, v3
v_cvt_pk_bf16_f32 v2, v128, v129
v_cvt_pk_bf16_f32 v3, v130, v131
v_cvt_pk_bf16_f32 v4, v4, v5
v_cvt_pk_bf16_f32 v5, v6, v7
v_cvt_pk_bf16_f32 v6, v132, v133
v_cvt_pk_bf16_f32 v7, v134, v135
v_cvt_pk_bf16_f32 v8, v8, v9
v_cvt_pk_bf16_f32 v9, v10, v11
v_cvt_pk_bf16_f32 v10, v136, v137
v_cvt_pk_bf16_f32 v11, v138, v139
v_cvt_pk_bf16_f32 v12, v12, v13
v_cvt_pk_bf16_f32 v13, v14, v15
v_cvt_pk_bf16_f32 v14, v140, v141
v_cvt_pk_bf16_f32 v15, v142, v143
v_cvt_pk_bf16_f32 v16, v16, v17
v_cvt_pk_bf16_f32 v17, v18, v19
v_cvt_pk_bf16_f32 v18, v144, v145
v_cvt_pk_bf16_f32 v19, v146, v147
v_cvt_pk_bf16_f32 v20, v20, v21
v_cvt_pk_bf16_f32 v21, v22, v23
v_cvt_pk_bf16_f32 v22, v148, v149
v_cvt_pk_bf16_f32 v23, v150, v151
v_cvt_pk_bf16_f32 v24, v24, v25
v_cvt_pk_bf16_f32 v25, v26, v27
v_cvt_pk_bf16_f32 v26, v152, v153
v_cvt_pk_bf16_f32 v27, v154, v155
v_cvt_pk_bf16_f32 v28, v28, v29
v_cvt_pk_bf16_f32 v29, v30, v31
v_cvt_pk_bf16_f32 v30, v156, v157
v_cvt_pk_bf16_f32 v31, v158, v159
v_cvt_pk_bf16_f32 v32, v32, v33
v_cvt_pk_bf16_f32 v33, v34, v35
v_cvt_pk_bf16_f32 v34, v160, v161
v_cvt_pk_bf16_f32 v35, v162, v163
v_cvt_pk_bf16_f32 v36, v36, v37
v_cvt_pk_bf16_f32 v37, v38, v39
v_cvt_pk_bf16_f32 v38, v164, v165
v_cvt_pk_bf16_f32 v39, v166, v167
v_cvt_pk_bf16_f32 v40, v40, v41
v_cvt_pk_bf16_f32 v41, v42, v43
v_cvt_pk_bf16_f32 v42, v168, v169
v_cvt_pk_bf16_f32 v43, v170, v171
v_cvt_pk_bf16_f32 v44, v44, v45
v_cvt_pk_bf16_f32 v45, v46, v47
v_cvt_pk_bf16_f32 v46, v172, v173
v_cvt_pk_bf16_f32 v47, v174, v175
v_cvt_pk_bf16_f32 v48, v48, v49
v_cvt_pk_bf16_f32 v49, v50, v51
v_cvt_pk_bf16_f32 v50, v176, v177
v_cvt_pk_bf16_f32 v51, v178, v179
v_cvt_pk_bf16_f32 v52, v52, v53
v_cvt_pk_bf16_f32 v53, v54, v55
v_cvt_pk_bf16_f32 v54, v180, v181
v_cvt_pk_bf16_f32 v55, v182, v183
v_cvt_pk_bf16_f32 v56, v56, v57
v_cvt_pk_bf16_f32 v57, v58, v59
v_cvt_pk_bf16_f32 v58, v184, v185
v_cvt_pk_bf16_f32 v59, v186, v187
v_cvt_pk_bf16_f32 v60, v60, v61
v_cvt_pk_bf16_f32 v61, v62, v63
v_cvt_pk_bf16_f32 v62, v188, v189
v_cvt_pk_bf16_f32 v63, v190, v191
v_cvt_pk_bf16_f32 v64, v64, v65
v_cvt_pk_bf16_f32 v65, v66, v67
v_cvt_pk_bf16_f32 v66, v192, v193
v_cvt_pk_bf16_f32 v67, v194, v195
v_cvt_pk_bf16_f32 v68, v68, v69
v_cvt_pk_bf16_f32 v69, v70, v71
v_cvt_pk_bf16_f32 v70, v196, v197
v_cvt_pk_bf16_f32 v71, v198, v199
v_cvt_pk_bf16_f32 v72, v72, v73
v_cvt_pk_bf16_f32 v73, v74, v75
v_cvt_pk_bf16_f32 v74, v200, v201
)ASM"
R"ASM(v_cvt_pk_bf16_f32 v75, v202, v203
v_cvt_pk_bf16_f32 v76, v76, v77
v_cvt_pk_bf16_f32 v77, v78, v79
v_cvt_pk_bf16_f32 v78, v204, v205
v_cvt_pk_bf16_f32 v79, v206, v207
v_cvt_pk_bf16_f32 v80, v80, v81
v_cvt_pk_bf16_f32 v81, v82, v83
v_cvt_pk_bf16_f32 v82, v208, v209
v_cvt_pk_bf16_f32 v83, v210, v211
v_cvt_pk_bf16_f32 v84, v84, v85
v_cvt_pk_bf16_f32 v85, v86, v87
v_cvt_pk_bf16_f32 v86, v212, v213
v_cvt_pk_bf16_f32 v87, v214, v215
v_cvt_pk_bf16_f32 v88, v88, v89
v_cvt_pk_bf16_f32 v89, v90, v91
v_cvt_pk_bf16_f32 v90, v216, v217
v_cvt_pk_bf16_f32 v91, v218, v219
v_cvt_pk_bf16_f32 v92, v92, v93
v_cvt_pk_bf16_f32 v93, v94, v95
v_cvt_pk_bf16_f32 v94, v220, v221
v_cvt_pk_bf16_f32 v95, v222, v223
v_cvt_pk_bf16_f32 v96, v96, v97
v_cvt_pk_bf16_f32 v97, v98, v99
v_cvt_pk_bf16_f32 v98, v224, v225
v_cvt_pk_bf16_f32 v99, v226, v227
v_cvt_pk_bf16_f32 v100, v100, v101
v_cvt_pk_bf16_f32 v101, v102, v103
v_cvt_pk_bf16_f32 v102, v228, v229
v_cvt_pk_bf16_f32 v103, v230, v231
v_cvt_pk_bf16_f32 v104, v104, v105
v_cvt_pk_bf16_f32 v105, v106, v107
v_cvt_pk_bf16_f32 v106, v232, v233
v_cvt_pk_bf16_f32 v107, v234, v235
v_cvt_pk_bf16_f32 v108, v108, v109
v_cvt_pk_bf16_f32 v109, v110, v111
v_cvt_pk_bf16_f32 v110, v236, v237
v_cvt_pk_bf16_f32 v111, v238, v239
v_accvgpr_read_b32 v128, a160
v_accvgpr_read_b32 v129, a161
v_accvgpr_read_b32 v130, a162
v_accvgpr_read_b32 v131, a163
v_accvgpr_read_b32 v132, a164
v_accvgpr_read_b32 v133, a165
v_accvgpr_read_b32 v134, a166
v_accvgpr_read_b32 v135, a167
v_accvgpr_read_b32 v136, a168
v_accvgpr_read_b32 v137, a169
v_accvgpr_read_b32 v138, a170
v_accvgpr_read_b32 v139, a171
v_accvgpr_read_b32 v140, a172
v_accvgpr_read_b32 v141, a173
v_accvgpr_read_b32 v142, a174
v_accvgpr_read_b32 v143, a175
v_cvt_pk_bf16_f32 v112, v112, v113
v_cvt_pk_bf16_f32 v113, v114, v115
v_cvt_pk_bf16_f32 v114, v128, v129
v_cvt_pk_bf16_f32 v115, v130, v131
v_cvt_pk_bf16_f32 v116, v116, v117
v_cvt_pk_bf16_f32 v117, v118, v119
v_cvt_pk_bf16_f32 v118, v132, v133
v_cvt_pk_bf16_f32 v119, v134, v135
v_cvt_pk_bf16_f32 v120, v120, v121
v_cvt_pk_bf16_f32 v121, v122, v123
v_cvt_pk_bf16_f32 v122, v136, v137
v_cvt_pk_bf16_f32 v123, v138, v139
v_cvt_pk_bf16_f32 v124, v124, v125
v_cvt_pk_bf16_f32 v125, v126, v127
v_cvt_pk_bf16_f32 v126, v140, v141
v_cvt_pk_bf16_f32 v127, v142, v143
v_accvgpr_read_b32 v192, a240
v_accvgpr_read_b32 v193, a241
v_accvgpr_read_b32 v194, a242
v_accvgpr_read_b32 v195, a243
v_lshlrev_b32 v196, 16, v192
v_and_b32 v197, 0xffff0000, v192
v_lshlrev_b32 v198, 16, v193
v_and_b32 v199, 0xffff0000, v193
v_lshlrev_b32 v200, 16, v194
v_and_b32 v201, 0xffff0000, v194
v_lshlrev_b32 v202, 16, v195
v_and_b32 v203, 0xffff0000, v195
v_mov_b64 v[128:129], v[196:197]
v_mov_b64 v[130:131], v[198:199]
v_mov_b64 v[132:133], v[196:197]
v_mov_b64 v[134:135], v[198:199]
v_mov_b64 v[136:137], v[196:197]
v_mov_b64 v[138:139], v[198:199]
v_mov_b64 v[140:141], v[196:197]
v_mov_b64 v[142:143], v[198:199]
v_mov_b64 v[144:145], v[196:197]
v_mov_b64 v[146:147], v[198:199]
v_mov_b64 v[148:149], v[196:197]
v_mov_b64 v[150:151], v[198:199]
v_mov_b64 v[152:153], v[196:197]
v_mov_b64 v[154:155], v[198:199]
v_mov_b64 v[156:157], v[196:197]
v_mov_b64 v[158:159], v[198:199]
v_mov_b64 v[160:161], v[200:201]
v_mov_b64 v[162:163], v[202:203]
v_mov_b64 v[164:165], v[200:201]
v_mov_b64 v[166:167], v[202:203]
v_mov_b64 v[168:169], v[200:201]
v_mov_b64 v[170:171], v[202:203]
v_mov_b64 v[172:173], v[200:201]
v_mov_b64 v[174:175], v[202:203]
v_mov_b64 v[176:177], v[200:201]
v_mov_b64 v[178:179], v[202:203]
v_mov_b64 v[180:181], v[200:201]
v_mov_b64 v[182:183], v[202:203]
v_mov_b64 v[184:185], v[200:201]
v_mov_b64 v[186:187], v[202:203]
v_mov_b64 v[188:189], v[200:201]
v_mov_b64 v[190:191], v[202:203]
s_mov_b32 s93, s90
s_waitcnt vmcnt(16) lgkmcnt(0)
s_barrier
v_mfma_f32_16x16x32_bf16 v[192:195], a[64:67], a[0:3], 0
ds_read_b64_tr_b16 a[128:129], v244 offset:0
v_mfma_f32_16x16x32_bf16 v[192:195], a[68:71], a[4:7], v[192:195]
v_mfma_f32_16x16x32_bf16 v[200:203], a[64:67], a[8:11], 0
ds_read_b64_tr_b16 a[130:131], v244 offset:256
v_mfma_f32_16x16x32_bf16 v[200:203], a[68:71], a[12:15], v[200:203]
s_add_u32 m0, s79, 0
s_add_u32 s82, s73, 0
v_mfma_f32_16x16x32_bf16 v[208:211], a[64:67], a[16:19], 0
ds_read_b64_tr_b16 a[132:133], v244 offset:4096
v_mfma_f32_16x16x32_bf16 v[208:211], a[68:71], a[20:23], v[208:211]
buffer_load_dwordx4 v246, s[36:39], s82 offen lds
s_mov_b32 s93, s90
v_mfma_f32_16x16x32_bf16 v[216:219], a[64:67], a[24:27], 0
v_mfma_f32_16x16x32_bf16 v[216:219], a[68:71], a[28:31], v[216:219]
v_mfma_f32_16x16x32_bf16 v[196:199], a[72:75], a[0:3], 0
s_add_u32 m0, s80, 0
s_add_u32 s82, s75, 0
ds_read_b64_tr_b16 a[134:135], v244 offset:4352
v_mfma_f32_16x16x32_bf16 v[196:199], a[76:79], a[4:7], v[196:199]
v_mfma_f32_16x16x32_bf16 v[204:207], a[72:75], a[8:11], 0
buffer_load_dwordx4 v248, s[44:47], s82 offen lds
v_mfma_f32_16x16x32_bf16 v[204:207], a[76:79], a[12:15], v[204:207]
ds_read_b64_tr_b16 a[136:137], v245 offset:0
v_mfma_f32_16x16x32_bf16 v[212:215], a[72:75], a[16:19], 0
v_mfma_f32_16x16x32_bf16 v[212:215], a[76:79], a[20:23], v[212:215]
s_add_u32 m0, s79, 1024
s_add_u32 s82, s73, 8192
v_mfma_f32_16x16x32_bf16 v[220:223], a[72:75], a[24:27], 0
ds_read_b64_tr_b16 a[138:139], v245 offset:256
v_mfma_f32_16x16x32_bf16 v[220:223], a[76:79], a[28:31], v[220:223]
ds_read_b64_tr_b16 a[140:141], v245 offset:4096
v_mfma_f32_16x16x32_bf16 a[176:179], a[80:83], a[0:3], 0
buffer_load_dwordx4 v247, s[36:39], s82 offen lds
v_mfma_f32_16x16x32_bf16 a[176:179], a[84:87], a[4:7], a[176:179]
buffer_store_dwordx4 v[0:3], v252, s[60:63], s93 offen sc1
v_mfma_f32_16x16x32_bf16 a[184:187], a[80:83], a[8:11], 0
s_add_u32 m0, s80, 16384
s_add_u32 s82, s75, 0
v_mfma_f32_16x16x32_bf16 a[184:187], a[84:87], a[12:15], a[184:187]
ds_read_b64_tr_b16 a[142:143], v245 offset:4352
v_mfma_f32_16x16x32_bf16 a[192:195], a[80:83], a[16:19], 0
v_mfma_f32_16x16x32_bf16 a[192:195], a[84:87], a[20:23], a[192:195]
buffer_load_dwordx4 v248, s[48:51], s82 offen lds
v_mfma_f32_16x16x32_bf16 a[200:203], a[80:83], a[24:27], 0
ds_read_b128 a[32:35], v240 offset:8192
v_mfma_f32_16x16x32_bf16 a[200:203], a[84:87], a[28:31], a[200:203]
s_add_u32 m0, s79, 2048
s_add_u32 s82, s73, 16384
v_mfma_f32_16x16x32_bf16 a[180:183], a[88:91], a[0:3], 0
v_mfma_f32_16x16x32_bf16 a[180:183], a[92:95], a[4:7], a[180:183]
ds_read_b128 a[36:39], v241 offset:8192
v_mfma_f32_16x16x32_bf16 a[188:191], a[88:91], a[8:11], 0
buffer_load_dwordx4 v246, s[36:39], s82 offen lds
v_mfma_f32_16x16x32_bf16 a[188:191], a[92:95], a[12:15], a[188:191]
v_mfma_f32_16x16x32_bf16 a[196:199], a[88:91], a[16:19], 0
ds_read_b128 a[40:43], v240 offset:10240
v_mfma_f32_16x16x32_bf16 a[196:199], a[92:95], a[20:23], a[196:199]
s_add_u32 m0, s80, 1024
s_add_u32 s82, s75, 65536
s_add_u32 s93, s93, s91
v_mfma_f32_16x16x32_bf16 a[204:207], a[88:91], a[24:27], 0
ds_read_b128 a[44:47], v241 offset:10240
v_mfma_f32_16x16x32_bf16 a[204:207], a[92:95], a[28:31], a[204:207]
buffer_load_dwordx4 v249, s[44:47], s82 offen lds
s_waitcnt lgkmcnt(10)
v_mfma_f32_16x16x32_bf16 v[128:131], a[128:131], a[0:3], v[128:131]
s_waitcnt lgkmcnt(8)
v_mfma_f32_16x16x32_bf16 v[128:131], a[132:135], a[4:7], v[128:131]
ds_read_b128 a[48:51], v240 offset:12288
v_mfma_f32_16x16x32_bf16 v[132:135], a[128:131], a[8:11], v[132:135]
s_add_u32 m0, s79, 3072
s_add_u32 s82, s73, 24576
v_mfma_f32_16x16x32_bf16 v[132:135], a[132:135], a[12:15], v[132:135]
ds_read_b128 a[52:55], v241 offset:12288
v_mfma_f32_16x16x32_bf16 v[136:139], a[128:131], a[16:19], v[136:139]
buffer_load_dwordx4 v247, s[36:39], s82 offen lds
v_mfma_f32_16x16x32_bf16 v[136:139], a[132:135], a[20:23], v[136:139]
v_mfma_f32_16x16x32_bf16 v[140:143], a[128:131], a[24:27], v[140:143]
v_mfma_f32_16x16x32_bf16 v[140:143], a[132:135], a[28:31], v[140:143]
s_add_u32 m0, s80, 17408
s_add_u32 s82, s75, 65536
ds_read_b128 a[56:59], v240 offset:14336
s_waitcnt lgkmcnt(9)
v_mfma_f32_16x16x32_bf16 v[160:163], a[136:139], a[0:3], v[160:163]
s_waitcnt lgkmcnt(7)
v_mfma_f32_16x16x32_bf16 v[160:163], a[140:143], a[4:7], v[160:163]
buffer_store_dwordx4 v[4:7], v252, s[60:63], s93 offen sc1
ds_read_b128 a[60:63], v241 offset:14336
v_mfma_f32_16x16x32_bf16 v[164:167], a[136:139], a[8:11], v[164:167]
buffer_load_dwordx4 v249, s[48:51], s82 offen lds
s_add_u32 s86, s86, 1
v_mfma_f32_16x16x32_bf16 v[164:167], a[140:143], a[12:15], v[164:167]
s_and_b32 s86, s86, 7
s_lshl_b32 s83, s86, 7
s_add_u32 s73, s94, s83
v_mfma_f32_16x16x32_bf16 v[168:171], a[136:139], a[16:19], v[168:171]
s_lshl_b32 s83, s86, 19
s_add_u32 s75, s96, s83
v_mfma_f32_16x16x32_bf16 v[168:171], a[140:143], a[20:23], v[168:171]
v_mfma_f32_16x16x32_bf16 v[172:175], a[136:139], a[24:27], v[172:175]
v_mfma_f32_16x16x32_bf16 v[172:175], a[140:143], a[28:31], v[172:175]
s_waitcnt vmcnt(18) lgkmcnt(0)
s_barrier
v_mfma_f32_16x16x32_bf16 v[144:147], a[128:131], a[32:35], v[144:147]
ds_read_b64_tr_b16 a[96:97], v242 offset:32768
v_mfma_f32_16x16x32_bf16 v[144:147], a[132:135], a[36:39], v[144:147]
ds_read_b64_tr_b16 a[98:99], v242 offset:33024
v_mfma_f32_16x16x32_bf16 v[148:151], a[128:131], a[40:43], v[148:151]
v_mfma_f32_16x16x32_bf16 v[148:151], a[132:135], a[44:47], v[148:151]
s_add_u32 m0, s79, 8192
s_add_u32 s82, s74, 0
ds_read_b64_tr_b16 a[100:101], v242 offset:36864
v_mfma_f32_16x16x32_bf16 v[152:155], a[128:131], a[48:51], v[152:155]
v_mfma_f32_16x16x32_bf16 v[152:155], a[132:135], a[52:55], v[152:155]
ds_read_b64_tr_b16 a[102:103], v242 offset:37120
v_mfma_f32_16x16x32_bf16 v[156:159], a[128:131], a[56:59], v[156:159]
buffer_load_dwordx4 v246, s[40:43], s82 offen lds
s_add_u32 s93, s93, s91
v_mfma_f32_16x16x32_bf16 v[156:159], a[132:135], a[60:63], v[156:159]
ds_read_b128 a[0:3], v240 offset:32768
v_mfma_f32_16x16x32_bf16 v[176:179], a[136:139], a[32:35], v[176:179]
ds_read_b128 a[4:7], v241 offset:32768
v_mfma_f32_16x16x32_bf16 v[176:179], a[140:143], a[36:39], v[176:179]
v_mfma_f32_16x16x32_bf16 v[180:183], a[136:139], a[40:43], v[180:183]
s_add_u32 m0, s80, 8192
s_mov_b32 s82, s76
ds_read_b128 a[8:11], v240 offset:34816
v_mfma_f32_16x16x32_bf16 v[180:183], a[140:143], a[44:47], v[180:183]
v_mfma_f32_16x16x32_bf16 v[184:187], a[136:139], a[48:51], v[184:187]
ds_read_b128 a[12:15], v241 offset:34816
v_mfma_f32_16x16x32_bf16 v[184:187], a[140:143], a[52:55], v[184:187]
buffer_load_dwordx4 v250, s[52:55], s82 offen lds
v_mfma_f32_16x16x32_bf16 v[188:191], a[136:139], a[56:59], v[188:191]
ds_read_b128 a[16:19], v240 offset:36864
v_mfma_f32_16x16x32_bf16 v[188:191], a[140:143], a[60:63], v[188:191]
v_mfma_f32_16x16x32_bf16 v[224:227], a[64:67], a[32:35], 0
ds_read_b128 a[20:23], v241 offset:36864
v_mfma_f32_16x16x32_bf16 v[224:227], a[68:71], a[36:39], v[224:227]
s_add_u32 m0, s79, 9216
s_add_u32 s82, s74, 8192
ds_read_b128 a[24:27], v240 offset:38912
v_mfma_f32_16x16x32_bf16 v[232:235], a[64:67], a[40:43], 0
ds_read_b128 a[28:31], v241 offset:38912
v_mfma_f32_16x16x32_bf16 v[232:235], a[68:71], a[44:47], v[232:235]
buffer_store_dwordx4 v[8:11], v252, s[60:63], s93 offen sc1
v_mfma_f32_16x16x32_bf16 a[160:163], a[64:67], a[48:51], 0
buffer_load_dwordx4 v247, s[40:43], s82 offen lds
v_mfma_f32_16x16x32_bf16 a[160:163], a[68:71], a[52:55], a[160:163]
ds_read_b64_tr_b16 a[104:105], v243 offset:32768
v_mfma_f32_16x16x32_bf16 a[168:171], a[64:67], a[56:59], 0
v_mfma_f32_16x16x32_bf16 a[168:171], a[68:71], a[60:63], a[168:171]
ds_read_b64_tr_b16 a[106:107], v243 offset:33024
v_mfma_f32_16x16x32_bf16 v[228:231], a[72:75], a[32:35], 0
s_add_u32 m0, s79, 10240
s_add_u32 s82, s74, 16384
ds_read_b64_tr_b16 a[108:109], v243 offset:36864
v_mfma_f32_16x16x32_bf16 v[228:231], a[76:79], a[36:39], v[228:231]
v_mfma_f32_16x16x32_bf16 v[236:239], a[72:75], a[40:43], 0
ds_read_b64_tr_b16 a[110:111], v243 offset:37120
v_mfma_f32_16x16x32_bf16 v[236:239], a[76:79], a[44:47], v[236:239]
buffer_load_dwordx4 v246, s[40:43], s82 offen lds
v_mfma_f32_16x16x32_bf16 a[164:167], a[72:75], a[48:51], 0
ds_read_b64_tr_b16 a[112:113], v242 offset:33280
v_mfma_f32_16x16x32_bf16 a[164:167], a[76:79], a[52:55], a[164:167]
)ASM"
R"ASM(v_mfma_f32_16x16x32_bf16 a[172:175], a[72:75], a[56:59], 0
ds_read_b64_tr_b16 a[114:115], v242 offset:33536
v_mfma_f32_16x16x32_bf16 a[172:175], a[76:79], a[60:63], a[172:175]
s_add_u32 m0, s80, 9216
s_add_u32 s82, s76, s77
s_add_u32 s93, s93, s91
v_mfma_f32_16x16x32_bf16 a[208:211], a[80:83], a[32:35], 0
ds_read_b64_tr_b16 a[116:117], v242 offset:37376
v_mfma_f32_16x16x32_bf16 a[208:211], a[84:87], a[36:39], a[208:211]
ds_read_b64_tr_b16 a[118:119], v242 offset:37632
v_mfma_f32_16x16x32_bf16 a[216:219], a[80:83], a[40:43], 0
buffer_load_dwordx4 v251, s[52:55], s82 offen lds
v_mfma_f32_16x16x32_bf16 a[216:219], a[84:87], a[44:47], a[216:219]
ds_read_b64_tr_b16 a[120:121], v243 offset:33280
v_mfma_f32_16x16x32_bf16 a[224:227], a[80:83], a[48:51], 0
v_mfma_f32_16x16x32_bf16 a[224:227], a[84:87], a[52:55], a[224:227]
ds_read_b64_tr_b16 a[122:123], v243 offset:33536
v_mfma_f32_16x16x32_bf16 a[232:235], a[80:83], a[56:59], 0
s_add_u32 m0, s79, 11264
s_add_u32 s82, s74, 24576
v_mfma_f32_16x16x32_bf16 a[232:235], a[84:87], a[60:63], a[232:235]
ds_read_b64_tr_b16 a[124:125], v243 offset:37376
v_mfma_f32_16x16x32_bf16 a[212:215], a[88:91], a[32:35], 0
v_mfma_f32_16x16x32_bf16 a[212:215], a[92:95], a[36:39], a[212:215]
ds_read_b64_tr_b16 a[126:127], v243 offset:37632
v_mfma_f32_16x16x32_bf16 a[220:223], a[88:91], a[40:43], 0
buffer_load_dwordx4 v247, s[40:43], s82 offen lds
s_add_u32 s87, s87, 1
v_mfma_f32_16x16x32_bf16 a[220:223], a[92:95], a[44:47], a[220:223]
s_and_b32 s87, s87, 7
s_lshl_b32 s83, s87, 7
s_add_u32 s74, s95, s83
v_mfma_f32_16x16x32_bf16 a[228:231], a[88:91], a[48:51], 0
s_mul_i32 s83, s87, s78
s_add_u32 s76, s97, s83
v_mfma_f32_16x16x32_bf16 a[228:231], a[92:95], a[52:55], a[228:231]
buffer_store_dwordx4 v[12:15], v252, s[60:63], s93 offen sc1
v_mfma_f32_16x16x32_bf16 a[236:239], a[88:91], a[56:59], 0
v_mfma_f32_16x16x32_bf16 a[236:239], a[92:95], a[60:63], a[236:239]
s_waitcnt vmcnt(18) lgkmcnt(0)
s_barrier
v_mfma_f32_16x16x32_bf16 v[192:195], a[96:99], a[0:3], v[192:195]
ds_read_b64_tr_b16 a[128:129], v244 offset:32768
v_mfma_f32_16x16x32_bf16 v[192:195], a[100:103], a[4:7], v[192:195]
v_mfma_f32_16x16x32_bf16 v[200:203], a[96:99], a[8:11], v[200:203]
ds_read_b64_tr_b16 a[130:131], v244 offset:33024
v_mfma_f32_16x16x32_bf16 v[200:203], a[100:103], a[12:15], v[200:203]
s_add_u32 m0, s79, 32768
s_add_u32 s82, s73, 0
v_mfma_f32_16x16x32_bf16 v[208:211], a[96:99], a[16:19], v[208:211]
ds_read_b64_tr_b16 a[132:133], v244 offset:36864
v_mfma_f32_16x16x32_bf16 v[208:211], a[100:103], a[20:23], v[208:211]
buffer_load_dwordx4 v246, s[36:39], s82 offen lds
s_add_u32 s93, s93, s91
v_mfma_f32_16x16x32_bf16 v[216:219], a[96:99], a[24:27], v[216:219]
s_getpc_b64 s[64:65]
mla_fused_g_pc:
s_add_u32 s64, s64, mla_fused_a_tile2end-mla_fused_g_pc
s_addc_u32 s65, s65, 0
s_and_b32 s65, s65, 0xffff
s_mov_b32 s66, mla_fused_b_pfend-mla_fused_a_tile2end
s_mov_b32 s67, 0x20000
v_lshlrev_b32 v254, 6, %[tid]
buffer_load_dword a254, v254, s[64:67], 0 offen
v_add_u32 v254, 0x4000, v254
buffer_load_dword a255, v254, s[64:67], 0 offen
v_mfma_f32_16x16x32_bf16 v[216:219], a[100:103], a[28:31], v[216:219]
v_mfma_f32_16x16x32_bf16 v[196:199], a[104:107], a[0:3], v[196:199]
s_add_u32 m0, s80, 32768
s_add_u32 s82, s75, 0
ds_read_b64_tr_b16 a[134:135], v244 offset:37120
v_mfma_f32_16x16x32_bf16 v[196:199], a[108:111], a[4:7], v[196:199]
v_mfma_f32_16x16x32_bf16 v[204:207], a[104:107], a[8:11], v[204:207]
buffer_load_dwordx4 v248, s[44:47], s82 offen lds
v_mfma_f32_16x16x32_bf16 v[204:207], a[108:111], a[12:15], v[204:207]
ds_read_b64_tr_b16 a[136:137], v245 offset:32768
v_mfma_f32_16x16x32_bf16 v[212:215], a[104:107], a[16:19], v[212:215]
v_mfma_f32_16x16x32_bf16 v[212:215], a[108:111], a[20:23], v[212:215]
s_add_u32 m0, s79, 33792
s_add_u32 s82, s73, 8192
v_mfma_f32_16x16x32_bf16 v[220:223], a[104:107], a[24:27], v[220:223]
ds_read_b64_tr_b16 a[138:139], v245 offset:33024
v_mfma_f32_16x16x32_bf16 v[220:223], a[108:111], a[28:31], v[220:223]
ds_read_b64_tr_b16 a[140:141], v245 offset:36864
v_mfma_f32_16x16x32_bf16 a[176:179], a[112:115], a[0:3], a[176:179]
buffer_load_dwordx4 v247, s[36:39], s82 offen lds
v_mfma_f32_16x16x32_bf16 a[176:179], a[116:119], a[4:7], a[176:179]
buffer_store_dwordx4 v[16:19], v252, s[60:63], s93 offen sc1
v_mfma_f32_16x16x32_bf16 a[184:187], a[112:115], a[8:11], a[184:187]
s_add_u32 m0, s80, 49152
s_add_u32 s82, s75, 0
v_mfma_f32_16x16x32_bf16 a[184:187], a[116:119], a[12:15], a[184:187]
ds_read_b64_tr_b16 a[142:143], v245 offset:37120
v_mfma_f32_16x16x32_bf16 a[192:195], a[112:115], a[16:19], a[192:195]
v_mfma_f32_16x16x32_bf16 a[192:195], a[116:119], a[20:23], a[192:195]
buffer_load_dwordx4 v248, s[48:51], s82 offen lds
v_mfma_f32_16x16x32_bf16 a[200:203], a[112:115], a[24:27], a[200:203]
ds_read_b128 a[32:35], v240 offset:40960
v_mfma_f32_16x16x32_bf16 a[200:203], a[116:119], a[28:31], a[200:203]
s_add_u32 m0, s79, 34816
s_add_u32 s82, s73, 16384
v_mfma_f32_16x16x32_bf16 a[180:183], a[120:123], a[0:3], a[180:183]
v_mfma_f32_16x16x32_bf16 a[180:183], a[124:127], a[4:7], a[180:183]
ds_read_b128 a[36:39], v241 offset:40960
v_mfma_f32_16x16x32_bf16 a[188:191], a[120:123], a[8:11], a[188:191]
buffer_load_dwordx4 v246, s[36:39], s82 offen lds
v_mfma_f32_16x16x32_bf16 a[188:191], a[124:127], a[12:15], a[188:191]
v_mfma_f32_16x16x32_bf16 a[196:199], a[120:123], a[16:19], a[196:199]
ds_read_b128 a[40:43], v240 offset:43008
v_mfma_f32_16x16x32_bf16 a[196:199], a[124:127], a[20:23], a[196:199]
s_add_u32 m0, s80, 33792
s_add_u32 s82, s75, 65536
s_add_u32 s93, s93, s91
v_mfma_f32_16x16x32_bf16 a[204:207], a[120:123], a[24:27], a[204:207]
ds_read_b128 a[44:47], v241 offset:43008
v_mfma_f32_16x16x32_bf16 a[204:207], a[124:127], a[28:31], a[204:207]
buffer_load_dwordx4 v249, s[44:47], s82 offen lds
s_waitcnt lgkmcnt(10)
v_mfma_f32_16x16x32_bf16 v[128:131], a[128:131], a[0:3], v[128:131]
s_waitcnt lgkmcnt(8)
v_mfma_f32_16x16x32_bf16 v[128:131], a[132:135], a[4:7], v[128:131]
ds_read_b128 a[48:51], v240 offset:45056
v_mfma_f32_16x16x32_bf16 v[132:135], a[128:131], a[8:11], v[132:135]
s_add_u32 m0, s79, 35840
s_add_u32 s82, s73, 24576
v_mfma_f32_16x16x32_bf16 v[132:135], a[132:135], a[12:15], v[132:135]
ds_read_b128 a[52:55], v241 offset:45056
v_mfma_f32_16x16x32_bf16 v[136:139], a[128:131], a[16:19], v[136:139]
buffer_load_dwordx4 v247, s[36:39], s82 offen lds
v_mfma_f32_16x16x32_bf16 v[136:139], a[132:135], a[20:23], v[136:139]
v_mfma_f32_16x16x32_bf16 v[140:143], a[128:131], a[24:27], v[140:143]
v_mfma_f32_16x16x32_bf16 v[140:143], a[132:135], a[28:31], v[140:143]
s_add_u32 m0, s80, 50176
s_add_u32 s82, s75, 65536
ds_read_b128 a[56:59], v240 offset:47104
s_waitcnt lgkmcnt(9)
v_mfma_f32_16x16x32_bf16 v[160:163], a[136:139], a[0:3], v[160:163]
s_waitcnt lgkmcnt(7)
v_mfma_f32_16x16x32_bf16 v[160:163], a[140:143], a[4:7], v[160:163]
buffer_store_dwordx4 v[20:23], v252, s[60:63], s93 offen sc1
ds_read_b128 a[60:63], v241 offset:47104
v_mfma_f32_16x16x32_bf16 v[164:167], a[136:139], a[8:11], v[164:167]
buffer_load_dwordx4 v249, s[48:51], s82 offen lds
s_add_u32 s86, s86, 1
v_mfma_f32_16x16x32_bf16 v[164:167], a[140:143], a[12:15], v[164:167]
s_and_b32 s86, s86, 7
s_lshl_b32 s83, s86, 7
s_add_u32 s73, s94, s83
v_mfma_f32_16x16x32_bf16 v[168:171], a[136:139], a[16:19], v[168:171]
s_lshl_b32 s83, s86, 19
s_add_u32 s75, s96, s83
v_mfma_f32_16x16x32_bf16 v[168:171], a[140:143], a[20:23], v[168:171]
v_mfma_f32_16x16x32_bf16 v[172:175], a[136:139], a[24:27], v[172:175]
v_mfma_f32_16x16x32_bf16 v[172:175], a[140:143], a[28:31], v[172:175]
s_waitcnt vmcnt(20) lgkmcnt(0)
s_barrier
v_mfma_f32_16x16x32_bf16 v[144:147], a[128:131], a[32:35], v[144:147]
ds_read_b64_tr_b16 a[64:65], v242 offset:0
v_mfma_f32_16x16x32_bf16 v[144:147], a[132:135], a[36:39], v[144:147]
ds_read_b64_tr_b16 a[66:67], v242 offset:256
v_mfma_f32_16x16x32_bf16 v[148:151], a[128:131], a[40:43], v[148:151]
v_mfma_f32_16x16x32_bf16 v[148:151], a[132:135], a[44:47], v[148:151]
s_add_u32 m0, s79, 40960
s_add_u32 s82, s74, 0
ds_read_b64_tr_b16 a[68:69], v242 offset:4096
v_mfma_f32_16x16x32_bf16 v[152:155], a[128:131], a[48:51], v[152:155]
v_mfma_f32_16x16x32_bf16 v[152:155], a[132:135], a[52:55], v[152:155]
ds_read_b64_tr_b16 a[70:71], v242 offset:4352
v_mfma_f32_16x16x32_bf16 v[156:159], a[128:131], a[56:59], v[156:159]
buffer_load_dwordx4 v246, s[40:43], s82 offen lds
s_add_u32 s93, s93, s91
v_mfma_f32_16x16x32_bf16 v[156:159], a[132:135], a[60:63], v[156:159]
ds_read_b128 a[0:3], v240 offset:0
v_mfma_f32_16x16x32_bf16 v[176:179], a[136:139], a[32:35], v[176:179]
ds_read_b128 a[4:7], v241 offset:0
v_mfma_f32_16x16x32_bf16 v[176:179], a[140:143], a[36:39], v[176:179]
v_mfma_f32_16x16x32_bf16 v[180:183], a[136:139], a[40:43], v[180:183]
s_add_u32 m0, s80, 40960
s_mov_b32 s82, s76
ds_read_b128 a[8:11], v240 offset:2048
v_mfma_f32_16x16x32_bf16 v[180:183], a[140:143], a[44:47], v[180:183]
v_mfma_f32_16x16x32_bf16 v[184:187], a[136:139], a[48:51], v[184:187]
ds_read_b128 a[12:15], v241 offset:2048
v_mfma_f32_16x16x32_bf16 v[184:187], a[140:143], a[52:55], v[184:187]
buffer_load_dwordx4 v250, s[52:55], s82 offen lds
v_mfma_f32_16x16x32_bf16 v[188:191], a[136:139], a[56:59], v[188:191]
ds_read_b128 a[16:19], v240 offset:4096
v_mfma_f32_16x16x32_bf16 v[188:191], a[140:143], a[60:63], v[188:191]
v_mfma_f32_16x16x32_bf16 v[224:227], a[96:99], a[32:35], v[224:227]
ds_read_b128 a[20:23], v241 offset:4096
v_mfma_f32_16x16x32_bf16 v[224:227], a[100:103], a[36:39], v[224:227]
s_add_u32 m0, s79, 41984
s_add_u32 s82, s74, 8192
ds_read_b128 a[24:27], v240 offset:6144
v_mfma_f32_16x16x32_bf16 v[232:235], a[96:99], a[40:43], v[232:235]
ds_read_b128 a[28:31], v241 offset:6144
v_mfma_f32_16x16x32_bf16 v[232:235], a[100:103], a[44:47], v[232:235]
buffer_store_dwordx4 v[24:27], v252, s[60:63], s93 offen sc1
v_mfma_f32_16x16x32_bf16 a[160:163], a[96:99], a[48:51], a[160:163]
buffer_load_dwordx4 v247, s[40:43], s82 offen lds
v_mfma_f32_16x16x32_bf16 a[160:163], a[100:103], a[52:55], a[160:163]
ds_read_b64_tr_b16 a[72:73], v243 offset:0
v_mfma_f32_16x16x32_bf16 a[168:171], a[96:99], a[56:59], a[168:171]
v_mfma_f32_16x16x32_bf16 a[168:171], a[100:103], a[60:63], a[168:171]
ds_read_b64_tr_b16 a[74:75], v243 offset:256
v_mfma_f32_16x16x32_bf16 v[228:231], a[104:107], a[32:35], v[228:231]
s_add_u32 m0, s79, 43008
s_add_u32 s82, s74, 16384
ds_read_b64_tr_b16 a[76:77], v243 offset:4096
v_mfma_f32_16x16x32_bf16 v[228:231], a[108:111], a[36:39], v[228:231]
v_mfma_f32_16x16x32_bf16 v[236:239], a[104:107], a[40:43], v[236:239]
ds_read_b64_tr_b16 a[78:79], v243 offset:4352
v_mfma_f32_16x16x32_bf16 v[236:239], a[108:111], a[44:47], v[236:239]
buffer_load_dwordx4 v246, s[40:43], s82 offen lds
v_mfma_f32_16x16x32_bf16 a[164:167], a[104:107], a[48:51], a[164:167]
ds_read_b64_tr_b16 a[80:81], v242 offset:512
v_mfma_f32_16x16x32_bf16 a[164:167], a[108:111], a[52:55], a[164:167]
v_mfma_f32_16x16x32_bf16 a[172:175], a[104:107], a[56:59], a[172:175]
ds_read_b64_tr_b16 a[82:83], v242 offset:768
v_mfma_f32_16x16x32_bf16 a[172:175], a[108:111], a[60:63], a[172:175]
s_add_u32 m0, s80, 41984
s_add_u32 s82, s76, s77
s_add_u32 s93, s93, s91
v_mfma_f32_16x16x32_bf16 a[208:211], a[112:115], a[32:35], a[208:211]
ds_read_b64_tr_b16 a[84:85], v242 offset:4608
v_mfma_f32_16x16x32_bf16 a[208:211], a[116:119], a[36:39], a[208:211]
ds_read_b64_tr_b16 a[86:87], v242 offset:4864
v_mfma_f32_16x16x32_bf16 a[216:219], a[112:115], a[40:43], a[216:219]
buffer_load_dwordx4 v251, s[52:55], s82 offen lds
v_mfma_f32_16x16x32_bf16 a[216:219], a[116:119], a[44:47], a[216:219]
ds_read_b64_tr_b16 a[88:89], v243 offset:512
v_mfma_f32_16x16x32_bf16 a[224:227], a[112:115], a[48:51], a[224:227]
v_mfma_f32_16x16x32_bf16 a[224:227], a[116:119], a[52:55], a[224:227]
ds_read_b64_tr_b16 a[90:91], v243 offset:768
v_mfma_f32_16x16x32_bf16 a[232:235], a[112:115], a[56:59], a[232:235]
s_add_u32 m0, s79, 44032
s_add_u32 s82, s74, 24576
v_mfma_f32_16x16x32_bf16 a[232:235], a[116:119], a[60:63], a[232:235]
ds_read_b64_tr_b16 a[92:93], v243 offset:4608
)ASM"
R"ASM(v_mfma_f32_16x16x32_bf16 a[212:215], a[120:123], a[32:35], a[212:215]
v_mfma_f32_16x16x32_bf16 a[212:215], a[124:127], a[36:39], a[212:215]
ds_read_b64_tr_b16 a[94:95], v243 offset:4864
v_mfma_f32_16x16x32_bf16 a[220:223], a[120:123], a[40:43], a[220:223]
buffer_load_dwordx4 v247, s[40:43], s82 offen lds
s_add_u32 s87, s87, 1
v_mfma_f32_16x16x32_bf16 a[220:223], a[124:127], a[44:47], a[220:223]
s_and_b32 s87, s87, 7
s_lshl_b32 s83, s87, 7
s_add_u32 s74, s95, s83
v_mfma_f32_16x16x32_bf16 a[228:231], a[120:123], a[48:51], a[228:231]
s_mul_i32 s83, s87, s78
s_add_u32 s76, s97, s83
v_mfma_f32_16x16x32_bf16 a[228:231], a[124:127], a[52:55], a[228:231]
buffer_store_dwordx4 v[28:31], v252, s[60:63], s93 offen sc1
v_mfma_f32_16x16x32_bf16 a[236:239], a[120:123], a[56:59], a[236:239]
v_mfma_f32_16x16x32_bf16 a[236:239], a[124:127], a[60:63], a[236:239]
s_waitcnt vmcnt(21) lgkmcnt(0)
s_barrier
v_mfma_f32_16x16x32_bf16 v[192:195], a[64:67], a[0:3], v[192:195]
ds_read_b64_tr_b16 a[128:129], v244 offset:0
v_mfma_f32_16x16x32_bf16 v[192:195], a[68:71], a[4:7], v[192:195]
v_mfma_f32_16x16x32_bf16 v[200:203], a[64:67], a[8:11], v[200:203]
ds_read_b64_tr_b16 a[130:131], v244 offset:256
v_mfma_f32_16x16x32_bf16 v[200:203], a[68:71], a[12:15], v[200:203]
s_add_u32 m0, s79, 0
s_add_u32 s82, s73, 0
v_mfma_f32_16x16x32_bf16 v[208:211], a[64:67], a[16:19], v[208:211]
ds_read_b64_tr_b16 a[132:133], v244 offset:4096
v_mfma_f32_16x16x32_bf16 v[208:211], a[68:71], a[20:23], v[208:211]
buffer_load_dwordx4 v246, s[36:39], s82 offen lds
v_mfma_f32_16x16x32_bf16 v[216:219], a[64:67], a[24:27], v[216:219]
s_add_u32 s90, s90, s92
s_mov_b32 s93, s90
v_mfma_f32_16x16x32_bf16 v[216:219], a[68:71], a[28:31], v[216:219]
v_mfma_f32_16x16x32_bf16 v[196:199], a[72:75], a[0:3], v[196:199]
s_add_u32 m0, s80, 0
s_add_u32 s82, s75, 0
ds_read_b64_tr_b16 a[134:135], v244 offset:4352
v_mfma_f32_16x16x32_bf16 v[196:199], a[76:79], a[4:7], v[196:199]
v_mfma_f32_16x16x32_bf16 v[204:207], a[72:75], a[8:11], v[204:207]
buffer_load_dwordx4 v248, s[44:47], s82 offen lds
v_mfma_f32_16x16x32_bf16 v[204:207], a[76:79], a[12:15], v[204:207]
ds_read_b64_tr_b16 a[136:137], v245 offset:0
v_mfma_f32_16x16x32_bf16 v[212:215], a[72:75], a[16:19], v[212:215]
v_mfma_f32_16x16x32_bf16 v[212:215], a[76:79], a[20:23], v[212:215]
s_add_u32 m0, s79, 1024
s_add_u32 s82, s73, 8192
v_mfma_f32_16x16x32_bf16 v[220:223], a[72:75], a[24:27], v[220:223]
ds_read_b64_tr_b16 a[138:139], v245 offset:256
v_mfma_f32_16x16x32_bf16 v[220:223], a[76:79], a[28:31], v[220:223]
ds_read_b64_tr_b16 a[140:141], v245 offset:4096
v_mfma_f32_16x16x32_bf16 a[176:179], a[80:83], a[0:3], a[176:179]
buffer_load_dwordx4 v247, s[36:39], s82 offen lds
v_mfma_f32_16x16x32_bf16 a[176:179], a[84:87], a[4:7], a[176:179]
buffer_store_dwordx4 v[32:35], v252, s[60:63], s93 offen sc1
v_mfma_f32_16x16x32_bf16 a[184:187], a[80:83], a[8:11], a[184:187]
s_add_u32 m0, s80, 16384
s_add_u32 s82, s75, 0
v_mfma_f32_16x16x32_bf16 a[184:187], a[84:87], a[12:15], a[184:187]
ds_read_b64_tr_b16 a[142:143], v245 offset:4352
v_mfma_f32_16x16x32_bf16 a[192:195], a[80:83], a[16:19], a[192:195]
v_mfma_f32_16x16x32_bf16 a[192:195], a[84:87], a[20:23], a[192:195]
buffer_load_dwordx4 v248, s[48:51], s82 offen lds
v_mfma_f32_16x16x32_bf16 a[200:203], a[80:83], a[24:27], a[200:203]
ds_read_b128 a[32:35], v240 offset:8192
v_mfma_f32_16x16x32_bf16 a[200:203], a[84:87], a[28:31], a[200:203]
s_add_u32 m0, s79, 2048
s_add_u32 s82, s73, 16384
v_mfma_f32_16x16x32_bf16 a[180:183], a[88:91], a[0:3], a[180:183]
v_mfma_f32_16x16x32_bf16 a[180:183], a[92:95], a[4:7], a[180:183]
ds_read_b128 a[36:39], v241 offset:8192
v_mfma_f32_16x16x32_bf16 a[188:191], a[88:91], a[8:11], a[188:191]
buffer_load_dwordx4 v246, s[36:39], s82 offen lds
v_mfma_f32_16x16x32_bf16 a[188:191], a[92:95], a[12:15], a[188:191]
v_mfma_f32_16x16x32_bf16 a[196:199], a[88:91], a[16:19], a[196:199]
ds_read_b128 a[40:43], v240 offset:10240
v_mfma_f32_16x16x32_bf16 a[196:199], a[92:95], a[20:23], a[196:199]
s_add_u32 m0, s80, 1024
s_add_u32 s82, s75, 65536
s_add_u32 s93, s93, s91
v_mfma_f32_16x16x32_bf16 a[204:207], a[88:91], a[24:27], a[204:207]
ds_read_b128 a[44:47], v241 offset:10240
v_mfma_f32_16x16x32_bf16 a[204:207], a[92:95], a[28:31], a[204:207]
buffer_load_dwordx4 v249, s[44:47], s82 offen lds
s_waitcnt lgkmcnt(10)
v_mfma_f32_16x16x32_bf16 v[128:131], a[128:131], a[0:3], v[128:131]
s_waitcnt lgkmcnt(8)
v_mfma_f32_16x16x32_bf16 v[128:131], a[132:135], a[4:7], v[128:131]
ds_read_b128 a[48:51], v240 offset:12288
v_mfma_f32_16x16x32_bf16 v[132:135], a[128:131], a[8:11], v[132:135]
s_add_u32 m0, s79, 3072
s_add_u32 s82, s73, 24576
v_mfma_f32_16x16x32_bf16 v[132:135], a[132:135], a[12:15], v[132:135]
ds_read_b128 a[52:55], v241 offset:12288
v_mfma_f32_16x16x32_bf16 v[136:139], a[128:131], a[16:19], v[136:139]
buffer_load_dwordx4 v247, s[36:39], s82 offen lds
v_mfma_f32_16x16x32_bf16 v[136:139], a[132:135], a[20:23], v[136:139]
v_mfma_f32_16x16x32_bf16 v[140:143], a[128:131], a[24:27], v[140:143]
v_mfma_f32_16x16x32_bf16 v[140:143], a[132:135], a[28:31], v[140:143]
s_add_u32 m0, s80, 17408
s_add_u32 s82, s75, 65536
ds_read_b128 a[56:59], v240 offset:14336
s_waitcnt lgkmcnt(9)
v_mfma_f32_16x16x32_bf16 v[160:163], a[136:139], a[0:3], v[160:163]
s_waitcnt lgkmcnt(7)
v_mfma_f32_16x16x32_bf16 v[160:163], a[140:143], a[4:7], v[160:163]
buffer_store_dwordx4 v[36:39], v252, s[60:63], s93 offen sc1
ds_read_b128 a[60:63], v241 offset:14336
v_mfma_f32_16x16x32_bf16 v[164:167], a[136:139], a[8:11], v[164:167]
buffer_load_dwordx4 v249, s[48:51], s82 offen lds
s_add_u32 s86, s86, 1
v_mfma_f32_16x16x32_bf16 v[164:167], a[140:143], a[12:15], v[164:167]
s_and_b32 s86, s86, 7
s_lshl_b32 s83, s86, 7
s_add_u32 s73, s94, s83
v_mfma_f32_16x16x32_bf16 v[168:171], a[136:139], a[16:19], v[168:171]
s_lshl_b32 s83, s86, 19
s_add_u32 s75, s96, s83
v_mfma_f32_16x16x32_bf16 v[168:171], a[140:143], a[20:23], v[168:171]
v_mfma_f32_16x16x32_bf16 v[172:175], a[136:139], a[24:27], v[172:175]
v_mfma_f32_16x16x32_bf16 v[172:175], a[140:143], a[28:31], v[172:175]
s_waitcnt vmcnt(18) lgkmcnt(0)
s_barrier
v_mfma_f32_16x16x32_bf16 v[144:147], a[128:131], a[32:35], v[144:147]
ds_read_b64_tr_b16 a[96:97], v242 offset:32768
v_mfma_f32_16x16x32_bf16 v[144:147], a[132:135], a[36:39], v[144:147]
ds_read_b64_tr_b16 a[98:99], v242 offset:33024
v_mfma_f32_16x16x32_bf16 v[148:151], a[128:131], a[40:43], v[148:151]
v_mfma_f32_16x16x32_bf16 v[148:151], a[132:135], a[44:47], v[148:151]
s_add_u32 m0, s79, 8192
s_add_u32 s82, s74, 0
ds_read_b64_tr_b16 a[100:101], v242 offset:36864
v_mfma_f32_16x16x32_bf16 v[152:155], a[128:131], a[48:51], v[152:155]
v_mfma_f32_16x16x32_bf16 v[152:155], a[132:135], a[52:55], v[152:155]
ds_read_b64_tr_b16 a[102:103], v242 offset:37120
v_mfma_f32_16x16x32_bf16 v[156:159], a[128:131], a[56:59], v[156:159]
buffer_load_dwordx4 v246, s[40:43], s82 offen lds
s_add_u32 s93, s93, s91
v_mfma_f32_16x16x32_bf16 v[156:159], a[132:135], a[60:63], v[156:159]
ds_read_b128 a[0:3], v240 offset:32768
v_mfma_f32_16x16x32_bf16 v[176:179], a[136:139], a[32:35], v[176:179]
ds_read_b128 a[4:7], v241 offset:32768
v_mfma_f32_16x16x32_bf16 v[176:179], a[140:143], a[36:39], v[176:179]
v_mfma_f32_16x16x32_bf16 v[180:183], a[136:139], a[40:43], v[180:183]
s_add_u32 m0, s80, 8192
s_mov_b32 s82, s76
ds_read_b128 a[8:11], v240 offset:34816
v_mfma_f32_16x16x32_bf16 v[180:183], a[140:143], a[44:47], v[180:183]
v_mfma_f32_16x16x32_bf16 v[184:187], a[136:139], a[48:51], v[184:187]
ds_read_b128 a[12:15], v241 offset:34816
v_mfma_f32_16x16x32_bf16 v[184:187], a[140:143], a[52:55], v[184:187]
buffer_load_dwordx4 v250, s[52:55], s82 offen lds
v_mfma_f32_16x16x32_bf16 v[188:191], a[136:139], a[56:59], v[188:191]
ds_read_b128 a[16:19], v240 offset:36864
v_mfma_f32_16x16x32_bf16 v[188:191], a[140:143], a[60:63], v[188:191]
v_mfma_f32_16x16x32_bf16 v[224:227], a[64:67], a[32:35], v[224:227]
ds_read_b128 a[20:23], v241 offset:36864
v_mfma_f32_16x16x32_bf16 v[224:227], a[68:71], a[36:39], v[224:227]
s_add_u32 m0, s79, 9216
s_add_u32 s82, s74, 8192
ds_read_b128 a[24:27], v240 offset:38912
v_mfma_f32_16x16x32_bf16 v[232:235], a[64:67], a[40:43], v[232:235]
ds_read_b128 a[28:31], v241 offset:38912
v_mfma_f32_16x16x32_bf16 v[232:235], a[68:71], a[44:47], v[232:235]
buffer_store_dwordx4 v[40:43], v252, s[60:63], s93 offen sc1
v_mfma_f32_16x16x32_bf16 a[160:163], a[64:67], a[48:51], a[160:163]
buffer_load_dwordx4 v247, s[40:43], s82 offen lds
v_mfma_f32_16x16x32_bf16 a[160:163], a[68:71], a[52:55], a[160:163]
ds_read_b64_tr_b16 a[104:105], v243 offset:32768
v_mfma_f32_16x16x32_bf16 a[168:171], a[64:67], a[56:59], a[168:171]
v_mfma_f32_16x16x32_bf16 a[168:171], a[68:71], a[60:63], a[168:171]
ds_read_b64_tr_b16 a[106:107], v243 offset:33024
v_mfma_f32_16x16x32_bf16 v[228:231], a[72:75], a[32:35], v[228:231]
s_add_u32 m0, s79, 10240
s_add_u32 s82, s74, 16384
ds_read_b64_tr_b16 a[108:109], v243 offset:36864
v_mfma_f32_16x16x32_bf16 v[228:231], a[76:79], a[36:39], v[228:231]
v_mfma_f32_16x16x32_bf16 v[236:239], a[72:75], a[40:43], v[236:239]
ds_read_b64_tr_b16 a[110:111], v243 offset:37120
v_mfma_f32_16x16x32_bf16 v[236:239], a[76:79], a[44:47], v[236:239]
buffer_load_dwordx4 v246, s[40:43], s82 offen lds
v_mfma_f32_16x16x32_bf16 a[164:167], a[72:75], a[48:51], a[164:167]
ds_read_b64_tr_b16 a[112:113], v242 offset:33280
v_mfma_f32_16x16x32_bf16 a[164:167], a[76:79], a[52:55], a[164:167]
v_mfma_f32_16x16x32_bf16 a[172:175], a[72:75], a[56:59], a[172:175]
ds_read_b64_tr_b16 a[114:115], v242 offset:33536
v_mfma_f32_16x16x32_bf16 a[172:175], a[76:79], a[60:63], a[172:175]
s_add_u32 m0, s80, 9216
s_add_u32 s82, s76, s77
s_add_u32 s93, s93, s91
v_mfma_f32_16x16x32_bf16 a[208:211], a[80:83], a[32:35], a[208:211]
ds_read_b64_tr_b16 a[116:117], v242 offset:37376
v_mfma_f32_16x16x32_bf16 a[208:211], a[84:87], a[36:39], a[208:211]
ds_read_b64_tr_b16 a[118:119], v242 offset:37632
v_mfma_f32_16x16x32_bf16 a[216:219], a[80:83], a[40:43], a[216:219]
buffer_load_dwordx4 v251, s[52:55], s82 offen lds
v_mfma_f32_16x16x32_bf16 a[216:219], a[84:87], a[44:47], a[216:219]
ds_read_b64_tr_b16 a[120:121], v243 offset:33280
v_mfma_f32_16x16x32_bf16 a[224:227], a[80:83], a[48:51], a[224:227]
v_mfma_f32_16x16x32_bf16 a[224:227], a[84:87], a[52:55], a[224:227]
ds_read_b64_tr_b16 a[122:123], v243 offset:33536
v_mfma_f32_16x16x32_bf16 a[232:235], a[80:83], a[56:59], a[232:235]
s_add_u32 m0, s79, 11264
s_add_u32 s82, s74, 24576
v_mfma_f32_16x16x32_bf16 a[232:235], a[84:87], a[60:63], a[232:235]
ds_read_b64_tr_b16 a[124:125], v243 offset:37376
v_mfma_f32_16x16x32_bf16 a[212:215], a[88:91], a[32:35], a[212:215]
v_mfma_f32_16x16x32_bf16 a[212:215], a[92:95], a[36:39], a[212:215]
ds_read_b64_tr_b16 a[126:127], v243 offset:37632
v_mfma_f32_16x16x32_bf16 a[220:223], a[88:91], a[40:43], a[220:223]
buffer_load_dwordx4 v247, s[40:43], s82 offen lds
s_add_u32 s87, s87, 1
v_mfma_f32_16x16x32_bf16 a[220:223], a[92:95], a[44:47], a[220:223]
s_and_b32 s87, s87, 7
s_lshl_b32 s83, s87, 7
s_add_u32 s74, s95, s83
v_mfma_f32_16x16x32_bf16 a[228:231], a[88:91], a[48:51], a[228:231]
s_mul_i32 s83, s87, s78
s_add_u32 s76, s97, s83
v_mfma_f32_16x16x32_bf16 a[228:231], a[92:95], a[52:55], a[228:231]
buffer_store_dwordx4 v[44:47], v252, s[60:63], s93 offen sc1
v_mfma_f32_16x16x32_bf16 a[236:239], a[88:91], a[56:59], a[236:239]
v_mfma_f32_16x16x32_bf16 a[236:239], a[92:95], a[60:63], a[236:239]
s_waitcnt vmcnt(19) lgkmcnt(0)
s_barrier
v_mfma_f32_16x16x32_bf16 v[192:195], a[96:99], a[0:3], v[192:195]
ds_read_b64_tr_b16 a[128:129], v244 offset:32768
v_mfma_f32_16x16x32_bf16 v[192:195], a[100:103], a[4:7], v[192:195]
v_mfma_f32_16x16x32_bf16 v[200:203], a[96:99], a[8:11], v[200:203]
ds_read_b64_tr_b16 a[130:131], v244 offset:33024
v_mfma_f32_16x16x32_bf16 v[200:203], a[100:103], a[12:15], v[200:203]
s_add_u32 m0, s79, 32768
s_add_u32 s82, s73, 0
v_mfma_f32_16x16x32_bf16 v[208:211], a[96:99], a[16:19], v[208:211]
ds_read_b64_tr_b16 a[132:133], v244 offset:36864
v_mfma_f32_16x16x32_bf16 v[208:211], a[100:103], a[20:23], v[208:211]
)ASM"
R"ASM(buffer_load_dwordx4 v246, s[36:39], s82 offen lds
s_add_u32 s93, s93, s91
v_mfma_f32_16x16x32_bf16 v[216:219], a[96:99], a[24:27], v[216:219]
v_mfma_f32_16x16x32_bf16 v[216:219], a[100:103], a[28:31], v[216:219]
v_mfma_f32_16x16x32_bf16 v[196:199], a[104:107], a[0:3], v[196:199]
s_add_u32 m0, s80, 32768
s_add_u32 s82, s75, 0
ds_read_b64_tr_b16 a[134:135], v244 offset:37120
v_mfma_f32_16x16x32_bf16 v[196:199], a[108:111], a[4:7], v[196:199]
v_mfma_f32_16x16x32_bf16 v[204:207], a[104:107], a[8:11], v[204:207]
buffer_load_dwordx4 v248, s[44:47], s82 offen lds
v_mfma_f32_16x16x32_bf16 v[204:207], a[108:111], a[12:15], v[204:207]
ds_read_b64_tr_b16 a[136:137], v245 offset:32768
v_mfma_f32_16x16x32_bf16 v[212:215], a[104:107], a[16:19], v[212:215]
v_mfma_f32_16x16x32_bf16 v[212:215], a[108:111], a[20:23], v[212:215]
s_add_u32 m0, s79, 33792
s_add_u32 s82, s73, 8192
v_mfma_f32_16x16x32_bf16 v[220:223], a[104:107], a[24:27], v[220:223]
ds_read_b64_tr_b16 a[138:139], v245 offset:33024
v_mfma_f32_16x16x32_bf16 v[220:223], a[108:111], a[28:31], v[220:223]
ds_read_b64_tr_b16 a[140:141], v245 offset:36864
v_mfma_f32_16x16x32_bf16 a[176:179], a[112:115], a[0:3], a[176:179]
buffer_load_dwordx4 v247, s[36:39], s82 offen lds
v_mfma_f32_16x16x32_bf16 a[176:179], a[116:119], a[4:7], a[176:179]
buffer_store_dwordx4 v[48:51], v252, s[60:63], s93 offen sc1
v_mfma_f32_16x16x32_bf16 a[184:187], a[112:115], a[8:11], a[184:187]
s_add_u32 m0, s80, 49152
s_add_u32 s82, s75, 0
v_mfma_f32_16x16x32_bf16 a[184:187], a[116:119], a[12:15], a[184:187]
ds_read_b64_tr_b16 a[142:143], v245 offset:37120
v_mfma_f32_16x16x32_bf16 a[192:195], a[112:115], a[16:19], a[192:195]
v_mfma_f32_16x16x32_bf16 a[192:195], a[116:119], a[20:23], a[192:195]
buffer_load_dwordx4 v248, s[48:51], s82 offen lds
v_mfma_f32_16x16x32_bf16 a[200:203], a[112:115], a[24:27], a[200:203]
ds_read_b128 a[32:35], v240 offset:40960
v_mfma_f32_16x16x32_bf16 a[200:203], a[116:119], a[28:31], a[200:203]
s_add_u32 m0, s79, 34816
s_add_u32 s82, s73, 16384
v_mfma_f32_16x16x32_bf16 a[180:183], a[120:123], a[0:3], a[180:183]
v_mfma_f32_16x16x32_bf16 a[180:183], a[124:127], a[4:7], a[180:183]
ds_read_b128 a[36:39], v241 offset:40960
v_mfma_f32_16x16x32_bf16 a[188:191], a[120:123], a[8:11], a[188:191]
buffer_load_dwordx4 v246, s[36:39], s82 offen lds
v_mfma_f32_16x16x32_bf16 a[188:191], a[124:127], a[12:15], a[188:191]
v_mfma_f32_16x16x32_bf16 a[196:199], a[120:123], a[16:19], a[196:199]
ds_read_b128 a[40:43], v240 offset:43008
v_mfma_f32_16x16x32_bf16 a[196:199], a[124:127], a[20:23], a[196:199]
s_add_u32 m0, s80, 33792
s_add_u32 s82, s75, 65536
s_add_u32 s93, s93, s91
v_mfma_f32_16x16x32_bf16 a[204:207], a[120:123], a[24:27], a[204:207]
ds_read_b128 a[44:47], v241 offset:43008
v_mfma_f32_16x16x32_bf16 a[204:207], a[124:127], a[28:31], a[204:207]
buffer_load_dwordx4 v249, s[44:47], s82 offen lds
s_waitcnt lgkmcnt(10)
v_mfma_f32_16x16x32_bf16 v[128:131], a[128:131], a[0:3], v[128:131]
s_waitcnt lgkmcnt(8)
v_mfma_f32_16x16x32_bf16 v[128:131], a[132:135], a[4:7], v[128:131]
ds_read_b128 a[48:51], v240 offset:45056
v_mfma_f32_16x16x32_bf16 v[132:135], a[128:131], a[8:11], v[132:135]
s_add_u32 m0, s79, 35840
s_add_u32 s82, s73, 24576
v_mfma_f32_16x16x32_bf16 v[132:135], a[132:135], a[12:15], v[132:135]
ds_read_b128 a[52:55], v241 offset:45056
v_mfma_f32_16x16x32_bf16 v[136:139], a[128:131], a[16:19], v[136:139]
buffer_load_dwordx4 v247, s[36:39], s82 offen lds
v_mfma_f32_16x16x32_bf16 v[136:139], a[132:135], a[20:23], v[136:139]
v_mfma_f32_16x16x32_bf16 v[140:143], a[128:131], a[24:27], v[140:143]
v_mfma_f32_16x16x32_bf16 v[140:143], a[132:135], a[28:31], v[140:143]
s_add_u32 m0, s80, 50176
s_add_u32 s82, s75, 65536
ds_read_b128 a[56:59], v240 offset:47104
s_waitcnt lgkmcnt(9)
v_mfma_f32_16x16x32_bf16 v[160:163], a[136:139], a[0:3], v[160:163]
s_waitcnt lgkmcnt(7)
v_mfma_f32_16x16x32_bf16 v[160:163], a[140:143], a[4:7], v[160:163]
buffer_store_dwordx4 v[52:55], v252, s[60:63], s93 offen sc1
ds_read_b128 a[60:63], v241 offset:47104
v_mfma_f32_16x16x32_bf16 v[164:167], a[136:139], a[8:11], v[164:167]
buffer_load_dwordx4 v249, s[48:51], s82 offen lds
s_add_u32 s86, s86, 1
v_mfma_f32_16x16x32_bf16 v[164:167], a[140:143], a[12:15], v[164:167]
s_and_b32 s86, s86, 7
s_lshl_b32 s83, s86, 7
s_add_u32 s73, s94, s83
v_mfma_f32_16x16x32_bf16 v[168:171], a[136:139], a[16:19], v[168:171]
s_lshl_b32 s83, s86, 19
s_add_u32 s75, s96, s83
v_mfma_f32_16x16x32_bf16 v[168:171], a[140:143], a[20:23], v[168:171]
v_mfma_f32_16x16x32_bf16 v[172:175], a[136:139], a[24:27], v[172:175]
v_mfma_f32_16x16x32_bf16 v[172:175], a[140:143], a[28:31], v[172:175]
s_waitcnt vmcnt(18) lgkmcnt(0)
s_barrier
v_mfma_f32_16x16x32_bf16 v[144:147], a[128:131], a[32:35], v[144:147]
ds_read_b64_tr_b16 a[64:65], v242 offset:0
v_mfma_f32_16x16x32_bf16 v[144:147], a[132:135], a[36:39], v[144:147]
ds_read_b64_tr_b16 a[66:67], v242 offset:256
v_mfma_f32_16x16x32_bf16 v[148:151], a[128:131], a[40:43], v[148:151]
v_mfma_f32_16x16x32_bf16 v[148:151], a[132:135], a[44:47], v[148:151]
s_add_u32 m0, s79, 40960
s_add_u32 s82, s74, 0
ds_read_b64_tr_b16 a[68:69], v242 offset:4096
v_mfma_f32_16x16x32_bf16 v[152:155], a[128:131], a[48:51], v[152:155]
v_mfma_f32_16x16x32_bf16 v[152:155], a[132:135], a[52:55], v[152:155]
ds_read_b64_tr_b16 a[70:71], v242 offset:4352
v_mfma_f32_16x16x32_bf16 v[156:159], a[128:131], a[56:59], v[156:159]
buffer_load_dwordx4 v246, s[40:43], s82 offen lds
s_add_u32 s93, s93, s91
v_mfma_f32_16x16x32_bf16 v[156:159], a[132:135], a[60:63], v[156:159]
ds_read_b128 a[0:3], v240 offset:0
v_mfma_f32_16x16x32_bf16 v[176:179], a[136:139], a[32:35], v[176:179]
ds_read_b128 a[4:7], v241 offset:0
v_mfma_f32_16x16x32_bf16 v[176:179], a[140:143], a[36:39], v[176:179]
v_mfma_f32_16x16x32_bf16 v[180:183], a[136:139], a[40:43], v[180:183]
s_add_u32 m0, s80, 40960
s_mov_b32 s82, s76
ds_read_b128 a[8:11], v240 offset:2048
v_mfma_f32_16x16x32_bf16 v[180:183], a[140:143], a[44:47], v[180:183]
v_mfma_f32_16x16x32_bf16 v[184:187], a[136:139], a[48:51], v[184:187]
ds_read_b128 a[12:15], v241 offset:2048
v_mfma_f32_16x16x32_bf16 v[184:187], a[140:143], a[52:55], v[184:187]
buffer_load_dwordx4 v250, s[52:55], s82 offen lds
v_mfma_f32_16x16x32_bf16 v[188:191], a[136:139], a[56:59], v[188:191]
ds_read_b128 a[16:19], v240 offset:4096
v_mfma_f32_16x16x32_bf16 v[188:191], a[140:143], a[60:63], v[188:191]
v_mfma_f32_16x16x32_bf16 v[224:227], a[96:99], a[32:35], v[224:227]
ds_read_b128 a[20:23], v241 offset:4096
v_mfma_f32_16x16x32_bf16 v[224:227], a[100:103], a[36:39], v[224:227]
s_add_u32 m0, s79, 41984
s_add_u32 s82, s74, 8192
ds_read_b128 a[24:27], v240 offset:6144
v_mfma_f32_16x16x32_bf16 v[232:235], a[96:99], a[40:43], v[232:235]
ds_read_b128 a[28:31], v241 offset:6144
v_mfma_f32_16x16x32_bf16 v[232:235], a[100:103], a[44:47], v[232:235]
buffer_store_dwordx4 v[56:59], v252, s[60:63], s93 offen sc1
v_mfma_f32_16x16x32_bf16 a[160:163], a[96:99], a[48:51], a[160:163]
buffer_load_dwordx4 v247, s[40:43], s82 offen lds
v_mfma_f32_16x16x32_bf16 a[160:163], a[100:103], a[52:55], a[160:163]
ds_read_b64_tr_b16 a[72:73], v243 offset:0
v_mfma_f32_16x16x32_bf16 a[168:171], a[96:99], a[56:59], a[168:171]
v_mfma_f32_16x16x32_bf16 a[168:171], a[100:103], a[60:63], a[168:171]
ds_read_b64_tr_b16 a[74:75], v243 offset:256
v_mfma_f32_16x16x32_bf16 v[228:231], a[104:107], a[32:35], v[228:231]
s_add_u32 m0, s79, 43008
s_add_u32 s82, s74, 16384
ds_read_b64_tr_b16 a[76:77], v243 offset:4096
v_mfma_f32_16x16x32_bf16 v[228:231], a[108:111], a[36:39], v[228:231]
v_mfma_f32_16x16x32_bf16 v[236:239], a[104:107], a[40:43], v[236:239]
ds_read_b64_tr_b16 a[78:79], v243 offset:4352
v_mfma_f32_16x16x32_bf16 v[236:239], a[108:111], a[44:47], v[236:239]
buffer_load_dwordx4 v246, s[40:43], s82 offen lds
v_mfma_f32_16x16x32_bf16 a[164:167], a[104:107], a[48:51], a[164:167]
ds_read_b64_tr_b16 a[80:81], v242 offset:512
v_mfma_f32_16x16x32_bf16 a[164:167], a[108:111], a[52:55], a[164:167]
v_mfma_f32_16x16x32_bf16 a[172:175], a[104:107], a[56:59], a[172:175]
ds_read_b64_tr_b16 a[82:83], v242 offset:768
v_mfma_f32_16x16x32_bf16 a[172:175], a[108:111], a[60:63], a[172:175]
s_add_u32 m0, s80, 41984
s_add_u32 s82, s76, s77
s_add_u32 s93, s93, s91
v_mfma_f32_16x16x32_bf16 a[208:211], a[112:115], a[32:35], a[208:211]
ds_read_b64_tr_b16 a[84:85], v242 offset:4608
v_mfma_f32_16x16x32_bf16 a[208:211], a[116:119], a[36:39], a[208:211]
ds_read_b64_tr_b16 a[86:87], v242 offset:4864
v_mfma_f32_16x16x32_bf16 a[216:219], a[112:115], a[40:43], a[216:219]
buffer_load_dwordx4 v251, s[52:55], s82 offen lds
v_mfma_f32_16x16x32_bf16 a[216:219], a[116:119], a[44:47], a[216:219]
ds_read_b64_tr_b16 a[88:89], v243 offset:512
v_mfma_f32_16x16x32_bf16 a[224:227], a[112:115], a[48:51], a[224:227]
v_mfma_f32_16x16x32_bf16 a[224:227], a[116:119], a[52:55], a[224:227]
ds_read_b64_tr_b16 a[90:91], v243 offset:768
v_mfma_f32_16x16x32_bf16 a[232:235], a[112:115], a[56:59], a[232:235]
s_add_u32 m0, s79, 44032
s_add_u32 s82, s74, 24576
v_mfma_f32_16x16x32_bf16 a[232:235], a[116:119], a[60:63], a[232:235]
ds_read_b64_tr_b16 a[92:93], v243 offset:4608
v_mfma_f32_16x16x32_bf16 a[212:215], a[120:123], a[32:35], a[212:215]
v_mfma_f32_16x16x32_bf16 a[212:215], a[124:127], a[36:39], a[212:215]
ds_read_b64_tr_b16 a[94:95], v243 offset:4864
v_mfma_f32_16x16x32_bf16 a[220:223], a[120:123], a[40:43], a[220:223]
buffer_load_dwordx4 v247, s[40:43], s82 offen lds
s_add_u32 s87, s87, 1
v_mfma_f32_16x16x32_bf16 a[220:223], a[124:127], a[44:47], a[220:223]
s_and_b32 s87, s87, 7
s_lshl_b32 s83, s87, 7
s_add_u32 s74, s95, s83
v_mfma_f32_16x16x32_bf16 a[228:231], a[120:123], a[48:51], a[228:231]
s_mul_i32 s83, s87, s78
s_add_u32 s76, s97, s83
v_mfma_f32_16x16x32_bf16 a[228:231], a[124:127], a[52:55], a[228:231]
buffer_store_dwordx4 v[60:63], v252, s[60:63], s93 offen sc1
v_mfma_f32_16x16x32_bf16 a[236:239], a[120:123], a[56:59], a[236:239]
v_mfma_f32_16x16x32_bf16 a[236:239], a[124:127], a[60:63], a[236:239]
s_waitcnt vmcnt(19) lgkmcnt(0)
s_barrier
v_mfma_f32_16x16x32_bf16 v[192:195], a[64:67], a[0:3], v[192:195]
ds_read_b64_tr_b16 a[128:129], v244 offset:0
v_mfma_f32_16x16x32_bf16 v[192:195], a[68:71], a[4:7], v[192:195]
v_mfma_f32_16x16x32_bf16 v[200:203], a[64:67], a[8:11], v[200:203]
ds_read_b64_tr_b16 a[130:131], v244 offset:256
v_mfma_f32_16x16x32_bf16 v[200:203], a[68:71], a[12:15], v[200:203]
s_add_u32 m0, s79, 0
s_add_u32 s82, s73, 0
v_mfma_f32_16x16x32_bf16 v[208:211], a[64:67], a[16:19], v[208:211]
ds_read_b64_tr_b16 a[132:133], v244 offset:4096
v_mfma_f32_16x16x32_bf16 v[208:211], a[68:71], a[20:23], v[208:211]
buffer_load_dwordx4 v246, s[36:39], s82 offen lds
v_mfma_f32_16x16x32_bf16 v[216:219], a[64:67], a[24:27], v[216:219]
s_add_u32 s90, s90, s92
s_mov_b32 s93, s90
v_mfma_f32_16x16x32_bf16 v[216:219], a[68:71], a[28:31], v[216:219]
v_mfma_f32_16x16x32_bf16 v[196:199], a[72:75], a[0:3], v[196:199]
s_add_u32 m0, s80, 0
s_add_u32 s82, s75, 0
ds_read_b64_tr_b16 a[134:135], v244 offset:4352
v_mfma_f32_16x16x32_bf16 v[196:199], a[76:79], a[4:7], v[196:199]
v_mfma_f32_16x16x32_bf16 v[204:207], a[72:75], a[8:11], v[204:207]
buffer_load_dwordx4 v248, s[44:47], s82 offen lds
v_mfma_f32_16x16x32_bf16 v[204:207], a[76:79], a[12:15], v[204:207]
ds_read_b64_tr_b16 a[136:137], v245 offset:0
v_mfma_f32_16x16x32_bf16 v[212:215], a[72:75], a[16:19], v[212:215]
v_mfma_f32_16x16x32_bf16 v[212:215], a[76:79], a[20:23], v[212:215]
s_add_u32 m0, s79, 1024
s_add_u32 s82, s73, 8192
v_mfma_f32_16x16x32_bf16 v[220:223], a[72:75], a[24:27], v[220:223]
ds_read_b64_tr_b16 a[138:139], v245 offset:256
v_mfma_f32_16x16x32_bf16 v[220:223], a[76:79], a[28:31], v[220:223]
ds_read_b64_tr_b16 a[140:141], v245 offset:4096
v_mfma_f32_16x16x32_bf16 a[176:179], a[80:83], a[0:3], a[176:179]
buffer_load_dwordx4 v247, s[36:39], s82 offen lds
v_mfma_f32_16x16x32_bf16 a[176:179], a[84:87], a[4:7], a[176:179]
buffer_store_dwordx4 v[64:67], v252, s[60:63], s93 offen sc1
v_mfma_f32_16x16x32_bf16 a[184:187], a[80:83], a[8:11], a[184:187]
s_add_u32 m0, s80, 16384
)ASM"
R"ASM(s_add_u32 s82, s75, 0
v_mfma_f32_16x16x32_bf16 a[184:187], a[84:87], a[12:15], a[184:187]
ds_read_b64_tr_b16 a[142:143], v245 offset:4352
v_mfma_f32_16x16x32_bf16 a[192:195], a[80:83], a[16:19], a[192:195]
v_mfma_f32_16x16x32_bf16 a[192:195], a[84:87], a[20:23], a[192:195]
buffer_load_dwordx4 v248, s[48:51], s82 offen lds
v_mfma_f32_16x16x32_bf16 a[200:203], a[80:83], a[24:27], a[200:203]
ds_read_b128 a[32:35], v240 offset:8192
v_mfma_f32_16x16x32_bf16 a[200:203], a[84:87], a[28:31], a[200:203]
s_add_u32 m0, s79, 2048
s_add_u32 s82, s73, 16384
v_mfma_f32_16x16x32_bf16 a[180:183], a[88:91], a[0:3], a[180:183]
v_mfma_f32_16x16x32_bf16 a[180:183], a[92:95], a[4:7], a[180:183]
ds_read_b128 a[36:39], v241 offset:8192
v_mfma_f32_16x16x32_bf16 a[188:191], a[88:91], a[8:11], a[188:191]
buffer_load_dwordx4 v246, s[36:39], s82 offen lds
v_mfma_f32_16x16x32_bf16 a[188:191], a[92:95], a[12:15], a[188:191]
v_mfma_f32_16x16x32_bf16 a[196:199], a[88:91], a[16:19], a[196:199]
ds_read_b128 a[40:43], v240 offset:10240
v_mfma_f32_16x16x32_bf16 a[196:199], a[92:95], a[20:23], a[196:199]
s_add_u32 m0, s80, 1024
s_add_u32 s82, s75, 65536
s_add_u32 s93, s93, s91
v_mfma_f32_16x16x32_bf16 a[204:207], a[88:91], a[24:27], a[204:207]
ds_read_b128 a[44:47], v241 offset:10240
v_mfma_f32_16x16x32_bf16 a[204:207], a[92:95], a[28:31], a[204:207]
buffer_load_dwordx4 v249, s[44:47], s82 offen lds
s_waitcnt lgkmcnt(10)
v_mfma_f32_16x16x32_bf16 v[128:131], a[128:131], a[0:3], v[128:131]
s_waitcnt lgkmcnt(8)
v_mfma_f32_16x16x32_bf16 v[128:131], a[132:135], a[4:7], v[128:131]
ds_read_b128 a[48:51], v240 offset:12288
v_mfma_f32_16x16x32_bf16 v[132:135], a[128:131], a[8:11], v[132:135]
s_add_u32 m0, s79, 3072
s_add_u32 s82, s73, 24576
v_mfma_f32_16x16x32_bf16 v[132:135], a[132:135], a[12:15], v[132:135]
ds_read_b128 a[52:55], v241 offset:12288
v_mfma_f32_16x16x32_bf16 v[136:139], a[128:131], a[16:19], v[136:139]
buffer_load_dwordx4 v247, s[36:39], s82 offen lds
v_mfma_f32_16x16x32_bf16 v[136:139], a[132:135], a[20:23], v[136:139]
v_mfma_f32_16x16x32_bf16 v[140:143], a[128:131], a[24:27], v[140:143]
v_mfma_f32_16x16x32_bf16 v[140:143], a[132:135], a[28:31], v[140:143]
s_add_u32 m0, s80, 17408
s_add_u32 s82, s75, 65536
ds_read_b128 a[56:59], v240 offset:14336
s_waitcnt lgkmcnt(9)
v_mfma_f32_16x16x32_bf16 v[160:163], a[136:139], a[0:3], v[160:163]
s_waitcnt lgkmcnt(7)
v_mfma_f32_16x16x32_bf16 v[160:163], a[140:143], a[4:7], v[160:163]
buffer_store_dwordx4 v[68:71], v252, s[60:63], s93 offen sc1
ds_read_b128 a[60:63], v241 offset:14336
v_mfma_f32_16x16x32_bf16 v[164:167], a[136:139], a[8:11], v[164:167]
buffer_load_dwordx4 v249, s[48:51], s82 offen lds
s_add_u32 s86, s86, 1
v_mfma_f32_16x16x32_bf16 v[164:167], a[140:143], a[12:15], v[164:167]
s_and_b32 s86, s86, 7
s_lshl_b32 s83, s86, 7
s_add_u32 s73, s94, s83
v_mfma_f32_16x16x32_bf16 v[168:171], a[136:139], a[16:19], v[168:171]
s_lshl_b32 s83, s86, 19
s_add_u32 s75, s96, s83
v_mfma_f32_16x16x32_bf16 v[168:171], a[140:143], a[20:23], v[168:171]
v_mfma_f32_16x16x32_bf16 v[172:175], a[136:139], a[24:27], v[172:175]
v_mfma_f32_16x16x32_bf16 v[172:175], a[140:143], a[28:31], v[172:175]
s_waitcnt vmcnt(18) lgkmcnt(0)
s_barrier
v_mfma_f32_16x16x32_bf16 v[144:147], a[128:131], a[32:35], v[144:147]
ds_read_b64_tr_b16 a[96:97], v242 offset:32768
v_mfma_f32_16x16x32_bf16 v[144:147], a[132:135], a[36:39], v[144:147]
ds_read_b64_tr_b16 a[98:99], v242 offset:33024
v_mfma_f32_16x16x32_bf16 v[148:151], a[128:131], a[40:43], v[148:151]
v_mfma_f32_16x16x32_bf16 v[148:151], a[132:135], a[44:47], v[148:151]
s_add_u32 m0, s79, 8192
s_add_u32 s82, s74, 0
ds_read_b64_tr_b16 a[100:101], v242 offset:36864
v_mfma_f32_16x16x32_bf16 v[152:155], a[128:131], a[48:51], v[152:155]
v_mfma_f32_16x16x32_bf16 v[152:155], a[132:135], a[52:55], v[152:155]
ds_read_b64_tr_b16 a[102:103], v242 offset:37120
v_mfma_f32_16x16x32_bf16 v[156:159], a[128:131], a[56:59], v[156:159]
buffer_load_dwordx4 v246, s[40:43], s82 offen lds
s_add_u32 s93, s93, s91
v_mfma_f32_16x16x32_bf16 v[156:159], a[132:135], a[60:63], v[156:159]
ds_read_b128 a[0:3], v240 offset:32768
v_mfma_f32_16x16x32_bf16 v[176:179], a[136:139], a[32:35], v[176:179]
ds_read_b128 a[4:7], v241 offset:32768
v_mfma_f32_16x16x32_bf16 v[176:179], a[140:143], a[36:39], v[176:179]
v_mfma_f32_16x16x32_bf16 v[180:183], a[136:139], a[40:43], v[180:183]
s_add_u32 m0, s80, 8192
s_mov_b32 s82, s76
ds_read_b128 a[8:11], v240 offset:34816
v_mfma_f32_16x16x32_bf16 v[180:183], a[140:143], a[44:47], v[180:183]
v_mfma_f32_16x16x32_bf16 v[184:187], a[136:139], a[48:51], v[184:187]
ds_read_b128 a[12:15], v241 offset:34816
v_mfma_f32_16x16x32_bf16 v[184:187], a[140:143], a[52:55], v[184:187]
buffer_load_dwordx4 v250, s[52:55], s82 offen lds
v_mfma_f32_16x16x32_bf16 v[188:191], a[136:139], a[56:59], v[188:191]
ds_read_b128 a[16:19], v240 offset:36864
v_mfma_f32_16x16x32_bf16 v[188:191], a[140:143], a[60:63], v[188:191]
v_mfma_f32_16x16x32_bf16 v[224:227], a[64:67], a[32:35], v[224:227]
ds_read_b128 a[20:23], v241 offset:36864
v_mfma_f32_16x16x32_bf16 v[224:227], a[68:71], a[36:39], v[224:227]
s_add_u32 m0, s79, 9216
s_add_u32 s82, s74, 8192
ds_read_b128 a[24:27], v240 offset:38912
v_mfma_f32_16x16x32_bf16 v[232:235], a[64:67], a[40:43], v[232:235]
ds_read_b128 a[28:31], v241 offset:38912
v_mfma_f32_16x16x32_bf16 v[232:235], a[68:71], a[44:47], v[232:235]
buffer_store_dwordx4 v[72:75], v252, s[60:63], s93 offen sc1
v_mfma_f32_16x16x32_bf16 a[160:163], a[64:67], a[48:51], a[160:163]
buffer_load_dwordx4 v247, s[40:43], s82 offen lds
v_mfma_f32_16x16x32_bf16 a[160:163], a[68:71], a[52:55], a[160:163]
ds_read_b64_tr_b16 a[104:105], v243 offset:32768
v_mfma_f32_16x16x32_bf16 a[168:171], a[64:67], a[56:59], a[168:171]
v_mfma_f32_16x16x32_bf16 a[168:171], a[68:71], a[60:63], a[168:171]
ds_read_b64_tr_b16 a[106:107], v243 offset:33024
v_mfma_f32_16x16x32_bf16 v[228:231], a[72:75], a[32:35], v[228:231]
s_add_u32 m0, s79, 10240
s_add_u32 s82, s74, 16384
ds_read_b64_tr_b16 a[108:109], v243 offset:36864
v_mfma_f32_16x16x32_bf16 v[228:231], a[76:79], a[36:39], v[228:231]
v_mfma_f32_16x16x32_bf16 v[236:239], a[72:75], a[40:43], v[236:239]
ds_read_b64_tr_b16 a[110:111], v243 offset:37120
v_mfma_f32_16x16x32_bf16 v[236:239], a[76:79], a[44:47], v[236:239]
buffer_load_dwordx4 v246, s[40:43], s82 offen lds
v_mfma_f32_16x16x32_bf16 a[164:167], a[72:75], a[48:51], a[164:167]
ds_read_b64_tr_b16 a[112:113], v242 offset:33280
v_mfma_f32_16x16x32_bf16 a[164:167], a[76:79], a[52:55], a[164:167]
v_mfma_f32_16x16x32_bf16 a[172:175], a[72:75], a[56:59], a[172:175]
ds_read_b64_tr_b16 a[114:115], v242 offset:33536
v_mfma_f32_16x16x32_bf16 a[172:175], a[76:79], a[60:63], a[172:175]
s_add_u32 m0, s80, 9216
s_add_u32 s82, s76, s77
s_add_u32 s93, s93, s91
v_mfma_f32_16x16x32_bf16 a[208:211], a[80:83], a[32:35], a[208:211]
ds_read_b64_tr_b16 a[116:117], v242 offset:37376
v_mfma_f32_16x16x32_bf16 a[208:211], a[84:87], a[36:39], a[208:211]
ds_read_b64_tr_b16 a[118:119], v242 offset:37632
v_mfma_f32_16x16x32_bf16 a[216:219], a[80:83], a[40:43], a[216:219]
buffer_load_dwordx4 v251, s[52:55], s82 offen lds
v_mfma_f32_16x16x32_bf16 a[216:219], a[84:87], a[44:47], a[216:219]
ds_read_b64_tr_b16 a[120:121], v243 offset:33280
v_mfma_f32_16x16x32_bf16 a[224:227], a[80:83], a[48:51], a[224:227]
v_mfma_f32_16x16x32_bf16 a[224:227], a[84:87], a[52:55], a[224:227]
ds_read_b64_tr_b16 a[122:123], v243 offset:33536
v_mfma_f32_16x16x32_bf16 a[232:235], a[80:83], a[56:59], a[232:235]
s_add_u32 m0, s79, 11264
s_add_u32 s82, s74, 24576
v_mfma_f32_16x16x32_bf16 a[232:235], a[84:87], a[60:63], a[232:235]
ds_read_b64_tr_b16 a[124:125], v243 offset:37376
v_mfma_f32_16x16x32_bf16 a[212:215], a[88:91], a[32:35], a[212:215]
v_mfma_f32_16x16x32_bf16 a[212:215], a[92:95], a[36:39], a[212:215]
ds_read_b64_tr_b16 a[126:127], v243 offset:37632
v_mfma_f32_16x16x32_bf16 a[220:223], a[88:91], a[40:43], a[220:223]
buffer_load_dwordx4 v247, s[40:43], s82 offen lds
s_add_u32 s87, s87, 1
v_mfma_f32_16x16x32_bf16 a[220:223], a[92:95], a[44:47], a[220:223]
s_and_b32 s87, s87, 7
s_lshl_b32 s83, s87, 7
s_add_u32 s74, s95, s83
v_mfma_f32_16x16x32_bf16 a[228:231], a[88:91], a[48:51], a[228:231]
s_mul_i32 s83, s87, s78
s_add_u32 s76, s97, s83
v_mfma_f32_16x16x32_bf16 a[228:231], a[92:95], a[52:55], a[228:231]
buffer_store_dwordx4 v[76:79], v252, s[60:63], s93 offen sc1
v_mfma_f32_16x16x32_bf16 a[236:239], a[88:91], a[56:59], a[236:239]
v_mfma_f32_16x16x32_bf16 a[236:239], a[92:95], a[60:63], a[236:239]
s_waitcnt vmcnt(19) lgkmcnt(0)
s_barrier
v_mfma_f32_16x16x32_bf16 v[192:195], a[96:99], a[0:3], v[192:195]
ds_read_b64_tr_b16 a[128:129], v244 offset:32768
v_mfma_f32_16x16x32_bf16 v[192:195], a[100:103], a[4:7], v[192:195]
v_mfma_f32_16x16x32_bf16 v[200:203], a[96:99], a[8:11], v[200:203]
ds_read_b64_tr_b16 a[130:131], v244 offset:33024
v_mfma_f32_16x16x32_bf16 v[200:203], a[100:103], a[12:15], v[200:203]
s_add_u32 m0, s79, 32768
s_add_u32 s82, s73, 0
v_mfma_f32_16x16x32_bf16 v[208:211], a[96:99], a[16:19], v[208:211]
ds_read_b64_tr_b16 a[132:133], v244 offset:36864
v_mfma_f32_16x16x32_bf16 v[208:211], a[100:103], a[20:23], v[208:211]
buffer_load_dwordx4 v246, s[36:39], s82 offen lds
s_add_u32 s93, s93, s91
v_mfma_f32_16x16x32_bf16 v[216:219], a[96:99], a[24:27], v[216:219]
v_mfma_f32_16x16x32_bf16 v[216:219], a[100:103], a[28:31], v[216:219]
v_mfma_f32_16x16x32_bf16 v[196:199], a[104:107], a[0:3], v[196:199]
s_add_u32 m0, s80, 32768
s_add_u32 s82, s75, 0
ds_read_b64_tr_b16 a[134:135], v244 offset:37120
v_mfma_f32_16x16x32_bf16 v[196:199], a[108:111], a[4:7], v[196:199]
v_mfma_f32_16x16x32_bf16 v[204:207], a[104:107], a[8:11], v[204:207]
buffer_load_dwordx4 v248, s[44:47], s82 offen lds
v_mfma_f32_16x16x32_bf16 v[204:207], a[108:111], a[12:15], v[204:207]
ds_read_b64_tr_b16 a[136:137], v245 offset:32768
v_mfma_f32_16x16x32_bf16 v[212:215], a[104:107], a[16:19], v[212:215]
v_mfma_f32_16x16x32_bf16 v[212:215], a[108:111], a[20:23], v[212:215]
s_add_u32 m0, s79, 33792
s_add_u32 s82, s73, 8192
v_mfma_f32_16x16x32_bf16 v[220:223], a[104:107], a[24:27], v[220:223]
ds_read_b64_tr_b16 a[138:139], v245 offset:33024
v_mfma_f32_16x16x32_bf16 v[220:223], a[108:111], a[28:31], v[220:223]
ds_read_b64_tr_b16 a[140:141], v245 offset:36864
v_mfma_f32_16x16x32_bf16 a[176:179], a[112:115], a[0:3], a[176:179]
buffer_load_dwordx4 v247, s[36:39], s82 offen lds
v_mfma_f32_16x16x32_bf16 a[176:179], a[116:119], a[4:7], a[176:179]
buffer_store_dwordx4 v[80:83], v252, s[60:63], s93 offen sc1
v_mfma_f32_16x16x32_bf16 a[184:187], a[112:115], a[8:11], a[184:187]
s_add_u32 m0, s80, 49152
s_add_u32 s82, s75, 0
v_mfma_f32_16x16x32_bf16 a[184:187], a[116:119], a[12:15], a[184:187]
ds_read_b64_tr_b16 a[142:143], v245 offset:37120
v_mfma_f32_16x16x32_bf16 a[192:195], a[112:115], a[16:19], a[192:195]
v_mfma_f32_16x16x32_bf16 a[192:195], a[116:119], a[20:23], a[192:195]
buffer_load_dwordx4 v248, s[48:51], s82 offen lds
v_mfma_f32_16x16x32_bf16 a[200:203], a[112:115], a[24:27], a[200:203]
ds_read_b128 a[32:35], v240 offset:40960
v_mfma_f32_16x16x32_bf16 a[200:203], a[116:119], a[28:31], a[200:203]
s_add_u32 m0, s79, 34816
s_add_u32 s82, s73, 16384
v_mfma_f32_16x16x32_bf16 a[180:183], a[120:123], a[0:3], a[180:183]
v_mfma_f32_16x16x32_bf16 a[180:183], a[124:127], a[4:7], a[180:183]
ds_read_b128 a[36:39], v241 offset:40960
v_mfma_f32_16x16x32_bf16 a[188:191], a[120:123], a[8:11], a[188:191]
buffer_load_dwordx4 v246, s[36:39], s82 offen lds
v_mfma_f32_16x16x32_bf16 a[188:191], a[124:127], a[12:15], a[188:191]
v_mfma_f32_16x16x32_bf16 a[196:199], a[120:123], a[16:19], a[196:199]
ds_read_b128 a[40:43], v240 offset:43008
v_mfma_f32_16x16x32_bf16 a[196:199], a[124:127], a[20:23], a[196:199]
s_add_u32 m0, s80, 33792
s_add_u32 s82, s75, 65536
s_add_u32 s93, s93, s91
v_mfma_f32_16x16x32_bf16 a[204:207], a[120:123], a[24:27], a[204:207]
ds_read_b128 a[44:47], v241 offset:43008
v_mfma_f32_16x16x32_bf16 a[204:207], a[124:127], a[28:31], a[204:207]
buffer_load_dwordx4 v249, s[44:47], s82 offen lds
s_waitcnt lgkmcnt(10)
)ASM"
R"ASM(v_mfma_f32_16x16x32_bf16 v[128:131], a[128:131], a[0:3], v[128:131]
s_waitcnt lgkmcnt(8)
v_mfma_f32_16x16x32_bf16 v[128:131], a[132:135], a[4:7], v[128:131]
ds_read_b128 a[48:51], v240 offset:45056
v_mfma_f32_16x16x32_bf16 v[132:135], a[128:131], a[8:11], v[132:135]
s_add_u32 m0, s79, 35840
s_add_u32 s82, s73, 24576
v_mfma_f32_16x16x32_bf16 v[132:135], a[132:135], a[12:15], v[132:135]
ds_read_b128 a[52:55], v241 offset:45056
v_mfma_f32_16x16x32_bf16 v[136:139], a[128:131], a[16:19], v[136:139]
buffer_load_dwordx4 v247, s[36:39], s82 offen lds
v_mfma_f32_16x16x32_bf16 v[136:139], a[132:135], a[20:23], v[136:139]
v_mfma_f32_16x16x32_bf16 v[140:143], a[128:131], a[24:27], v[140:143]
v_mfma_f32_16x16x32_bf16 v[140:143], a[132:135], a[28:31], v[140:143]
s_add_u32 m0, s80, 50176
s_add_u32 s82, s75, 65536
ds_read_b128 a[56:59], v240 offset:47104
s_waitcnt lgkmcnt(9)
v_mfma_f32_16x16x32_bf16 v[160:163], a[136:139], a[0:3], v[160:163]
s_waitcnt lgkmcnt(7)
v_mfma_f32_16x16x32_bf16 v[160:163], a[140:143], a[4:7], v[160:163]
buffer_store_dwordx4 v[84:87], v252, s[60:63], s93 offen sc1
ds_read_b128 a[60:63], v241 offset:47104
v_mfma_f32_16x16x32_bf16 v[164:167], a[136:139], a[8:11], v[164:167]
buffer_load_dwordx4 v249, s[48:51], s82 offen lds
s_add_u32 s86, s86, 1
v_mfma_f32_16x16x32_bf16 v[164:167], a[140:143], a[12:15], v[164:167]
s_and_b32 s86, s86, 7
s_lshl_b32 s83, s86, 7
s_add_u32 s73, s94, s83
v_mfma_f32_16x16x32_bf16 v[168:171], a[136:139], a[16:19], v[168:171]
s_lshl_b32 s83, s86, 19
s_add_u32 s75, s96, s83
v_mfma_f32_16x16x32_bf16 v[168:171], a[140:143], a[20:23], v[168:171]
v_mfma_f32_16x16x32_bf16 v[172:175], a[136:139], a[24:27], v[172:175]
v_mfma_f32_16x16x32_bf16 v[172:175], a[140:143], a[28:31], v[172:175]
s_waitcnt vmcnt(18) lgkmcnt(0)
s_barrier
v_mfma_f32_16x16x32_bf16 v[144:147], a[128:131], a[32:35], v[144:147]
ds_read_b64_tr_b16 a[64:65], v242 offset:0
v_mfma_f32_16x16x32_bf16 v[144:147], a[132:135], a[36:39], v[144:147]
ds_read_b64_tr_b16 a[66:67], v242 offset:256
v_mfma_f32_16x16x32_bf16 v[148:151], a[128:131], a[40:43], v[148:151]
v_mfma_f32_16x16x32_bf16 v[148:151], a[132:135], a[44:47], v[148:151]
s_add_u32 m0, s79, 40960
s_add_u32 s82, s74, 0
ds_read_b64_tr_b16 a[68:69], v242 offset:4096
v_mfma_f32_16x16x32_bf16 v[152:155], a[128:131], a[48:51], v[152:155]
v_mfma_f32_16x16x32_bf16 v[152:155], a[132:135], a[52:55], v[152:155]
ds_read_b64_tr_b16 a[70:71], v242 offset:4352
v_mfma_f32_16x16x32_bf16 v[156:159], a[128:131], a[56:59], v[156:159]
buffer_load_dwordx4 v246, s[40:43], s82 offen lds
s_add_u32 s93, s93, s91
v_mfma_f32_16x16x32_bf16 v[156:159], a[132:135], a[60:63], v[156:159]
ds_read_b128 a[0:3], v240 offset:0
v_mfma_f32_16x16x32_bf16 v[176:179], a[136:139], a[32:35], v[176:179]
ds_read_b128 a[4:7], v241 offset:0
v_mfma_f32_16x16x32_bf16 v[176:179], a[140:143], a[36:39], v[176:179]
v_mfma_f32_16x16x32_bf16 v[180:183], a[136:139], a[40:43], v[180:183]
s_add_u32 m0, s80, 40960
s_mov_b32 s82, s76
ds_read_b128 a[8:11], v240 offset:2048
v_mfma_f32_16x16x32_bf16 v[180:183], a[140:143], a[44:47], v[180:183]
v_mfma_f32_16x16x32_bf16 v[184:187], a[136:139], a[48:51], v[184:187]
ds_read_b128 a[12:15], v241 offset:2048
v_mfma_f32_16x16x32_bf16 v[184:187], a[140:143], a[52:55], v[184:187]
buffer_load_dwordx4 v250, s[52:55], s82 offen lds
v_mfma_f32_16x16x32_bf16 v[188:191], a[136:139], a[56:59], v[188:191]
ds_read_b128 a[16:19], v240 offset:4096
v_mfma_f32_16x16x32_bf16 v[188:191], a[140:143], a[60:63], v[188:191]
v_mfma_f32_16x16x32_bf16 v[224:227], a[96:99], a[32:35], v[224:227]
ds_read_b128 a[20:23], v241 offset:4096
v_mfma_f32_16x16x32_bf16 v[224:227], a[100:103], a[36:39], v[224:227]
s_add_u32 m0, s79, 41984
s_add_u32 s82, s74, 8192
ds_read_b128 a[24:27], v240 offset:6144
v_mfma_f32_16x16x32_bf16 v[232:235], a[96:99], a[40:43], v[232:235]
ds_read_b128 a[28:31], v241 offset:6144
v_mfma_f32_16x16x32_bf16 v[232:235], a[100:103], a[44:47], v[232:235]
buffer_store_dwordx4 v[88:91], v252, s[60:63], s93 offen sc1
v_mfma_f32_16x16x32_bf16 a[160:163], a[96:99], a[48:51], a[160:163]
buffer_load_dwordx4 v247, s[40:43], s82 offen lds
v_mfma_f32_16x16x32_bf16 a[160:163], a[100:103], a[52:55], a[160:163]
ds_read_b64_tr_b16 a[72:73], v243 offset:0
v_mfma_f32_16x16x32_bf16 a[168:171], a[96:99], a[56:59], a[168:171]
v_mfma_f32_16x16x32_bf16 a[168:171], a[100:103], a[60:63], a[168:171]
ds_read_b64_tr_b16 a[74:75], v243 offset:256
v_mfma_f32_16x16x32_bf16 v[228:231], a[104:107], a[32:35], v[228:231]
s_add_u32 m0, s79, 43008
s_add_u32 s82, s74, 16384
ds_read_b64_tr_b16 a[76:77], v243 offset:4096
v_mfma_f32_16x16x32_bf16 v[228:231], a[108:111], a[36:39], v[228:231]
v_mfma_f32_16x16x32_bf16 v[236:239], a[104:107], a[40:43], v[236:239]
ds_read_b64_tr_b16 a[78:79], v243 offset:4352
v_mfma_f32_16x16x32_bf16 v[236:239], a[108:111], a[44:47], v[236:239]
buffer_load_dwordx4 v246, s[40:43], s82 offen lds
v_mfma_f32_16x16x32_bf16 a[164:167], a[104:107], a[48:51], a[164:167]
ds_read_b64_tr_b16 a[80:81], v242 offset:512
v_mfma_f32_16x16x32_bf16 a[164:167], a[108:111], a[52:55], a[164:167]
v_mfma_f32_16x16x32_bf16 a[172:175], a[104:107], a[56:59], a[172:175]
ds_read_b64_tr_b16 a[82:83], v242 offset:768
v_mfma_f32_16x16x32_bf16 a[172:175], a[108:111], a[60:63], a[172:175]
s_add_u32 m0, s80, 41984
s_add_u32 s82, s76, s77
s_add_u32 s93, s93, s91
v_mfma_f32_16x16x32_bf16 a[208:211], a[112:115], a[32:35], a[208:211]
ds_read_b64_tr_b16 a[84:85], v242 offset:4608
v_mfma_f32_16x16x32_bf16 a[208:211], a[116:119], a[36:39], a[208:211]
ds_read_b64_tr_b16 a[86:87], v242 offset:4864
v_mfma_f32_16x16x32_bf16 a[216:219], a[112:115], a[40:43], a[216:219]
buffer_load_dwordx4 v251, s[52:55], s82 offen lds
v_mfma_f32_16x16x32_bf16 a[216:219], a[116:119], a[44:47], a[216:219]
ds_read_b64_tr_b16 a[88:89], v243 offset:512
v_mfma_f32_16x16x32_bf16 a[224:227], a[112:115], a[48:51], a[224:227]
v_mfma_f32_16x16x32_bf16 a[224:227], a[116:119], a[52:55], a[224:227]
ds_read_b64_tr_b16 a[90:91], v243 offset:768
v_mfma_f32_16x16x32_bf16 a[232:235], a[112:115], a[56:59], a[232:235]
s_add_u32 m0, s79, 44032
s_add_u32 s82, s74, 24576
v_mfma_f32_16x16x32_bf16 a[232:235], a[116:119], a[60:63], a[232:235]
ds_read_b64_tr_b16 a[92:93], v243 offset:4608
v_mfma_f32_16x16x32_bf16 a[212:215], a[120:123], a[32:35], a[212:215]
v_mfma_f32_16x16x32_bf16 a[212:215], a[124:127], a[36:39], a[212:215]
ds_read_b64_tr_b16 a[94:95], v243 offset:4864
v_mfma_f32_16x16x32_bf16 a[220:223], a[120:123], a[40:43], a[220:223]
buffer_load_dwordx4 v247, s[40:43], s82 offen lds
s_add_u32 s87, s87, 1
v_mfma_f32_16x16x32_bf16 a[220:223], a[124:127], a[44:47], a[220:223]
s_and_b32 s87, s87, 7
s_lshl_b32 s83, s87, 7
s_add_u32 s74, s95, s83
v_mfma_f32_16x16x32_bf16 a[228:231], a[120:123], a[48:51], a[228:231]
s_mul_i32 s83, s87, s78
s_add_u32 s76, s97, s83
v_mfma_f32_16x16x32_bf16 a[228:231], a[124:127], a[52:55], a[228:231]
buffer_store_dwordx4 v[92:95], v252, s[60:63], s93 offen sc1
v_mfma_f32_16x16x32_bf16 a[236:239], a[120:123], a[56:59], a[236:239]
v_mfma_f32_16x16x32_bf16 a[236:239], a[124:127], a[60:63], a[236:239]
s_waitcnt vmcnt(19) lgkmcnt(0)
s_barrier
v_mfma_f32_16x16x32_bf16 v[192:195], a[64:67], a[0:3], v[192:195]
ds_read_b64_tr_b16 a[128:129], v244 offset:0
v_mfma_f32_16x16x32_bf16 v[192:195], a[68:71], a[4:7], v[192:195]
v_mfma_f32_16x16x32_bf16 v[200:203], a[64:67], a[8:11], v[200:203]
ds_read_b64_tr_b16 a[130:131], v244 offset:256
v_mfma_f32_16x16x32_bf16 v[200:203], a[68:71], a[12:15], v[200:203]
v_mfma_f32_16x16x32_bf16 v[208:211], a[64:67], a[16:19], v[208:211]
v_mfma_f32_16x16x32_bf16 v[208:211], a[68:71], a[20:23], v[208:211]
s_add_u32 s90, s90, s92
s_mov_b32 s93, s90
ds_read_b64_tr_b16 a[132:133], v244 offset:4096
v_mfma_f32_16x16x32_bf16 v[216:219], a[64:67], a[24:27], v[216:219]
v_mfma_f32_16x16x32_bf16 v[216:219], a[68:71], a[28:31], v[216:219]
v_mfma_f32_16x16x32_bf16 v[196:199], a[72:75], a[0:3], v[196:199]
ds_read_b64_tr_b16 a[134:135], v244 offset:4352
v_mfma_f32_16x16x32_bf16 v[196:199], a[76:79], a[4:7], v[196:199]
v_mfma_f32_16x16x32_bf16 v[204:207], a[72:75], a[8:11], v[204:207]
v_mfma_f32_16x16x32_bf16 v[204:207], a[76:79], a[12:15], v[204:207]
ds_read_b64_tr_b16 a[136:137], v245 offset:0
v_mfma_f32_16x16x32_bf16 v[212:215], a[72:75], a[16:19], v[212:215]
v_mfma_f32_16x16x32_bf16 v[212:215], a[76:79], a[20:23], v[212:215]
v_mfma_f32_16x16x32_bf16 v[220:223], a[72:75], a[24:27], v[220:223]
ds_read_b64_tr_b16 a[138:139], v245 offset:256
v_mfma_f32_16x16x32_bf16 v[220:223], a[76:79], a[28:31], v[220:223]
v_mfma_f32_16x16x32_bf16 a[176:179], a[80:83], a[0:3], a[176:179]
ds_read_b64_tr_b16 a[140:141], v245 offset:4096
v_mfma_f32_16x16x32_bf16 a[176:179], a[84:87], a[4:7], a[176:179]
buffer_store_dwordx4 v[96:99], v252, s[60:63], s93 offen sc1
v_mfma_f32_16x16x32_bf16 a[184:187], a[80:83], a[8:11], a[184:187]
v_mfma_f32_16x16x32_bf16 a[184:187], a[84:87], a[12:15], a[184:187]
ds_read_b64_tr_b16 a[142:143], v245 offset:4352
v_mfma_f32_16x16x32_bf16 a[192:195], a[80:83], a[16:19], a[192:195]
v_mfma_f32_16x16x32_bf16 a[192:195], a[84:87], a[20:23], a[192:195]
v_mfma_f32_16x16x32_bf16 a[200:203], a[80:83], a[24:27], a[200:203]
ds_read_b128 a[32:35], v240 offset:8192
v_mfma_f32_16x16x32_bf16 a[200:203], a[84:87], a[28:31], a[200:203]
v_mfma_f32_16x16x32_bf16 a[180:183], a[88:91], a[0:3], a[180:183]
v_mfma_f32_16x16x32_bf16 a[180:183], a[92:95], a[4:7], a[180:183]
ds_read_b128 a[36:39], v241 offset:8192
v_mfma_f32_16x16x32_bf16 a[188:191], a[88:91], a[8:11], a[188:191]
v_mfma_f32_16x16x32_bf16 a[188:191], a[92:95], a[12:15], a[188:191]
v_mfma_f32_16x16x32_bf16 a[196:199], a[88:91], a[16:19], a[196:199]
ds_read_b128 a[40:43], v240 offset:10240
v_mfma_f32_16x16x32_bf16 a[196:199], a[92:95], a[20:23], a[196:199]
s_add_u32 s93, s93, s91
v_mfma_f32_16x16x32_bf16 a[204:207], a[88:91], a[24:27], a[204:207]
ds_read_b128 a[44:47], v241 offset:10240
v_mfma_f32_16x16x32_bf16 a[204:207], a[92:95], a[28:31], a[204:207]
s_waitcnt lgkmcnt(10)
v_mfma_f32_16x16x32_bf16 v[128:131], a[128:131], a[0:3], v[128:131]
s_waitcnt lgkmcnt(8)
v_mfma_f32_16x16x32_bf16 v[128:131], a[132:135], a[4:7], v[128:131]
ds_read_b128 a[48:51], v240 offset:12288
v_mfma_f32_16x16x32_bf16 v[132:135], a[128:131], a[8:11], v[132:135]
v_mfma_f32_16x16x32_bf16 v[132:135], a[132:135], a[12:15], v[132:135]
v_mfma_f32_16x16x32_bf16 v[136:139], a[128:131], a[16:19], v[136:139]
ds_read_b128 a[52:55], v241 offset:12288
v_mfma_f32_16x16x32_bf16 v[136:139], a[132:135], a[20:23], v[136:139]
v_mfma_f32_16x16x32_bf16 v[140:143], a[128:131], a[24:27], v[140:143]
v_mfma_f32_16x16x32_bf16 v[140:143], a[132:135], a[28:31], v[140:143]
ds_read_b128 a[56:59], v240 offset:14336
s_waitcnt lgkmcnt(9)
v_mfma_f32_16x16x32_bf16 v[160:163], a[136:139], a[0:3], v[160:163]
s_waitcnt lgkmcnt(7)
v_mfma_f32_16x16x32_bf16 v[160:163], a[140:143], a[4:7], v[160:163]
ds_read_b128 a[60:63], v241 offset:14336
v_mfma_f32_16x16x32_bf16 v[164:167], a[136:139], a[8:11], v[164:167]
buffer_store_dwordx4 v[100:103], v252, s[60:63], s93 offen sc1
v_mfma_f32_16x16x32_bf16 v[164:167], a[140:143], a[12:15], v[164:167]
v_mfma_f32_16x16x32_bf16 v[168:171], a[136:139], a[16:19], v[168:171]
v_mfma_f32_16x16x32_bf16 v[168:171], a[140:143], a[20:23], v[168:171]
v_mfma_f32_16x16x32_bf16 v[172:175], a[136:139], a[24:27], v[172:175]
v_mfma_f32_16x16x32_bf16 v[172:175], a[140:143], a[28:31], v[172:175]
s_waitcnt vmcnt(10) lgkmcnt(0)
s_barrier
v_mfma_f32_16x16x32_bf16 v[144:147], a[128:131], a[32:35], v[144:147]
ds_read_b64_tr_b16 a[96:97], v242 offset:32768
v_mfma_f32_16x16x32_bf16 v[144:147], a[132:135], a[36:39], v[144:147]
ds_read_b64_tr_b16 a[98:99], v242 offset:33024
v_mfma_f32_16x16x32_bf16 v[148:151], a[128:131], a[40:43], v[148:151]
v_mfma_f32_16x16x32_bf16 v[148:151], a[132:135], a[44:47], v[148:151]
ds_read_b64_tr_b16 a[100:101], v242 offset:36864
v_mfma_f32_16x16x32_bf16 v[152:155], a[128:131], a[48:51], v[152:155]
v_mfma_f32_16x16x32_bf16 v[152:155], a[132:135], a[52:55], v[152:155]
ds_read_b64_tr_b16 a[102:103], v242 offset:37120
)ASM"
R"ASM(v_mfma_f32_16x16x32_bf16 v[156:159], a[128:131], a[56:59], v[156:159]
s_add_u32 s93, s93, s91
v_mfma_f32_16x16x32_bf16 v[156:159], a[132:135], a[60:63], v[156:159]
ds_read_b128 a[0:3], v240 offset:32768
v_mfma_f32_16x16x32_bf16 v[176:179], a[136:139], a[32:35], v[176:179]
ds_read_b128 a[4:7], v241 offset:32768
v_mfma_f32_16x16x32_bf16 v[176:179], a[140:143], a[36:39], v[176:179]
v_mfma_f32_16x16x32_bf16 v[180:183], a[136:139], a[40:43], v[180:183]
ds_read_b128 a[8:11], v240 offset:34816
v_mfma_f32_16x16x32_bf16 v[180:183], a[140:143], a[44:47], v[180:183]
v_mfma_f32_16x16x32_bf16 v[184:187], a[136:139], a[48:51], v[184:187]
ds_read_b128 a[12:15], v241 offset:34816
v_mfma_f32_16x16x32_bf16 v[184:187], a[140:143], a[52:55], v[184:187]
v_mfma_f32_16x16x32_bf16 v[188:191], a[136:139], a[56:59], v[188:191]
ds_read_b128 a[16:19], v240 offset:36864
v_mfma_f32_16x16x32_bf16 v[188:191], a[140:143], a[60:63], v[188:191]
v_mfma_f32_16x16x32_bf16 v[224:227], a[64:67], a[32:35], v[224:227]
ds_read_b128 a[20:23], v241 offset:36864
v_mfma_f32_16x16x32_bf16 v[224:227], a[68:71], a[36:39], v[224:227]
ds_read_b128 a[24:27], v240 offset:38912
v_mfma_f32_16x16x32_bf16 v[232:235], a[64:67], a[40:43], v[232:235]
ds_read_b128 a[28:31], v241 offset:38912
v_mfma_f32_16x16x32_bf16 v[232:235], a[68:71], a[44:47], v[232:235]
buffer_store_dwordx4 v[104:107], v252, s[60:63], s93 offen sc1
v_mfma_f32_16x16x32_bf16 a[160:163], a[64:67], a[48:51], a[160:163]
v_mfma_f32_16x16x32_bf16 a[160:163], a[68:71], a[52:55], a[160:163]
ds_read_b64_tr_b16 a[104:105], v243 offset:32768
v_mfma_f32_16x16x32_bf16 a[168:171], a[64:67], a[56:59], a[168:171]
v_mfma_f32_16x16x32_bf16 a[168:171], a[68:71], a[60:63], a[168:171]
ds_read_b64_tr_b16 a[106:107], v243 offset:33024
v_mfma_f32_16x16x32_bf16 v[228:231], a[72:75], a[32:35], v[228:231]
ds_read_b64_tr_b16 a[108:109], v243 offset:36864
v_mfma_f32_16x16x32_bf16 v[228:231], a[76:79], a[36:39], v[228:231]
v_mfma_f32_16x16x32_bf16 v[236:239], a[72:75], a[40:43], v[236:239]
ds_read_b64_tr_b16 a[110:111], v243 offset:37120
v_mfma_f32_16x16x32_bf16 v[236:239], a[76:79], a[44:47], v[236:239]
v_mfma_f32_16x16x32_bf16 a[164:167], a[72:75], a[48:51], a[164:167]
ds_read_b64_tr_b16 a[112:113], v242 offset:33280
v_mfma_f32_16x16x32_bf16 a[164:167], a[76:79], a[52:55], a[164:167]
v_mfma_f32_16x16x32_bf16 a[172:175], a[72:75], a[56:59], a[172:175]
ds_read_b64_tr_b16 a[114:115], v242 offset:33536
v_mfma_f32_16x16x32_bf16 a[172:175], a[76:79], a[60:63], a[172:175]
s_add_u32 s93, s93, s91
v_mfma_f32_16x16x32_bf16 a[208:211], a[80:83], a[32:35], a[208:211]
ds_read_b64_tr_b16 a[116:117], v242 offset:37376
v_mfma_f32_16x16x32_bf16 a[208:211], a[84:87], a[36:39], a[208:211]
ds_read_b64_tr_b16 a[118:119], v242 offset:37632
v_mfma_f32_16x16x32_bf16 a[216:219], a[80:83], a[40:43], a[216:219]
v_mfma_f32_16x16x32_bf16 a[216:219], a[84:87], a[44:47], a[216:219]
ds_read_b64_tr_b16 a[120:121], v243 offset:33280
v_mfma_f32_16x16x32_bf16 a[224:227], a[80:83], a[48:51], a[224:227]
v_mfma_f32_16x16x32_bf16 a[224:227], a[84:87], a[52:55], a[224:227]
ds_read_b64_tr_b16 a[122:123], v243 offset:33536
v_mfma_f32_16x16x32_bf16 a[232:235], a[80:83], a[56:59], a[232:235]
v_mfma_f32_16x16x32_bf16 a[232:235], a[84:87], a[60:63], a[232:235]
ds_read_b64_tr_b16 a[124:125], v243 offset:37376
v_mfma_f32_16x16x32_bf16 a[212:215], a[88:91], a[32:35], a[212:215]
v_mfma_f32_16x16x32_bf16 a[212:215], a[92:95], a[36:39], a[212:215]
ds_read_b64_tr_b16 a[126:127], v243 offset:37632
v_mfma_f32_16x16x32_bf16 a[220:223], a[88:91], a[40:43], a[220:223]
v_mfma_f32_16x16x32_bf16 a[220:223], a[92:95], a[44:47], a[220:223]
v_mfma_f32_16x16x32_bf16 a[228:231], a[88:91], a[48:51], a[228:231]
buffer_store_dwordx4 v[108:111], v252, s[60:63], s93 offen sc1
v_mfma_f32_16x16x32_bf16 a[228:231], a[92:95], a[52:55], a[228:231]
v_mfma_f32_16x16x32_bf16 a[236:239], a[88:91], a[56:59], a[236:239]
v_mfma_f32_16x16x32_bf16 a[236:239], a[92:95], a[60:63], a[236:239]
s_waitcnt vmcnt(5) lgkmcnt(0)
s_barrier
v_mfma_f32_16x16x32_bf16 v[192:195], a[96:99], a[0:3], v[192:195]
ds_read_b64_tr_b16 a[128:129], v244 offset:32768
v_mfma_f32_16x16x32_bf16 v[192:195], a[100:103], a[4:7], v[192:195]
v_mfma_f32_16x16x32_bf16 v[200:203], a[96:99], a[8:11], v[200:203]
ds_read_b64_tr_b16 a[130:131], v244 offset:33024
v_mfma_f32_16x16x32_bf16 v[200:203], a[100:103], a[12:15], v[200:203]
v_mfma_f32_16x16x32_bf16 v[208:211], a[96:99], a[16:19], v[208:211]
v_mfma_f32_16x16x32_bf16 v[208:211], a[100:103], a[20:23], v[208:211]
s_add_u32 s93, s93, s91
ds_read_b64_tr_b16 a[132:133], v244 offset:36864
v_mfma_f32_16x16x32_bf16 v[216:219], a[96:99], a[24:27], v[216:219]
v_mfma_f32_16x16x32_bf16 v[216:219], a[100:103], a[28:31], v[216:219]
v_mfma_f32_16x16x32_bf16 v[196:199], a[104:107], a[0:3], v[196:199]
ds_read_b64_tr_b16 a[134:135], v244 offset:37120
v_mfma_f32_16x16x32_bf16 v[196:199], a[108:111], a[4:7], v[196:199]
v_mfma_f32_16x16x32_bf16 v[204:207], a[104:107], a[8:11], v[204:207]
buffer_store_dwordx4 v[112:115], v252, s[60:63], s93 offen sc1
v_mfma_f32_16x16x32_bf16 v[204:207], a[108:111], a[12:15], v[204:207]
ds_read_b64_tr_b16 a[136:137], v245 offset:32768
v_mfma_f32_16x16x32_bf16 v[212:215], a[104:107], a[16:19], v[212:215]
v_mfma_f32_16x16x32_bf16 v[212:215], a[108:111], a[20:23], v[212:215]
v_mfma_f32_16x16x32_bf16 v[220:223], a[104:107], a[24:27], v[220:223]
ds_read_b64_tr_b16 a[138:139], v245 offset:33024
v_mfma_f32_16x16x32_bf16 v[220:223], a[108:111], a[28:31], v[220:223]
v_mfma_f32_16x16x32_bf16 a[176:179], a[112:115], a[0:3], a[176:179]
s_add_u32 s93, s93, s91
ds_read_b64_tr_b16 a[140:141], v245 offset:36864
v_mfma_f32_16x16x32_bf16 a[176:179], a[116:119], a[4:7], a[176:179]
v_mfma_f32_16x16x32_bf16 a[184:187], a[112:115], a[8:11], a[184:187]
v_mfma_f32_16x16x32_bf16 a[184:187], a[116:119], a[12:15], a[184:187]
ds_read_b64_tr_b16 a[142:143], v245 offset:37120
v_mfma_f32_16x16x32_bf16 a[192:195], a[112:115], a[16:19], a[192:195]
v_mfma_f32_16x16x32_bf16 a[192:195], a[116:119], a[20:23], a[192:195]
buffer_store_dwordx4 v[116:119], v252, s[60:63], s93 offen sc1
v_mfma_f32_16x16x32_bf16 a[200:203], a[112:115], a[24:27], a[200:203]
ds_read_b128 a[32:35], v240 offset:40960
v_mfma_f32_16x16x32_bf16 a[200:203], a[116:119], a[28:31], a[200:203]
v_mfma_f32_16x16x32_bf16 a[180:183], a[120:123], a[0:3], a[180:183]
v_mfma_f32_16x16x32_bf16 a[180:183], a[124:127], a[4:7], a[180:183]
ds_read_b128 a[36:39], v241 offset:40960
v_mfma_f32_16x16x32_bf16 a[188:191], a[120:123], a[8:11], a[188:191]
v_mfma_f32_16x16x32_bf16 a[188:191], a[124:127], a[12:15], a[188:191]
s_add_u32 s93, s93, s91
v_mfma_f32_16x16x32_bf16 a[196:199], a[120:123], a[16:19], a[196:199]
ds_read_b128 a[40:43], v240 offset:43008
v_mfma_f32_16x16x32_bf16 a[196:199], a[124:127], a[20:23], a[196:199]
v_mfma_f32_16x16x32_bf16 a[204:207], a[120:123], a[24:27], a[204:207]
ds_read_b128 a[44:47], v241 offset:43008
v_mfma_f32_16x16x32_bf16 a[204:207], a[124:127], a[28:31], a[204:207]
s_waitcnt lgkmcnt(10)
v_mfma_f32_16x16x32_bf16 v[128:131], a[128:131], a[0:3], v[128:131]
buffer_store_dwordx4 v[120:123], v252, s[60:63], s93 offen sc1
s_waitcnt lgkmcnt(8)
v_mfma_f32_16x16x32_bf16 v[128:131], a[132:135], a[4:7], v[128:131]
ds_read_b128 a[48:51], v240 offset:45056
v_mfma_f32_16x16x32_bf16 v[132:135], a[128:131], a[8:11], v[132:135]
v_mfma_f32_16x16x32_bf16 v[132:135], a[132:135], a[12:15], v[132:135]
v_mfma_f32_16x16x32_bf16 v[136:139], a[128:131], a[16:19], v[136:139]
ds_read_b128 a[52:55], v241 offset:45056
v_mfma_f32_16x16x32_bf16 v[136:139], a[132:135], a[20:23], v[136:139]
v_mfma_f32_16x16x32_bf16 v[140:143], a[128:131], a[24:27], v[140:143]
s_add_u32 s93, s93, s91
v_mfma_f32_16x16x32_bf16 v[140:143], a[132:135], a[28:31], v[140:143]
ds_read_b128 a[56:59], v240 offset:47104
s_waitcnt lgkmcnt(9)
v_mfma_f32_16x16x32_bf16 v[160:163], a[136:139], a[0:3], v[160:163]
s_waitcnt lgkmcnt(7)
v_mfma_f32_16x16x32_bf16 v[160:163], a[140:143], a[4:7], v[160:163]
v_mfma_f32_16x16x32_bf16 v[164:167], a[136:139], a[8:11], v[164:167]
ds_read_b128 a[60:63], v241 offset:47104
v_mfma_f32_16x16x32_bf16 v[164:167], a[140:143], a[12:15], v[164:167]
v_mfma_f32_16x16x32_bf16 v[168:171], a[136:139], a[16:19], v[168:171]
buffer_store_dwordx4 v[124:127], v252, s[60:63], s93 offen sc1
v_mfma_f32_16x16x32_bf16 v[168:171], a[140:143], a[20:23], v[168:171]
v_mfma_f32_16x16x32_bf16 v[172:175], a[136:139], a[24:27], v[172:175]
v_mfma_f32_16x16x32_bf16 v[172:175], a[140:143], a[28:31], v[172:175]
s_waitcnt lgkmcnt(0)
s_barrier
s_mov_b64 s[36:37], %[ws]
s_and_b32 s37, s37, 0xffff
s_mov_b32 s38, 0x5712000
s_mov_b32 s39, 0x20000
s_mov_b64 s[40:41], %[ws]
s_and_b32 s41, s41, 0xffff
s_mov_b32 s42, 0x5712000
s_mov_b32 s43, 0x20000
s_sub_u32 s48, 15, s72
s_mov_b32 s49, s72
s_mov_b32 s59, 0
s_mov_b32 s73, 0
s_lshl_b32 s75, s68, 13
s_lshl_b32 s84, s71, 19
s_add_u32 s84, s84, s75
s_lshl_b32 s83, s48, 15
s_add_u32 s83, s83, s84
s_add_u32 s74, s83, 0x1000000
s_add_u32 s52, s83, 0x2000000
s_sub_u32 s51, s74, 32768
s_lshl_b32 s83, s49, 15
s_add_u32 s83, s83, s84
s_add_u32 s53, s83, 0x1000000
s_mov_b32 s54, s75
s_add_u32 s55, s75, 65536
s_cmp_eq_u32 s69, 1
s_cbranch_scc1 mla_fused_a_t15B
s_cmp_eq_u32 s70, 1
s_cbranch_scc1 mla_fused_a_t15V
v_mfma_f32_16x16x32_bf16 v[144:147], a[128:131], a[32:35], v[144:147]
ds_write_b128 v252, v[0:3] offset:0
v_mfma_f32_16x16x32_bf16 v[144:147], a[132:135], a[36:39], v[144:147]
ds_write_b128 v252, v[4:7] offset:4096
v_mfma_f32_16x16x32_bf16 v[148:151], a[128:131], a[40:43], v[148:151]
v_mfma_f32_16x16x32_bf16 v[148:151], a[132:135], a[44:47], v[148:151]
ds_write_b128 v252, v[8:11] offset:8192
v_mfma_f32_16x16x32_bf16 v[152:155], a[128:131], a[48:51], v[152:155]
ds_write_b128 v252, v[12:15] offset:12288
v_mfma_f32_16x16x32_bf16 v[152:155], a[132:135], a[52:55], v[152:155]
v_mfma_f32_16x16x32_bf16 v[156:159], a[128:131], a[56:59], v[156:159]
ds_write_b128 v252, v[16:19] offset:16384
v_mfma_f32_16x16x32_bf16 v[156:159], a[132:135], a[60:63], v[156:159]
ds_write_b128 v252, v[20:23] offset:20480
v_mfma_f32_16x16x32_bf16 v[176:179], a[136:139], a[32:35], v[176:179]
v_mfma_f32_16x16x32_bf16 v[176:179], a[140:143], a[36:39], v[176:179]
ds_write_b128 v252, v[24:27] offset:24576
v_mfma_f32_16x16x32_bf16 v[180:183], a[136:139], a[40:43], v[180:183]
ds_write_b128 v252, v[28:31] offset:28672
v_mfma_f32_16x16x32_bf16 v[180:183], a[140:143], a[44:47], v[180:183]
v_mfma_f32_16x16x32_bf16 v[184:187], a[136:139], a[48:51], v[184:187]
ds_write_b128 v252, v[32:35] offset:1024
v_mfma_f32_16x16x32_bf16 v[184:187], a[140:143], a[52:55], v[184:187]
ds_write_b128 v252, v[36:39] offset:5120
v_mfma_f32_16x16x32_bf16 v[188:191], a[136:139], a[56:59], v[188:191]
v_mfma_f32_16x16x32_bf16 v[188:191], a[140:143], a[60:63], v[188:191]
ds_write_b128 v252, v[40:43] offset:9216
v_mfma_f32_16x16x32_bf16 v[224:227], a[96:99], a[32:35], v[224:227]
ds_write_b128 v252, v[44:47] offset:13312
v_mfma_f32_16x16x32_bf16 v[224:227], a[100:103], a[36:39], v[224:227]
v_mfma_f32_16x16x32_bf16 v[232:235], a[96:99], a[40:43], v[232:235]
ds_write_b128 v252, v[48:51] offset:17408
v_mfma_f32_16x16x32_bf16 v[232:235], a[100:103], a[44:47], v[232:235]
ds_write_b128 v252, v[52:55] offset:21504
v_mfma_f32_16x16x32_bf16 a[160:163], a[96:99], a[48:51], a[160:163]
v_mfma_f32_16x16x32_bf16 a[160:163], a[100:103], a[52:55], a[160:163]
ds_write_b128 v252, v[56:59] offset:25600
v_mfma_f32_16x16x32_bf16 a[168:171], a[96:99], a[56:59], a[168:171]
ds_write_b128 v252, v[60:63] offset:29696
v_mfma_f32_16x16x32_bf16 a[168:171], a[100:103], a[60:63], a[168:171]
v_mfma_f32_16x16x32_bf16 v[228:231], a[104:107], a[32:35], v[228:231]
ds_write_b128 v252, v[64:67] offset:2048
v_mfma_f32_16x16x32_bf16 v[228:231], a[108:111], a[36:39], v[228:231]
ds_write_b128 v252, v[68:71] offset:6144
v_mfma_f32_16x16x32_bf16 v[236:239], a[104:107], a[40:43], v[236:239]
v_mfma_f32_16x16x32_bf16 v[236:239], a[108:111], a[44:47], v[236:239]
ds_write_b128 v252, v[72:75] offset:10240
v_mfma_f32_16x16x32_bf16 a[164:167], a[104:107], a[48:51], a[164:167]
)ASM"
R"ASM(ds_write_b128 v252, v[76:79] offset:14336
v_mfma_f32_16x16x32_bf16 a[164:167], a[108:111], a[52:55], a[164:167]
v_mfma_f32_16x16x32_bf16 a[172:175], a[104:107], a[56:59], a[172:175]
ds_write_b128 v252, v[80:83] offset:18432
v_mfma_f32_16x16x32_bf16 a[172:175], a[108:111], a[60:63], a[172:175]
ds_write_b128 v252, v[84:87] offset:22528
v_mfma_f32_16x16x32_bf16 a[208:211], a[112:115], a[32:35], a[208:211]
v_mfma_f32_16x16x32_bf16 a[208:211], a[116:119], a[36:39], a[208:211]
ds_write_b128 v252, v[88:91] offset:26624
v_mfma_f32_16x16x32_bf16 a[216:219], a[112:115], a[40:43], a[216:219]
ds_write_b128 v252, v[92:95] offset:30720
v_mfma_f32_16x16x32_bf16 a[216:219], a[116:119], a[44:47], a[216:219]
v_mfma_f32_16x16x32_bf16 a[224:227], a[112:115], a[48:51], a[224:227]
ds_write_b128 v252, v[96:99] offset:3072
v_mfma_f32_16x16x32_bf16 a[224:227], a[116:119], a[52:55], a[224:227]
ds_write_b128 v252, v[100:103] offset:7168
v_mfma_f32_16x16x32_bf16 a[232:235], a[112:115], a[56:59], a[232:235]
v_mfma_f32_16x16x32_bf16 a[232:235], a[116:119], a[60:63], a[232:235]
ds_write_b128 v252, v[104:107] offset:11264
v_mfma_f32_16x16x32_bf16 a[212:215], a[120:123], a[32:35], a[212:215]
ds_write_b128 v252, v[108:111] offset:15360
v_mfma_f32_16x16x32_bf16 a[212:215], a[124:127], a[36:39], a[212:215]
v_mfma_f32_16x16x32_bf16 a[220:223], a[120:123], a[40:43], a[220:223]
ds_write_b128 v252, v[112:115] offset:19456
v_mfma_f32_16x16x32_bf16 a[220:223], a[124:127], a[44:47], a[220:223]
ds_write_b128 v252, v[116:119] offset:23552
v_mfma_f32_16x16x32_bf16 a[228:231], a[120:123], a[48:51], a[228:231]
v_mfma_f32_16x16x32_bf16 a[228:231], a[124:127], a[52:55], a[228:231]
ds_write_b128 v252, v[120:123] offset:27648
v_mfma_f32_16x16x32_bf16 a[236:239], a[120:123], a[56:59], a[236:239]
v_mfma_f32_16x16x32_bf16 a[236:239], a[124:127], a[60:63], a[236:239]
ds_write_b128 v252, v[124:127] offset:31744
s_branch mla_fused_a_t15E
mla_fused_a_t15V:
v_add_u32 v253, 0x10000, v252
v_mfma_f32_16x16x32_bf16 v[144:147], a[128:131], a[32:35], v[144:147]
ds_write_b128 v253, v[0:3] offset:0
v_mfma_f32_16x16x32_bf16 v[144:147], a[132:135], a[36:39], v[144:147]
ds_write_b128 v253, v[4:7] offset:1024
v_mfma_f32_16x16x32_bf16 v[148:151], a[128:131], a[40:43], v[148:151]
v_mfma_f32_16x16x32_bf16 v[148:151], a[132:135], a[44:47], v[148:151]
ds_write_b128 v253, v[8:11] offset:2048
v_mfma_f32_16x16x32_bf16 v[152:155], a[128:131], a[48:51], v[152:155]
ds_write_b128 v253, v[12:15] offset:3072
v_mfma_f32_16x16x32_bf16 v[152:155], a[132:135], a[52:55], v[152:155]
v_mfma_f32_16x16x32_bf16 v[156:159], a[128:131], a[56:59], v[156:159]
ds_write_b128 v253, v[16:19] offset:4096
v_mfma_f32_16x16x32_bf16 v[156:159], a[132:135], a[60:63], v[156:159]
ds_write_b128 v253, v[20:23] offset:5120
v_mfma_f32_16x16x32_bf16 v[176:179], a[136:139], a[32:35], v[176:179]
v_mfma_f32_16x16x32_bf16 v[176:179], a[140:143], a[36:39], v[176:179]
ds_write_b128 v253, v[24:27] offset:6144
v_mfma_f32_16x16x32_bf16 v[180:183], a[136:139], a[40:43], v[180:183]
ds_write_b128 v253, v[28:31] offset:7168
v_mfma_f32_16x16x32_bf16 v[180:183], a[140:143], a[44:47], v[180:183]
v_mfma_f32_16x16x32_bf16 v[184:187], a[136:139], a[48:51], v[184:187]
ds_write_b128 v253, v[32:35] offset:8192
v_mfma_f32_16x16x32_bf16 v[184:187], a[140:143], a[52:55], v[184:187]
ds_write_b128 v253, v[36:39] offset:9216
v_mfma_f32_16x16x32_bf16 v[188:191], a[136:139], a[56:59], v[188:191]
v_mfma_f32_16x16x32_bf16 v[188:191], a[140:143], a[60:63], v[188:191]
ds_write_b128 v253, v[40:43] offset:10240
v_mfma_f32_16x16x32_bf16 v[224:227], a[96:99], a[32:35], v[224:227]
ds_write_b128 v253, v[44:47] offset:11264
v_mfma_f32_16x16x32_bf16 v[224:227], a[100:103], a[36:39], v[224:227]
v_mfma_f32_16x16x32_bf16 v[232:235], a[96:99], a[40:43], v[232:235]
ds_write_b128 v253, v[48:51] offset:12288
v_mfma_f32_16x16x32_bf16 v[232:235], a[100:103], a[44:47], v[232:235]
ds_write_b128 v253, v[52:55] offset:13312
v_mfma_f32_16x16x32_bf16 a[160:163], a[96:99], a[48:51], a[160:163]
v_mfma_f32_16x16x32_bf16 a[160:163], a[100:103], a[52:55], a[160:163]
ds_write_b128 v253, v[56:59] offset:14336
v_mfma_f32_16x16x32_bf16 a[168:171], a[96:99], a[56:59], a[168:171]
ds_write_b128 v253, v[60:63] offset:15360
v_mfma_f32_16x16x32_bf16 a[168:171], a[100:103], a[60:63], a[168:171]
v_mfma_f32_16x16x32_bf16 v[228:231], a[104:107], a[32:35], v[228:231]
ds_write_b128 v253, v[64:67] offset:16384
v_mfma_f32_16x16x32_bf16 v[228:231], a[108:111], a[36:39], v[228:231]
ds_write_b128 v253, v[68:71] offset:17408
v_mfma_f32_16x16x32_bf16 v[236:239], a[104:107], a[40:43], v[236:239]
v_mfma_f32_16x16x32_bf16 v[236:239], a[108:111], a[44:47], v[236:239]
ds_write_b128 v253, v[72:75] offset:18432
v_mfma_f32_16x16x32_bf16 a[164:167], a[104:107], a[48:51], a[164:167]
ds_write_b128 v253, v[76:79] offset:19456
v_mfma_f32_16x16x32_bf16 a[164:167], a[108:111], a[52:55], a[164:167]
v_mfma_f32_16x16x32_bf16 a[172:175], a[104:107], a[56:59], a[172:175]
ds_write_b128 v253, v[80:83] offset:20480
v_mfma_f32_16x16x32_bf16 a[172:175], a[108:111], a[60:63], a[172:175]
ds_write_b128 v253, v[84:87] offset:21504
v_mfma_f32_16x16x32_bf16 a[208:211], a[112:115], a[32:35], a[208:211]
v_mfma_f32_16x16x32_bf16 a[208:211], a[116:119], a[36:39], a[208:211]
ds_write_b128 v253, v[88:91] offset:22528
v_mfma_f32_16x16x32_bf16 a[216:219], a[112:115], a[40:43], a[216:219]
ds_write_b128 v253, v[92:95] offset:23552
v_mfma_f32_16x16x32_bf16 a[216:219], a[116:119], a[44:47], a[216:219]
v_mfma_f32_16x16x32_bf16 a[224:227], a[112:115], a[48:51], a[224:227]
ds_write_b128 v253, v[96:99] offset:24576
v_mfma_f32_16x16x32_bf16 a[224:227], a[116:119], a[52:55], a[224:227]
ds_write_b128 v253, v[100:103] offset:25600
v_mfma_f32_16x16x32_bf16 a[232:235], a[112:115], a[56:59], a[232:235]
v_mfma_f32_16x16x32_bf16 a[232:235], a[116:119], a[60:63], a[232:235]
ds_write_b128 v253, v[104:107] offset:26624
v_mfma_f32_16x16x32_bf16 a[212:215], a[120:123], a[32:35], a[212:215]
ds_write_b128 v253, v[108:111] offset:27648
v_mfma_f32_16x16x32_bf16 a[212:215], a[124:127], a[36:39], a[212:215]
v_mfma_f32_16x16x32_bf16 a[220:223], a[120:123], a[40:43], a[220:223]
ds_write_b128 v253, v[112:115] offset:28672
v_mfma_f32_16x16x32_bf16 a[220:223], a[124:127], a[44:47], a[220:223]
ds_write_b128 v253, v[116:119] offset:29696
v_mfma_f32_16x16x32_bf16 a[228:231], a[120:123], a[48:51], a[228:231]
v_mfma_f32_16x16x32_bf16 a[228:231], a[124:127], a[52:55], a[228:231]
ds_write_b128 v253, v[120:123] offset:30720
v_mfma_f32_16x16x32_bf16 a[236:239], a[120:123], a[56:59], a[236:239]
v_mfma_f32_16x16x32_bf16 a[236:239], a[124:127], a[60:63], a[236:239]
ds_write_b128 v253, v[124:127] offset:31744
s_branch mla_fused_a_t15E
mla_fused_a_t15B:
v_mfma_f32_16x16x32_bf16 v[144:147], a[128:131], a[32:35], v[144:147]
v_mfma_f32_16x16x32_bf16 v[144:147], a[132:135], a[36:39], v[144:147]
v_mfma_f32_16x16x32_bf16 v[148:151], a[128:131], a[40:43], v[148:151]
v_mfma_f32_16x16x32_bf16 v[148:151], a[132:135], a[44:47], v[148:151]
v_mfma_f32_16x16x32_bf16 v[152:155], a[128:131], a[48:51], v[152:155]
v_mfma_f32_16x16x32_bf16 v[152:155], a[132:135], a[52:55], v[152:155]
v_mfma_f32_16x16x32_bf16 v[156:159], a[128:131], a[56:59], v[156:159]
v_mfma_f32_16x16x32_bf16 v[156:159], a[132:135], a[60:63], v[156:159]
v_mfma_f32_16x16x32_bf16 v[176:179], a[136:139], a[32:35], v[176:179]
v_mfma_f32_16x16x32_bf16 v[176:179], a[140:143], a[36:39], v[176:179]
v_mfma_f32_16x16x32_bf16 v[180:183], a[136:139], a[40:43], v[180:183]
v_mfma_f32_16x16x32_bf16 v[180:183], a[140:143], a[44:47], v[180:183]
v_mfma_f32_16x16x32_bf16 v[184:187], a[136:139], a[48:51], v[184:187]
v_mfma_f32_16x16x32_bf16 v[184:187], a[140:143], a[52:55], v[184:187]
v_mfma_f32_16x16x32_bf16 v[188:191], a[136:139], a[56:59], v[188:191]
v_mfma_f32_16x16x32_bf16 v[188:191], a[140:143], a[60:63], v[188:191]
v_mfma_f32_16x16x32_bf16 v[224:227], a[96:99], a[32:35], v[224:227]
v_mfma_f32_16x16x32_bf16 v[224:227], a[100:103], a[36:39], v[224:227]
v_mfma_f32_16x16x32_bf16 v[232:235], a[96:99], a[40:43], v[232:235]
v_mfma_f32_16x16x32_bf16 v[232:235], a[100:103], a[44:47], v[232:235]
v_mfma_f32_16x16x32_bf16 a[160:163], a[96:99], a[48:51], a[160:163]
v_mfma_f32_16x16x32_bf16 a[160:163], a[100:103], a[52:55], a[160:163]
v_mfma_f32_16x16x32_bf16 a[168:171], a[96:99], a[56:59], a[168:171]
v_mfma_f32_16x16x32_bf16 a[168:171], a[100:103], a[60:63], a[168:171]
v_mfma_f32_16x16x32_bf16 v[228:231], a[104:107], a[32:35], v[228:231]
v_mfma_f32_16x16x32_bf16 v[228:231], a[108:111], a[36:39], v[228:231]
v_mfma_f32_16x16x32_bf16 v[236:239], a[104:107], a[40:43], v[236:239]
v_mfma_f32_16x16x32_bf16 v[236:239], a[108:111], a[44:47], v[236:239]
v_mfma_f32_16x16x32_bf16 a[164:167], a[104:107], a[48:51], a[164:167]
v_mfma_f32_16x16x32_bf16 a[164:167], a[108:111], a[52:55], a[164:167]
v_mfma_f32_16x16x32_bf16 a[172:175], a[104:107], a[56:59], a[172:175]
v_mfma_f32_16x16x32_bf16 a[172:175], a[108:111], a[60:63], a[172:175]
v_mfma_f32_16x16x32_bf16 a[208:211], a[112:115], a[32:35], a[208:211]
v_mfma_f32_16x16x32_bf16 a[208:211], a[116:119], a[36:39], a[208:211]
v_mfma_f32_16x16x32_bf16 a[216:219], a[112:115], a[40:43], a[216:219]
v_mfma_f32_16x16x32_bf16 a[216:219], a[116:119], a[44:47], a[216:219]
v_mfma_f32_16x16x32_bf16 a[224:227], a[112:115], a[48:51], a[224:227]
v_mfma_f32_16x16x32_bf16 a[224:227], a[116:119], a[52:55], a[224:227]
v_mfma_f32_16x16x32_bf16 a[232:235], a[112:115], a[56:59], a[232:235]
v_mfma_f32_16x16x32_bf16 a[232:235], a[116:119], a[60:63], a[232:235]
v_mfma_f32_16x16x32_bf16 a[212:215], a[120:123], a[32:35], a[212:215]
v_mfma_f32_16x16x32_bf16 a[212:215], a[124:127], a[36:39], a[212:215]
v_mfma_f32_16x16x32_bf16 a[220:223], a[120:123], a[40:43], a[220:223]
v_mfma_f32_16x16x32_bf16 a[220:223], a[124:127], a[44:47], a[220:223]
v_mfma_f32_16x16x32_bf16 a[228:231], a[120:123], a[48:51], a[228:231]
v_mfma_f32_16x16x32_bf16 a[228:231], a[124:127], a[52:55], a[228:231]
v_mfma_f32_16x16x32_bf16 a[236:239], a[120:123], a[56:59], a[236:239]
v_mfma_f32_16x16x32_bf16 a[236:239], a[124:127], a[60:63], a[236:239]
mla_fused_a_t15E:
mla_fused_a_tile2end:
s_mov_b32 s86, 0x3e0293ee
s_mov_b32 s87, 0x3e0293ee
v_pk_mul_f32 v[192:193], v[192:193], s[86:87]
v_pk_mul_f32 v[194:195], v[194:195], s[86:87]
v_pk_mul_f32 v[196:197], v[196:197], s[86:87]
v_pk_mul_f32 v[198:199], v[198:199], s[86:87]
v_cvt_pk_bf16_f32 v0, v192, v193
v_cvt_pk_bf16_f32 v1, v194, v195
v_cvt_pk_bf16_f32 v2, v196, v197
v_cvt_pk_bf16_f32 v3, v198, v199
v_pk_mul_f32 v[200:201], v[200:201], s[86:87]
v_pk_mul_f32 v[202:203], v[202:203], s[86:87]
v_pk_mul_f32 v[204:205], v[204:205], s[86:87]
v_pk_mul_f32 v[206:207], v[206:207], s[86:87]
v_cvt_pk_bf16_f32 v4, v200, v201
v_cvt_pk_bf16_f32 v5, v202, v203
v_cvt_pk_bf16_f32 v6, v204, v205
v_cvt_pk_bf16_f32 v7, v206, v207
v_pk_mul_f32 v[208:209], v[208:209], s[86:87]
v_pk_mul_f32 v[210:211], v[210:211], s[86:87]
v_pk_mul_f32 v[212:213], v[212:213], s[86:87]
v_pk_mul_f32 v[214:215], v[214:215], s[86:87]
v_cvt_pk_bf16_f32 v8, v208, v209
v_cvt_pk_bf16_f32 v9, v210, v211
v_cvt_pk_bf16_f32 v10, v212, v213
v_cvt_pk_bf16_f32 v11, v214, v215
v_pk_mul_f32 v[216:217], v[216:217], s[86:87]
v_pk_mul_f32 v[218:219], v[218:219], s[86:87]
v_pk_mul_f32 v[220:221], v[220:221], s[86:87]
v_pk_mul_f32 v[222:223], v[222:223], s[86:87]
v_cvt_pk_bf16_f32 v12, v216, v217
v_cvt_pk_bf16_f32 v13, v218, v219
v_cvt_pk_bf16_f32 v14, v220, v221
v_cvt_pk_bf16_f32 v15, v222, v223
v_pk_mul_f32 v[224:225], v[224:225], s[86:87]
v_pk_mul_f32 v[226:227], v[226:227], s[86:87]
v_pk_mul_f32 v[228:229], v[228:229], s[86:87]
v_pk_mul_f32 v[230:231], v[230:231], s[86:87]
v_cvt_pk_bf16_f32 v16, v224, v225
v_cvt_pk_bf16_f32 v17, v226, v227
v_cvt_pk_bf16_f32 v18, v228, v229
v_cvt_pk_bf16_f32 v19, v230, v231
v_pk_mul_f32 v[232:233], v[232:233], s[86:87]
v_pk_mul_f32 v[234:235], v[234:235], s[86:87]
v_pk_mul_f32 v[236:237], v[236:237], s[86:87]
v_pk_mul_f32 v[238:239], v[238:239], s[86:87]
v_cvt_pk_bf16_f32 v20, v232, v233
v_cvt_pk_bf16_f32 v21, v234, v235
v_cvt_pk_bf16_f32 v22, v236, v237
)ASM"
R"ASM(v_cvt_pk_bf16_f32 v23, v238, v239
v_accvgpr_read_b32 v64, a160
v_accvgpr_read_b32 v65, a161
v_accvgpr_read_b32 v66, a162
v_accvgpr_read_b32 v67, a163
v_accvgpr_read_b32 v68, a164
v_accvgpr_read_b32 v69, a165
v_accvgpr_read_b32 v70, a166
v_accvgpr_read_b32 v71, a167
v_pk_mul_f32 v[64:65], v[64:65], s[86:87]
v_pk_mul_f32 v[66:67], v[66:67], s[86:87]
v_pk_mul_f32 v[68:69], v[68:69], s[86:87]
v_pk_mul_f32 v[70:71], v[70:71], s[86:87]
v_cvt_pk_bf16_f32 v24, v64, v65
v_cvt_pk_bf16_f32 v25, v66, v67
v_cvt_pk_bf16_f32 v26, v68, v69
v_cvt_pk_bf16_f32 v27, v70, v71
v_accvgpr_read_b32 v72, a168
v_accvgpr_read_b32 v73, a169
v_accvgpr_read_b32 v74, a170
v_accvgpr_read_b32 v75, a171
v_accvgpr_read_b32 v76, a172
v_accvgpr_read_b32 v77, a173
v_accvgpr_read_b32 v78, a174
v_accvgpr_read_b32 v79, a175
v_pk_mul_f32 v[72:73], v[72:73], s[86:87]
v_pk_mul_f32 v[74:75], v[74:75], s[86:87]
v_pk_mul_f32 v[76:77], v[76:77], s[86:87]
v_pk_mul_f32 v[78:79], v[78:79], s[86:87]
v_cvt_pk_bf16_f32 v28, v72, v73
v_cvt_pk_bf16_f32 v29, v74, v75
v_cvt_pk_bf16_f32 v30, v76, v77
v_cvt_pk_bf16_f32 v31, v78, v79
v_accvgpr_read_b32 v80, a176
v_accvgpr_read_b32 v81, a177
v_accvgpr_read_b32 v82, a178
v_accvgpr_read_b32 v83, a179
v_accvgpr_read_b32 v84, a180
v_accvgpr_read_b32 v85, a181
v_accvgpr_read_b32 v86, a182
v_accvgpr_read_b32 v87, a183
v_pk_mul_f32 v[80:81], v[80:81], s[86:87]
v_pk_mul_f32 v[82:83], v[82:83], s[86:87]
v_pk_mul_f32 v[84:85], v[84:85], s[86:87]
v_pk_mul_f32 v[86:87], v[86:87], s[86:87]
v_cvt_pk_bf16_f32 v32, v80, v81
v_cvt_pk_bf16_f32 v33, v82, v83
v_cvt_pk_bf16_f32 v34, v84, v85
v_cvt_pk_bf16_f32 v35, v86, v87
v_accvgpr_read_b32 v88, a184
v_accvgpr_read_b32 v89, a185
v_accvgpr_read_b32 v90, a186
v_accvgpr_read_b32 v91, a187
v_accvgpr_read_b32 v92, a188
v_accvgpr_read_b32 v93, a189
v_accvgpr_read_b32 v94, a190
v_accvgpr_read_b32 v95, a191
v_pk_mul_f32 v[88:89], v[88:89], s[86:87]
v_pk_mul_f32 v[90:91], v[90:91], s[86:87]
v_pk_mul_f32 v[92:93], v[92:93], s[86:87]
v_pk_mul_f32 v[94:95], v[94:95], s[86:87]
v_cvt_pk_bf16_f32 v36, v88, v89
v_cvt_pk_bf16_f32 v37, v90, v91
v_cvt_pk_bf16_f32 v38, v92, v93
v_cvt_pk_bf16_f32 v39, v94, v95
v_accvgpr_read_b32 v96, a192
v_accvgpr_read_b32 v97, a193
v_accvgpr_read_b32 v98, a194
v_accvgpr_read_b32 v99, a195
v_accvgpr_read_b32 v100, a196
v_accvgpr_read_b32 v101, a197
v_accvgpr_read_b32 v102, a198
v_accvgpr_read_b32 v103, a199
v_pk_mul_f32 v[96:97], v[96:97], s[86:87]
v_pk_mul_f32 v[98:99], v[98:99], s[86:87]
v_pk_mul_f32 v[100:101], v[100:101], s[86:87]
v_pk_mul_f32 v[102:103], v[102:103], s[86:87]
v_cvt_pk_bf16_f32 v40, v96, v97
v_cvt_pk_bf16_f32 v41, v98, v99
v_cvt_pk_bf16_f32 v42, v100, v101
v_cvt_pk_bf16_f32 v43, v102, v103
v_accvgpr_read_b32 v104, a200
v_accvgpr_read_b32 v105, a201
v_accvgpr_read_b32 v106, a202
v_accvgpr_read_b32 v107, a203
v_accvgpr_read_b32 v108, a204
v_accvgpr_read_b32 v109, a205
v_accvgpr_read_b32 v110, a206
v_accvgpr_read_b32 v111, a207
v_pk_mul_f32 v[104:105], v[104:105], s[86:87]
v_pk_mul_f32 v[106:107], v[106:107], s[86:87]
v_pk_mul_f32 v[108:109], v[108:109], s[86:87]
v_pk_mul_f32 v[110:111], v[110:111], s[86:87]
v_cvt_pk_bf16_f32 v44, v104, v105
v_cvt_pk_bf16_f32 v45, v106, v107
v_cvt_pk_bf16_f32 v46, v108, v109
v_cvt_pk_bf16_f32 v47, v110, v111
v_accvgpr_read_b32 v112, a208
v_accvgpr_read_b32 v113, a209
v_accvgpr_read_b32 v114, a210
v_accvgpr_read_b32 v115, a211
v_accvgpr_read_b32 v116, a212
v_accvgpr_read_b32 v117, a213
v_accvgpr_read_b32 v118, a214
v_accvgpr_read_b32 v119, a215
v_pk_mul_f32 v[112:113], v[112:113], s[86:87]
v_pk_mul_f32 v[114:115], v[114:115], s[86:87]
v_pk_mul_f32 v[116:117], v[116:117], s[86:87]
v_pk_mul_f32 v[118:119], v[118:119], s[86:87]
v_cvt_pk_bf16_f32 v48, v112, v113
v_cvt_pk_bf16_f32 v49, v114, v115
v_cvt_pk_bf16_f32 v50, v116, v117
v_cvt_pk_bf16_f32 v51, v118, v119
v_accvgpr_read_b32 v64, a216
v_accvgpr_read_b32 v65, a217
v_accvgpr_read_b32 v66, a218
v_accvgpr_read_b32 v67, a219
v_accvgpr_read_b32 v68, a220
v_accvgpr_read_b32 v69, a221
v_accvgpr_read_b32 v70, a222
v_accvgpr_read_b32 v71, a223
v_pk_mul_f32 v[64:65], v[64:65], s[86:87]
v_pk_mul_f32 v[66:67], v[66:67], s[86:87]
v_pk_mul_f32 v[68:69], v[68:69], s[86:87]
v_pk_mul_f32 v[70:71], v[70:71], s[86:87]
v_cvt_pk_bf16_f32 v52, v64, v65
v_cvt_pk_bf16_f32 v53, v66, v67
v_cvt_pk_bf16_f32 v54, v68, v69
v_cvt_pk_bf16_f32 v55, v70, v71
v_accvgpr_read_b32 v72, a224
v_accvgpr_read_b32 v73, a225
v_accvgpr_read_b32 v74, a226
v_accvgpr_read_b32 v75, a227
v_accvgpr_read_b32 v76, a228
v_accvgpr_read_b32 v77, a229
v_accvgpr_read_b32 v78, a230
v_accvgpr_read_b32 v79, a231
v_pk_mul_f32 v[72:73], v[72:73], s[86:87]
v_pk_mul_f32 v[74:75], v[74:75], s[86:87]
v_pk_mul_f32 v[76:77], v[76:77], s[86:87]
v_pk_mul_f32 v[78:79], v[78:79], s[86:87]
v_cvt_pk_bf16_f32 v56, v72, v73
v_cvt_pk_bf16_f32 v57, v74, v75
v_cvt_pk_bf16_f32 v58, v76, v77
v_cvt_pk_bf16_f32 v59, v78, v79
v_accvgpr_read_b32 v80, a232
v_accvgpr_read_b32 v81, a233
v_accvgpr_read_b32 v82, a234
v_accvgpr_read_b32 v83, a235
v_accvgpr_read_b32 v84, a236
v_accvgpr_read_b32 v85, a237
v_accvgpr_read_b32 v86, a238
v_accvgpr_read_b32 v87, a239
v_pk_mul_f32 v[80:81], v[80:81], s[86:87]
v_pk_mul_f32 v[82:83], v[82:83], s[86:87]
v_pk_mul_f32 v[84:85], v[84:85], s[86:87]
v_pk_mul_f32 v[86:87], v[86:87], s[86:87]
v_cvt_pk_bf16_f32 v60, v80, v81
v_cvt_pk_bf16_f32 v61, v82, v83
v_cvt_pk_bf16_f32 v62, v84, v85
v_cvt_pk_bf16_f32 v63, v86, v87
s_lshl_b32 s82, s70, 11
s_add_u32 s82, s82, 98304
s_lshl_b32 s83, s69, 15
s_add_u32 s82, s82, s83
v_add_u32 v254, s82, v252
ds_write_b128 v254, v[0:3] offset:0
ds_write_b128 v254, v[4:7] offset:4096
ds_write_b128 v254, v[8:11] offset:8192
ds_write_b128 v254, v[12:15] offset:12288
ds_write_b128 v254, v[16:19] offset:16384
ds_write_b128 v254, v[20:23] offset:20480
ds_write_b128 v254, v[24:27] offset:24576
ds_write_b128 v254, v[28:31] offset:28672
ds_write_b128 v254, v[32:35] offset:1024
ds_write_b128 v254, v[36:39] offset:5120
ds_write_b128 v254, v[40:43] offset:9216
ds_write_b128 v254, v[44:47] offset:13312
ds_write_b128 v254, v[48:51] offset:17408
ds_write_b128 v254, v[52:55] offset:21504
ds_write_b128 v254, v[56:59] offset:25600
ds_write_b128 v254, v[60:63] offset:29696
mla_fused_f_qdone:
s_mul_i32 s83, s88, 184
s_lshl_b32 s84, s71, 1
s_add_u32 s83, s83, s84
s_add_u32 s83, s83, s70
s_lshl_b32 s83, s83, 13
s_add_u32 s83, s83, 0x3000000
s_cmp_ge_u32 s71, 28
s_cselect_b32 s76, 0x7ff00000, s83
s_and_b32 s82, s89, 7
s_and_b32 s83, s82, 1
s_lshl_b32 s83, s83, 3
s_lshr_b32 s84, s89, 5
s_add_u32 s83, s83, s84
s_sub_u32 s84, 15, s83
s_cmp_lt_u32 s83, 8
s_cselect_b32 s83, s83, s84
s_lshl_b32 s83, s83, 5
s_lshr_b32 s82, s82, 1
s_mul_i32 s82, s82, 7
s_add_u32 s83, s83, s82
s_lshl_b32 s83, s83, 3
s_add_u32 s77, s83, 0x5703000
s_waitcnt vmcnt(0) lgkmcnt(0)
s_barrier
s_lshl_b32 s84, s71, 3
s_add_u32 s84, s84, s72
s_lshl_b32 s84, s84, 3
s_add_u32 s84, s84, 0x5702100
s_cmp_lg_u32 s68, 0
s_cbranch_scc1 mla_fused_a_pub1
v_and_b32 v248, 63, %[tid]
v_cmp_eq_u32 vcc, 0, v248
v_mov_b32 v246, 0x7ffffff0
v_mov_b32 v247, 0
v_cndmask_b32 v248, v246, v247, vcc
v_mov_b32 v246, s98
v_mov_b32 v247, s99
buffer_store_dwordx2 v[246:247], v248, s[60:63], s84 offen sc1
mla_fused_a_pub1:
s_lshl_b32 s82, s70, 14
s_add_u32 s82, s82, 98304
s_lshl_b32 s83, s69, 13
s_add_u32 s84, s82, s83
v_add_u32 v66, s84, v252
s_xor_b32 s83, s83, 8192
s_add_u32 s84, s82, s83
v_add_u32 v67, s84, v252
ds_read_b128 a[128:131], v66 offset:0
ds_read_b128 a[132:135], v66 offset:1024
ds_read_b128 a[136:139], v66 offset:2048
ds_read_b128 a[140:143], v66 offset:3072
ds_read_b128 a[144:147], v66 offset:4096
ds_read_b128 a[148:151], v66 offset:5120
ds_read_b128 a[152:155], v66 offset:6144
ds_read_b128 a[156:159], v66 offset:7168
ds_read_b128 a[160:163], v67 offset:0
ds_read_b128 a[164:167], v67 offset:1024
ds_read_b128 a[168:171], v67 offset:2048
ds_read_b128 a[172:175], v67 offset:3072
ds_read_b128 a[176:179], v67 offset:4096
ds_read_b128 a[180:183], v67 offset:5120
ds_read_b128 a[184:187], v67 offset:6144
ds_read_b128 a[188:191], v67 offset:7168
s_mov_b32 s86, 0x3fb8aa3b
s_mov_b32 s87, 0x3fb8aa3b
v_mov_b32 v120, 2.0
v_mov_b32 v121, 2.0
v_pk_mul_f32 v[72:73], v[128:129], s[86:87]
v_pk_mul_f32 v[74:75], v[130:131], s[86:87]
v_pk_mul_f32 v[76:77], v[160:161], s[86:87]
v_pk_mul_f32 v[78:79], v[162:163], s[86:87]
v_min_f32 v72, 0x4266d4ca, v72
v_min_f32 v73, 0x4266d4ca, v73
v_min_f32 v74, 0x4266d4ca, v74
v_min_f32 v75, 0x4266d4ca, v75
v_min_f32 v76, 0x4266d4ca, v76
v_min_f32 v77, 0x4266d4ca, v77
v_min_f32 v78, 0x4266d4ca, v78
v_min_f32 v79, 0x4266d4ca, v79
v_exp_f32 v72, v72
v_exp_f32 v73, v73
v_exp_f32 v74, v74
v_exp_f32 v75, v75
v_exp_f32 v76, v76
v_exp_f32 v77, v77
v_exp_f32 v78, v78
v_exp_f32 v79, v79
v_pk_add_f32 v[80:81], v[72:73], v[120:121]
v_pk_add_f32 v[82:83], v[74:75], v[120:121]
v_pk_add_f32 v[84:85], v[76:77], v[120:121]
v_pk_add_f32 v[86:87], v[78:79], v[120:121]
v_pk_mul_f32 v[80:81], v[80:81], v[72:73]
v_pk_mul_f32 v[82:83], v[82:83], v[74:75]
v_pk_mul_f32 v[84:85], v[84:85], v[76:77]
v_pk_mul_f32 v[86:87], v[86:87], v[78:79]
v_pk_add_f32 v[88:89], v[80:81], v[120:121]
v_pk_add_f32 v[90:91], v[82:83], v[120:121]
v_pk_add_f32 v[92:93], v[84:85], v[120:121]
v_pk_add_f32 v[94:95], v[86:87], v[120:121]
v_rcp_f32 v88, v88
v_rcp_f32 v89, v89
v_rcp_f32 v90, v90
v_rcp_f32 v91, v91
v_rcp_f32 v92, v92
v_rcp_f32 v93, v93
v_rcp_f32 v94, v94
v_rcp_f32 v95, v95
v_pk_mul_f32 v[80:81], v[80:81], v[88:89]
v_pk_mul_f32 v[82:83], v[82:83], v[90:91]
v_pk_mul_f32 v[84:85], v[84:85], v[92:93]
v_pk_mul_f32 v[86:87], v[86:87], v[94:95]
v_pk_mul_f32 v[128:129], v[128:129], v[80:81]
v_pk_mul_f32 v[130:131], v[130:131], v[82:83]
v_pk_mul_f32 v[160:161], v[160:161], v[84:85]
v_pk_mul_f32 v[162:163], v[162:163], v[86:87]
v_cvt_pk_bf16_f32 v128, v128, v129
v_cvt_pk_bf16_f32 v129, v130, v131
v_cvt_pk_bf16_f32 v130, v160, v161
v_cvt_pk_bf16_f32 v131, v162, v163
buffer_store_dwordx4 v[128:131], v252, s[60:63], s76 offen offset:0 sc1
v_pk_mul_f32 v[72:73], v[132:133], s[86:87]
v_pk_mul_f32 v[74:75], v[134:135], s[86:87]
v_pk_mul_f32 v[76:77], v[164:165], s[86:87]
v_pk_mul_f32 v[78:79], v[166:167], s[86:87]
v_min_f32 v72, 0x4266d4ca, v72
v_min_f32 v73, 0x4266d4ca, v73
v_min_f32 v74, 0x4266d4ca, v74
v_min_f32 v75, 0x4266d4ca, v75
v_min_f32 v76, 0x4266d4ca, v76
v_min_f32 v77, 0x4266d4ca, v77
v_min_f32 v78, 0x4266d4ca, v78
v_min_f32 v79, 0x4266d4ca, v79
v_exp_f32 v72, v72
v_exp_f32 v73, v73
v_exp_f32 v74, v74
v_exp_f32 v75, v75
v_exp_f32 v76, v76
v_exp_f32 v77, v77
v_exp_f32 v78, v78
v_exp_f32 v79, v79
v_pk_add_f32 v[80:81], v[72:73], v[120:121]
v_pk_add_f32 v[82:83], v[74:75], v[120:121]
v_pk_add_f32 v[84:85], v[76:77], v[120:121]
v_pk_add_f32 v[86:87], v[78:79], v[120:121]
v_pk_mul_f32 v[80:81], v[80:81], v[72:73]
v_pk_mul_f32 v[82:83], v[82:83], v[74:75]
v_pk_mul_f32 v[84:85], v[84:85], v[76:77]
v_pk_mul_f32 v[86:87], v[86:87], v[78:79]
v_pk_add_f32 v[88:89], v[80:81], v[120:121]
v_pk_add_f32 v[90:91], v[82:83], v[120:121]
v_pk_add_f32 v[92:93], v[84:85], v[120:121]
v_pk_add_f32 v[94:95], v[86:87], v[120:121]
v_rcp_f32 v88, v88
v_rcp_f32 v89, v89
v_rcp_f32 v90, v90
v_rcp_f32 v91, v91
v_rcp_f32 v92, v92
v_rcp_f32 v93, v93
v_rcp_f32 v94, v94
v_rcp_f32 v95, v95
v_pk_mul_f32 v[80:81], v[80:81], v[88:89]
v_pk_mul_f32 v[82:83], v[82:83], v[90:91]
v_pk_mul_f32 v[84:85], v[84:85], v[92:93]
v_pk_mul_f32 v[86:87], v[86:87], v[94:95]
v_pk_mul_f32 v[132:133], v[132:133], v[80:81]
v_pk_mul_f32 v[134:135], v[134:135], v[82:83]
v_pk_mul_f32 v[164:165], v[164:165], v[84:85]
v_pk_mul_f32 v[166:167], v[166:167], v[86:87]
v_cvt_pk_bf16_f32 v132, v132, v133
v_cvt_pk_bf16_f32 v133, v134, v135
v_cvt_pk_bf16_f32 v134, v164, v165
v_cvt_pk_bf16_f32 v135, v166, v167
buffer_store_dwordx4 v[132:135], v252, s[60:63], s76 offen offset:1024 sc1
v_pk_mul_f32 v[72:73], v[136:137], s[86:87]
v_pk_mul_f32 v[74:75], v[138:139], s[86:87]
v_pk_mul_f32 v[76:77], v[168:169], s[86:87]
v_pk_mul_f32 v[78:79], v[170:171], s[86:87]
v_min_f32 v72, 0x4266d4ca, v72
v_min_f32 v73, 0x4266d4ca, v73
v_min_f32 v74, 0x4266d4ca, v74
)ASM"
R"ASM(v_min_f32 v75, 0x4266d4ca, v75
v_min_f32 v76, 0x4266d4ca, v76
v_min_f32 v77, 0x4266d4ca, v77
v_min_f32 v78, 0x4266d4ca, v78
v_min_f32 v79, 0x4266d4ca, v79
v_exp_f32 v72, v72
v_exp_f32 v73, v73
v_exp_f32 v74, v74
v_exp_f32 v75, v75
v_exp_f32 v76, v76
v_exp_f32 v77, v77
v_exp_f32 v78, v78
v_exp_f32 v79, v79
v_pk_add_f32 v[80:81], v[72:73], v[120:121]
v_pk_add_f32 v[82:83], v[74:75], v[120:121]
v_pk_add_f32 v[84:85], v[76:77], v[120:121]
v_pk_add_f32 v[86:87], v[78:79], v[120:121]
v_pk_mul_f32 v[80:81], v[80:81], v[72:73]
v_pk_mul_f32 v[82:83], v[82:83], v[74:75]
v_pk_mul_f32 v[84:85], v[84:85], v[76:77]
v_pk_mul_f32 v[86:87], v[86:87], v[78:79]
v_pk_add_f32 v[88:89], v[80:81], v[120:121]
v_pk_add_f32 v[90:91], v[82:83], v[120:121]
v_pk_add_f32 v[92:93], v[84:85], v[120:121]
v_pk_add_f32 v[94:95], v[86:87], v[120:121]
v_rcp_f32 v88, v88
v_rcp_f32 v89, v89
v_rcp_f32 v90, v90
v_rcp_f32 v91, v91
v_rcp_f32 v92, v92
v_rcp_f32 v93, v93
v_rcp_f32 v94, v94
v_rcp_f32 v95, v95
v_pk_mul_f32 v[80:81], v[80:81], v[88:89]
v_pk_mul_f32 v[82:83], v[82:83], v[90:91]
v_pk_mul_f32 v[84:85], v[84:85], v[92:93]
v_pk_mul_f32 v[86:87], v[86:87], v[94:95]
v_pk_mul_f32 v[136:137], v[136:137], v[80:81]
v_pk_mul_f32 v[138:139], v[138:139], v[82:83]
v_pk_mul_f32 v[168:169], v[168:169], v[84:85]
v_pk_mul_f32 v[170:171], v[170:171], v[86:87]
v_cvt_pk_bf16_f32 v136, v136, v137
v_cvt_pk_bf16_f32 v137, v138, v139
v_cvt_pk_bf16_f32 v138, v168, v169
v_cvt_pk_bf16_f32 v139, v170, v171
buffer_store_dwordx4 v[136:139], v252, s[60:63], s76 offen offset:2048 sc1
v_pk_mul_f32 v[72:73], v[140:141], s[86:87]
v_pk_mul_f32 v[74:75], v[142:143], s[86:87]
v_pk_mul_f32 v[76:77], v[172:173], s[86:87]
v_pk_mul_f32 v[78:79], v[174:175], s[86:87]
v_min_f32 v72, 0x4266d4ca, v72
v_min_f32 v73, 0x4266d4ca, v73
v_min_f32 v74, 0x4266d4ca, v74
v_min_f32 v75, 0x4266d4ca, v75
v_min_f32 v76, 0x4266d4ca, v76
v_min_f32 v77, 0x4266d4ca, v77
v_min_f32 v78, 0x4266d4ca, v78
v_min_f32 v79, 0x4266d4ca, v79
v_exp_f32 v72, v72
v_exp_f32 v73, v73
v_exp_f32 v74, v74
v_exp_f32 v75, v75
v_exp_f32 v76, v76
v_exp_f32 v77, v77
v_exp_f32 v78, v78
v_exp_f32 v79, v79
v_pk_add_f32 v[80:81], v[72:73], v[120:121]
v_pk_add_f32 v[82:83], v[74:75], v[120:121]
v_pk_add_f32 v[84:85], v[76:77], v[120:121]
v_pk_add_f32 v[86:87], v[78:79], v[120:121]
v_pk_mul_f32 v[80:81], v[80:81], v[72:73]
v_pk_mul_f32 v[82:83], v[82:83], v[74:75]
v_pk_mul_f32 v[84:85], v[84:85], v[76:77]
v_pk_mul_f32 v[86:87], v[86:87], v[78:79]
v_pk_add_f32 v[88:89], v[80:81], v[120:121]
v_pk_add_f32 v[90:91], v[82:83], v[120:121]
v_pk_add_f32 v[92:93], v[84:85], v[120:121]
v_pk_add_f32 v[94:95], v[86:87], v[120:121]
v_rcp_f32 v88, v88
v_rcp_f32 v89, v89
v_rcp_f32 v90, v90
v_rcp_f32 v91, v91
v_rcp_f32 v92, v92
v_rcp_f32 v93, v93
v_rcp_f32 v94, v94
v_rcp_f32 v95, v95
v_pk_mul_f32 v[80:81], v[80:81], v[88:89]
v_pk_mul_f32 v[82:83], v[82:83], v[90:91]
v_pk_mul_f32 v[84:85], v[84:85], v[92:93]
v_pk_mul_f32 v[86:87], v[86:87], v[94:95]
v_pk_mul_f32 v[140:141], v[140:141], v[80:81]
v_pk_mul_f32 v[142:143], v[142:143], v[82:83]
v_pk_mul_f32 v[172:173], v[172:173], v[84:85]
v_pk_mul_f32 v[174:175], v[174:175], v[86:87]
v_cvt_pk_bf16_f32 v140, v140, v141
v_cvt_pk_bf16_f32 v141, v142, v143
v_cvt_pk_bf16_f32 v142, v172, v173
v_cvt_pk_bf16_f32 v143, v174, v175
buffer_store_dwordx4 v[140:143], v252, s[60:63], s76 offen offset:3072 sc1
s_add_u32 s76, s76, 4096
s_lshl_b32 s85, s71, 6
s_add_u32 s85, s85, 0x5702100
s_cmp_lg_u32 s68, 0
s_cbranch_scc1 mla_fused_f_pi2
v_and_b32 v248, 63, %[tid]
v_cmp_gt_u32 vcc, 8, v248
v_lshlrev_b32 v248, 3, v248
s_nop 2
s_mov_b64 s[92:93], vcc
v_mov_b32 v246, 0x7ffffff0
v_cndmask_b32 v248, v246, v248, vcc
buffer_load_dwordx2 v[246:247], v248, s[60:63], s85 offen sc1
mla_fused_f_pi2:
v_pk_mul_f32 v[72:73], v[144:145], s[86:87]
v_pk_mul_f32 v[74:75], v[146:147], s[86:87]
v_pk_mul_f32 v[76:77], v[176:177], s[86:87]
v_pk_mul_f32 v[78:79], v[178:179], s[86:87]
v_min_f32 v72, 0x4266d4ca, v72
v_min_f32 v73, 0x4266d4ca, v73
v_min_f32 v74, 0x4266d4ca, v74
v_min_f32 v75, 0x4266d4ca, v75
v_min_f32 v76, 0x4266d4ca, v76
v_min_f32 v77, 0x4266d4ca, v77
v_min_f32 v78, 0x4266d4ca, v78
v_min_f32 v79, 0x4266d4ca, v79
v_exp_f32 v72, v72
v_exp_f32 v73, v73
v_exp_f32 v74, v74
v_exp_f32 v75, v75
v_exp_f32 v76, v76
v_exp_f32 v77, v77
v_exp_f32 v78, v78
v_exp_f32 v79, v79
v_pk_add_f32 v[80:81], v[72:73], v[120:121]
v_pk_add_f32 v[82:83], v[74:75], v[120:121]
v_pk_add_f32 v[84:85], v[76:77], v[120:121]
v_pk_add_f32 v[86:87], v[78:79], v[120:121]
v_pk_mul_f32 v[80:81], v[80:81], v[72:73]
v_pk_mul_f32 v[82:83], v[82:83], v[74:75]
v_pk_mul_f32 v[84:85], v[84:85], v[76:77]
v_pk_mul_f32 v[86:87], v[86:87], v[78:79]
v_pk_add_f32 v[88:89], v[80:81], v[120:121]
v_pk_add_f32 v[90:91], v[82:83], v[120:121]
v_pk_add_f32 v[92:93], v[84:85], v[120:121]
v_pk_add_f32 v[94:95], v[86:87], v[120:121]
v_rcp_f32 v88, v88
v_rcp_f32 v89, v89
v_rcp_f32 v90, v90
v_rcp_f32 v91, v91
v_rcp_f32 v92, v92
v_rcp_f32 v93, v93
v_rcp_f32 v94, v94
v_rcp_f32 v95, v95
v_pk_mul_f32 v[80:81], v[80:81], v[88:89]
v_pk_mul_f32 v[82:83], v[82:83], v[90:91]
v_pk_mul_f32 v[84:85], v[84:85], v[92:93]
v_pk_mul_f32 v[86:87], v[86:87], v[94:95]
v_pk_mul_f32 v[144:145], v[144:145], v[80:81]
v_pk_mul_f32 v[146:147], v[146:147], v[82:83]
v_pk_mul_f32 v[176:177], v[176:177], v[84:85]
v_pk_mul_f32 v[178:179], v[178:179], v[86:87]
v_cvt_pk_bf16_f32 v144, v144, v145
v_cvt_pk_bf16_f32 v145, v146, v147
v_cvt_pk_bf16_f32 v146, v176, v177
v_cvt_pk_bf16_f32 v147, v178, v179
buffer_store_dwordx4 v[144:147], v252, s[60:63], s76 offen offset:0 sc1
v_pk_mul_f32 v[72:73], v[148:149], s[86:87]
v_pk_mul_f32 v[74:75], v[150:151], s[86:87]
v_pk_mul_f32 v[76:77], v[180:181], s[86:87]
v_pk_mul_f32 v[78:79], v[182:183], s[86:87]
v_min_f32 v72, 0x4266d4ca, v72
v_min_f32 v73, 0x4266d4ca, v73
v_min_f32 v74, 0x4266d4ca, v74
v_min_f32 v75, 0x4266d4ca, v75
v_min_f32 v76, 0x4266d4ca, v76
v_min_f32 v77, 0x4266d4ca, v77
v_min_f32 v78, 0x4266d4ca, v78
v_min_f32 v79, 0x4266d4ca, v79
v_exp_f32 v72, v72
v_exp_f32 v73, v73
v_exp_f32 v74, v74
v_exp_f32 v75, v75
v_exp_f32 v76, v76
v_exp_f32 v77, v77
v_exp_f32 v78, v78
v_exp_f32 v79, v79
v_pk_add_f32 v[80:81], v[72:73], v[120:121]
v_pk_add_f32 v[82:83], v[74:75], v[120:121]
v_pk_add_f32 v[84:85], v[76:77], v[120:121]
v_pk_add_f32 v[86:87], v[78:79], v[120:121]
v_pk_mul_f32 v[80:81], v[80:81], v[72:73]
v_pk_mul_f32 v[82:83], v[82:83], v[74:75]
v_pk_mul_f32 v[84:85], v[84:85], v[76:77]
v_pk_mul_f32 v[86:87], v[86:87], v[78:79]
v_pk_add_f32 v[88:89], v[80:81], v[120:121]
v_pk_add_f32 v[90:91], v[82:83], v[120:121]
v_pk_add_f32 v[92:93], v[84:85], v[120:121]
v_pk_add_f32 v[94:95], v[86:87], v[120:121]
v_rcp_f32 v88, v88
v_rcp_f32 v89, v89
v_rcp_f32 v90, v90
v_rcp_f32 v91, v91
v_rcp_f32 v92, v92
v_rcp_f32 v93, v93
v_rcp_f32 v94, v94
v_rcp_f32 v95, v95
v_pk_mul_f32 v[80:81], v[80:81], v[88:89]
v_pk_mul_f32 v[82:83], v[82:83], v[90:91]
v_pk_mul_f32 v[84:85], v[84:85], v[92:93]
v_pk_mul_f32 v[86:87], v[86:87], v[94:95]
v_pk_mul_f32 v[148:149], v[148:149], v[80:81]
v_pk_mul_f32 v[150:151], v[150:151], v[82:83]
v_pk_mul_f32 v[180:181], v[180:181], v[84:85]
v_pk_mul_f32 v[182:183], v[182:183], v[86:87]
v_cvt_pk_bf16_f32 v148, v148, v149
v_cvt_pk_bf16_f32 v149, v150, v151
v_cvt_pk_bf16_f32 v150, v180, v181
v_cvt_pk_bf16_f32 v151, v182, v183
buffer_store_dwordx4 v[148:151], v252, s[60:63], s76 offen offset:1024 sc1
v_pk_mul_f32 v[72:73], v[152:153], s[86:87]
v_pk_mul_f32 v[74:75], v[154:155], s[86:87]
v_pk_mul_f32 v[76:77], v[184:185], s[86:87]
v_pk_mul_f32 v[78:79], v[186:187], s[86:87]
v_min_f32 v72, 0x4266d4ca, v72
v_min_f32 v73, 0x4266d4ca, v73
v_min_f32 v74, 0x4266d4ca, v74
v_min_f32 v75, 0x4266d4ca, v75
v_min_f32 v76, 0x4266d4ca, v76
v_min_f32 v77, 0x4266d4ca, v77
v_min_f32 v78, 0x4266d4ca, v78
v_min_f32 v79, 0x4266d4ca, v79
v_exp_f32 v72, v72
v_exp_f32 v73, v73
v_exp_f32 v74, v74
v_exp_f32 v75, v75
v_exp_f32 v76, v76
v_exp_f32 v77, v77
v_exp_f32 v78, v78
v_exp_f32 v79, v79
v_pk_add_f32 v[80:81], v[72:73], v[120:121]
v_pk_add_f32 v[82:83], v[74:75], v[120:121]
v_pk_add_f32 v[84:85], v[76:77], v[120:121]
v_pk_add_f32 v[86:87], v[78:79], v[120:121]
v_pk_mul_f32 v[80:81], v[80:81], v[72:73]
v_pk_mul_f32 v[82:83], v[82:83], v[74:75]
v_pk_mul_f32 v[84:85], v[84:85], v[76:77]
v_pk_mul_f32 v[86:87], v[86:87], v[78:79]
v_pk_add_f32 v[88:89], v[80:81], v[120:121]
v_pk_add_f32 v[90:91], v[82:83], v[120:121]
v_pk_add_f32 v[92:93], v[84:85], v[120:121]
v_pk_add_f32 v[94:95], v[86:87], v[120:121]
v_rcp_f32 v88, v88
v_rcp_f32 v89, v89
v_rcp_f32 v90, v90
v_rcp_f32 v91, v91
v_rcp_f32 v92, v92
v_rcp_f32 v93, v93
v_rcp_f32 v94, v94
v_rcp_f32 v95, v95
v_pk_mul_f32 v[80:81], v[80:81], v[88:89]
v_pk_mul_f32 v[82:83], v[82:83], v[90:91]
v_pk_mul_f32 v[84:85], v[84:85], v[92:93]
v_pk_mul_f32 v[86:87], v[86:87], v[94:95]
v_pk_mul_f32 v[152:153], v[152:153], v[80:81]
v_pk_mul_f32 v[154:155], v[154:155], v[82:83]
v_pk_mul_f32 v[184:185], v[184:185], v[84:85]
v_pk_mul_f32 v[186:187], v[186:187], v[86:87]
v_cvt_pk_bf16_f32 v152, v152, v153
v_cvt_pk_bf16_f32 v153, v154, v155
v_cvt_pk_bf16_f32 v154, v184, v185
v_cvt_pk_bf16_f32 v155, v186, v187
buffer_store_dwordx4 v[152:155], v252, s[60:63], s76 offen offset:2048 sc1
v_pk_mul_f32 v[72:73], v[156:157], s[86:87]
v_pk_mul_f32 v[74:75], v[158:159], s[86:87]
v_pk_mul_f32 v[76:77], v[188:189], s[86:87]
v_pk_mul_f32 v[78:79], v[190:191], s[86:87]
v_min_f32 v72, 0x4266d4ca, v72
v_min_f32 v73, 0x4266d4ca, v73
v_min_f32 v74, 0x4266d4ca, v74
v_min_f32 v75, 0x4266d4ca, v75
v_min_f32 v76, 0x4266d4ca, v76
v_min_f32 v77, 0x4266d4ca, v77
v_min_f32 v78, 0x4266d4ca, v78
v_min_f32 v79, 0x4266d4ca, v79
v_exp_f32 v72, v72
v_exp_f32 v73, v73
v_exp_f32 v74, v74
v_exp_f32 v75, v75
v_exp_f32 v76, v76
v_exp_f32 v77, v77
v_exp_f32 v78, v78
v_exp_f32 v79, v79
v_pk_add_f32 v[80:81], v[72:73], v[120:121]
v_pk_add_f32 v[82:83], v[74:75], v[120:121]
v_pk_add_f32 v[84:85], v[76:77], v[120:121]
v_pk_add_f32 v[86:87], v[78:79], v[120:121]
v_pk_mul_f32 v[80:81], v[80:81], v[72:73]
v_pk_mul_f32 v[82:83], v[82:83], v[74:75]
v_pk_mul_f32 v[84:85], v[84:85], v[76:77]
v_pk_mul_f32 v[86:87], v[86:87], v[78:79]
v_pk_add_f32 v[88:89], v[80:81], v[120:121]
v_pk_add_f32 v[90:91], v[82:83], v[120:121]
v_pk_add_f32 v[92:93], v[84:85], v[120:121]
v_pk_add_f32 v[94:95], v[86:87], v[120:121]
v_rcp_f32 v88, v88
v_rcp_f32 v89, v89
v_rcp_f32 v90, v90
v_rcp_f32 v91, v91
v_rcp_f32 v92, v92
v_rcp_f32 v93, v93
v_rcp_f32 v94, v94
v_rcp_f32 v95, v95
v_pk_mul_f32 v[80:81], v[80:81], v[88:89]
v_pk_mul_f32 v[82:83], v[82:83], v[90:91]
v_pk_mul_f32 v[84:85], v[84:85], v[92:93]
v_pk_mul_f32 v[86:87], v[86:87], v[94:95]
v_pk_mul_f32 v[156:157], v[156:157], v[80:81]
v_pk_mul_f32 v[158:159], v[158:159], v[82:83]
v_pk_mul_f32 v[188:189], v[188:189], v[84:85]
v_pk_mul_f32 v[190:191], v[190:191], v[86:87]
v_cvt_pk_bf16_f32 v156, v156, v157
v_cvt_pk_bf16_f32 v157, v158, v159
v_cvt_pk_bf16_f32 v158, v188, v189
v_cvt_pk_bf16_f32 v159, v190, v191
buffer_store_dwordx4 v[156:159], v252, s[60:63], s76 offen offset:3072 sc1
s_cmp_lg_u32 s68, 0
s_cbranch_scc1 mla_fused_f_pc3x
s_mov_b32 s90, 262144
s_waitcnt vmcnt(4)
s_branch mla_fused_f_pc3c
mla_fused_f_pc3:
buffer_load_dwordx2 v[246:247], v248, s[60:63], s85 offen sc1
s_waitcnt vmcnt(0)
mla_fused_f_pc3c:
v_cmp_eq_u32 vcc, s98, v246
v_cmp_eq_u32 s[94:95], s99, v247
s_nop 3
s_and_b64 vcc, vcc, s[94:95]
s_and_b64 vcc, vcc, s[92:93]
s_cmp_eq_u64 vcc, s[92:93]
s_cbranch_scc1 mla_fused_f_pc3d
s_sub_u32 s90, s90, 1
s_cmp_eq_u32 s90, 0
s_cbranch_scc1 mla_fused_f_pc3d
s_sleep 2
s_branch mla_fused_f_pc3
mla_fused_f_pc3d:
mla_fused_f_pc3x:
v_and_b32 v253, 63, %[tid]
s_lshl_b32 s82, s69, 14
v_add_u32 v240, s82, v252
s_add_u32 s82, s82, 65536
v_add_u32 v241, s82, v252
s_lshl_b32 s82, s68, 12
s_add_u32 s82, s82, 131072
v_add_u32 v249, s82, v252
s_xor_b32 s82, s82, 8192
v_add_u32 v250, s82, v252
v_lshrrev_b32 v254, 4, v253
v_lshlrev_b32 v254, 2, v254
v_and_b32 v245, 15, v253
v_sub_u32 v254, v254, v245
s_sub_u32 s82, s69, s70
s_lshl_b32 s82, s82, 6
s_lshl_b32 s83, s69, 5
)ASM"
R"ASM(s_sub_u32 s84, s82, s83
v_add_u32 v245, s84, v254
s_add_u32 s84, s82, s83
v_add_u32 v246, s84, v254
v_mov_b32 v160, 0x3f803f80
v_mov_b32 v161, 0x3f803f80
v_mov_b32 v162, 0x3f803f80
v_mov_b32 v163, 0x3f803f80
v_mov_b64 v[204:205], 0
v_mov_b64 v[206:207], 0
v_mov_b32 v254, 0xf149f2ca
v_add_u32 v208, -16, v245
v_cmp_lt_i32 s[90:91], 0, v208
v_cmp_lt_i32 s[92:93], -1, v208
v_cmp_lt_i32 s[94:95], -2, v208
v_cmp_lt_i32 s[96:97], -3, v208
v_cndmask_b32 v164, 0, v254, s[90:91]
v_cndmask_b32 v165, 0, v254, s[92:93]
v_cndmask_b32 v166, 0, v254, s[94:95]
v_cndmask_b32 v167, 0, v254, s[96:97]
v_add_u32 v208, 0, v245
v_cmp_lt_i32 s[90:91], 0, v208
v_cmp_lt_i32 s[92:93], -1, v208
v_cmp_lt_i32 s[94:95], -2, v208
v_cmp_lt_i32 s[96:97], -3, v208
v_cndmask_b32 v168, 0, v254, s[90:91]
v_cndmask_b32 v169, 0, v254, s[92:93]
v_cndmask_b32 v170, 0, v254, s[94:95]
v_cndmask_b32 v171, 0, v254, s[96:97]
v_add_u32 v208, 16, v245
v_cmp_lt_i32 s[90:91], 0, v208
v_cmp_lt_i32 s[92:93], -1, v208
v_cmp_lt_i32 s[94:95], -2, v208
v_cmp_lt_i32 s[96:97], -3, v208
v_cndmask_b32 v172, 0, v254, s[90:91]
v_cndmask_b32 v173, 0, v254, s[92:93]
v_cndmask_b32 v174, 0, v254, s[94:95]
v_cndmask_b32 v175, 0, v254, s[96:97]
v_add_u32 v208, 32, v245
v_cmp_lt_i32 s[90:91], 0, v208
v_cmp_lt_i32 s[92:93], -1, v208
v_cmp_lt_i32 s[94:95], -2, v208
v_cmp_lt_i32 s[96:97], -3, v208
v_cndmask_b32 v176, 0, v254, s[90:91]
v_cndmask_b32 v177, 0, v254, s[92:93]
v_cndmask_b32 v178, 0, v254, s[94:95]
v_cndmask_b32 v179, 0, v254, s[96:97]
v_add_u32 v208, 48, v245
v_cmp_lt_i32 s[90:91], 0, v208
v_cmp_lt_i32 s[92:93], -1, v208
v_cmp_lt_i32 s[94:95], -2, v208
v_cmp_lt_i32 s[96:97], -3, v208
v_cndmask_b32 v180, 0, v254, s[90:91]
v_cndmask_b32 v181, 0, v254, s[92:93]
v_cndmask_b32 v182, 0, v254, s[94:95]
v_cndmask_b32 v183, 0, v254, s[96:97]
v_add_u32 v208, -48, v246
v_cmp_lt_i32 s[90:91], 0, v208
v_cmp_lt_i32 s[92:93], -1, v208
v_cmp_lt_i32 s[94:95], -2, v208
v_cmp_lt_i32 s[96:97], -3, v208
v_cndmask_b32 v184, 0, v254, s[90:91]
v_cndmask_b32 v185, 0, v254, s[92:93]
v_cndmask_b32 v186, 0, v254, s[94:95]
v_cndmask_b32 v187, 0, v254, s[96:97]
v_add_u32 v208, -32, v246
v_cmp_lt_i32 s[90:91], 0, v208
v_cmp_lt_i32 s[92:93], -1, v208
v_cmp_lt_i32 s[94:95], -2, v208
v_cmp_lt_i32 s[96:97], -3, v208
v_cndmask_b32 v188, 0, v254, s[90:91]
v_cndmask_b32 v189, 0, v254, s[92:93]
v_cndmask_b32 v190, 0, v254, s[94:95]
v_cndmask_b32 v191, 0, v254, s[96:97]
v_add_u32 v208, -16, v246
v_cmp_lt_i32 s[90:91], 0, v208
v_cmp_lt_i32 s[92:93], -1, v208
v_cmp_lt_i32 s[94:95], -2, v208
v_cmp_lt_i32 s[96:97], -3, v208
v_cndmask_b32 v192, 0, v254, s[90:91]
v_cndmask_b32 v193, 0, v254, s[92:93]
v_cndmask_b32 v194, 0, v254, s[94:95]
v_cndmask_b32 v195, 0, v254, s[96:97]
v_add_u32 v208, 0, v246
v_cmp_lt_i32 s[90:91], 0, v208
v_cmp_lt_i32 s[92:93], -1, v208
v_cmp_lt_i32 s[94:95], -2, v208
v_cmp_lt_i32 s[96:97], -3, v208
v_cndmask_b32 v196, 0, v254, s[90:91]
v_cndmask_b32 v197, 0, v254, s[92:93]
v_cndmask_b32 v198, 0, v254, s[94:95]
v_cndmask_b32 v199, 0, v254, s[96:97]
v_add_u32 v208, 16, v246
v_cmp_lt_i32 s[90:91], 0, v208
v_cmp_lt_i32 s[92:93], -1, v208
v_cmp_lt_i32 s[94:95], -2, v208
v_cmp_lt_i32 s[96:97], -3, v208
v_cndmask_b32 v200, 0, v254, s[90:91]
v_cndmask_b32 v201, 0, v254, s[92:93]
v_cndmask_b32 v202, 0, v254, s[94:95]
v_cndmask_b32 v203, 0, v254, s[96:97]
s_lshl_b32 s82, s70, 14
s_add_u32 s82, s82, 131072
s_lshl_b32 s83, s69, 13
s_add_u32 s84, s82, s83
v_add_u32 v230, s84, v252
s_xor_b32 s83, s83, 8192
s_add_u32 s84, s82, s83
v_add_u32 v231, s84, v252
s_lshl_b32 s82, s68, 10
s_add_u32 s82, s82, 98304
v_add_u32 v232, s82, v252
s_xor_b32 s82, s82, 2048
v_add_u32 v233, s82, v252
s_waitcnt lgkmcnt(0)
s_barrier
ds_read_b128 a[192:195], v240 offset:0
ds_read_b128 a[196:199], v240 offset:1024
ds_read_b128 a[200:203], v240 offset:2048
ds_read_b128 a[204:207], v240 offset:3072
mla_fused_b_initA:
s_mov_b32 s50, s48
s_waitcnt lgkmcnt(3)
v_mfma_f32_16x16x32_bf16 v[0:3], a[192:195], a[128:131], v[168:171]
s_waitcnt lgkmcnt(2)
v_mfma_f32_16x16x32_bf16 v[0:3], a[196:199], a[132:135], v[0:3]
ds_read_b128 a[208:211], v240 offset:4096
s_waitcnt lgkmcnt(2)
v_mfma_f32_16x16x32_bf16 v[0:3], a[200:203], a[136:139], v[0:3]
s_waitcnt lgkmcnt(1)
v_mfma_f32_16x16x32_bf16 v[0:3], a[204:207], a[140:143], v[0:3]
s_xor_b32 s54, s54, 32768
s_mov_b32 s74, s51
s_add_u32 m0, s54, 0
s_add_u32 s82, s74, 0
buffer_load_dwordx4 v252, s[40:43], s82 offen offset:0 sc1 lds
v_mfma_f32_16x16x32_bf16 v[4:7], a[192:195], a[144:147], v[164:167]
v_mfma_f32_16x16x32_bf16 v[4:7], a[196:199], a[148:151], v[4:7]
ds_read_b128 a[212:215], v240 offset:5120
v_mfma_f32_16x16x32_bf16 v[4:7], a[200:203], a[152:155], v[4:7]
v_mfma_f32_16x16x32_bf16 v[4:7], a[204:207], a[156:159], v[4:7]
buffer_load_dwordx4 v252, s[40:43], s82 offen offset:1024 sc1 lds
v_mfma_f32_16x16x32_bf16 v[8:11], a[192:195], a[160:163], v[188:191]
v_mfma_f32_16x16x32_bf16 v[8:11], a[196:199], a[164:167], v[8:11]
ds_read_b128 a[216:219], v240 offset:6144
v_mfma_f32_16x16x32_bf16 v[8:11], a[200:203], a[168:171], v[8:11]
v_mfma_f32_16x16x32_bf16 v[8:11], a[204:207], a[172:175], v[8:11]
buffer_load_dwordx4 v252, s[40:43], s82 offen offset:2048 sc1 lds
v_mfma_f32_16x16x32_bf16 v[12:15], a[192:195], a[176:179], v[184:187]
v_mfma_f32_16x16x32_bf16 v[12:15], a[196:199], a[180:183], v[12:15]
ds_read_b128 a[220:223], v240 offset:7168
v_mfma_f32_16x16x32_bf16 v[12:15], a[200:203], a[184:187], v[12:15]
v_mfma_f32_16x16x32_bf16 v[12:15], a[204:207], a[188:191], v[12:15]
buffer_load_dwordx4 v252, s[40:43], s82 offen offset:3072 sc1 lds
v_mfma_f32_32x32x16_bf16 a[0:15], v[160:163], v[204:207], 0
s_add_u32 m0, s54, 4096
s_add_u32 s82, s74, 4096
buffer_load_dwordx4 v252, s[40:43], s82 offen offset:0 sc1 lds
v_mfma_f32_32x32x16_bf16 a[16:31], v[160:163], v[204:207], 0
buffer_load_dwordx4 v252, s[40:43], s82 offen offset:1024 sc1 lds
buffer_load_dwordx4 v252, s[40:43], s82 offen offset:2048 sc1 lds
v_max3_f32 v208, v0, v1, v2
v_max3_f32 v209, v4, v5, v6
v_max3_f32 v210, v8, v9, v10
v_max3_f32 v211, v12, v13, v14
v_mfma_f32_32x32x16_bf16 a[32:47], v[160:163], v[204:207], 0
buffer_load_dwordx4 v252, s[40:43], s82 offen offset:3072 sc1 lds
v_max_f32 v208, v208, v3
v_max_f32 v209, v209, v7
v_max_f32 v210, v210, v11
v_max_f32 v211, v211, v15
v_mov_b32 v212, v208
v_mov_b32 v213, v209
v_mov_b32 v214, v210
v_mov_b32 v215, v211
v_mfma_f32_32x32x16_bf16 a[48:63], v[160:163], v[204:207], 0
v_permlane16_swap_b32 v208, v212
v_permlane16_swap_b32 v209, v213
v_permlane16_swap_b32 v210, v214
v_permlane16_swap_b32 v211, v215
v_max_f32 v208, v208, v212
v_max_f32 v209, v209, v213
v_max_f32 v210, v210, v214
v_max_f32 v211, v211, v215
v_mov_b32 v212, v208
v_mov_b32 v213, v209
v_mov_b32 v214, v210
v_mov_b32 v215, v211
v_mfma_f32_32x32x16_bf16 a[64:79], v[160:163], v[204:207], 0
v_permlane32_swap_b32 v208, v212
v_permlane32_swap_b32 v209, v213
v_permlane32_swap_b32 v210, v214
v_permlane32_swap_b32 v211, v215
v_max_f32 v208, v208, v212
v_max_f32 v209, v209, v213
v_max_f32 v210, v210, v214
v_max_f32 v211, v211, v215
ds_write_b128 v232, v[208:211]
v_mfma_f32_32x32x16_bf16 a[80:95], v[160:163], v[204:207], 0
v_mfma_f32_32x32x16_bf16 a[96:111], v[160:163], v[204:207], 0
s_waitcnt lgkmcnt(0)
s_barrier
ds_read_b128 v[212:215], v233
v_mfma_f32_32x32x16_bf16 a[112:127], v[160:163], v[204:207], 0
v_mfma_f32_32x32x16_bf16 v[144:159], v[160:163], v[204:207], 0
s_waitcnt lgkmcnt(0)
v_max_f32 v208, v208, v214
v_max_f32 v209, v209, v215
v_max_f32 v210, v210, v212
v_max_f32 v211, v211, v213
v_sub_f32 v128, 0, v208
v_sub_f32 v132, 0, v209
v_sub_f32 v136, 0, v210
v_sub_f32 v140, 0, v211
v_add_f32 v16, v172, v128
v_add_f32 v17, v173, v128
v_add_f32 v18, v174, v128
v_add_f32 v19, v175, v128
v_add_f32 v20, v168, v132
v_add_f32 v21, v169, v132
v_add_f32 v22, v170, v132
v_add_f32 v23, v171, v132
v_mov_b32 v129, v128
v_mov_b32 v130, v128
v_mov_b32 v131, v128
v_mov_b32 v133, v132
v_mov_b32 v134, v132
v_mov_b32 v135, v132
v_mov_b32 v137, v136
v_mov_b32 v138, v136
v_mov_b32 v139, v136
v_mov_b32 v141, v140
v_mov_b32 v142, v140
v_mov_b32 v143, v140
v_mfma_f32_16x16x32_bf16 v[16:19], a[208:211], a[128:131], v[16:19]
ds_read_b128 a[192:195], v240 offset:8192
ds_read_b128 a[196:199], v240 offset:9216
ds_read_b128 a[200:203], v240 offset:10240
ds_read_b128 a[204:207], v240 offset:11264
v_mfma_f32_16x16x32_bf16 v[16:19], a[212:215], a[132:135], v[16:19]
v_add_f32 v24, v192, v136
v_add_f32 v25, v193, v136
v_add_f32 v26, v194, v136
v_add_f32 v27, v195, v136
v_mfma_f32_16x16x32_bf16 v[16:19], a[216:219], a[136:139], v[16:19]
v_add_f32 v0, v0, v128
v_add_f32 v1, v1, v128
v_add_f32 v2, v2, v128
v_add_f32 v3, v3, v128
v_mfma_f32_16x16x32_bf16 v[16:19], a[220:223], a[140:143], v[16:19]
v_exp_f32 v0, v0
v_exp_f32 v1, v1
v_exp_f32 v2, v2
v_cvt_pk_bf16_f32 v64, v0, v1
v_exp_f32 v3, v3
v_mfma_f32_16x16x32_bf16 v[20:23], a[208:211], a[144:147], v[20:23]
v_add_f32 v28, v188, v140
v_add_f32 v29, v189, v140
v_add_f32 v30, v190, v140
v_mfma_f32_16x16x32_bf16 v[20:23], a[212:215], a[148:151], v[20:23]
v_add_f32 v31, v191, v140
v_add_f32 v4, v4, v132
v_add_f32 v5, v5, v132
v_add_f32 v6, v6, v132
v_mfma_f32_16x16x32_bf16 v[20:23], a[216:219], a[152:155], v[20:23]
v_add_f32 v7, v7, v132
v_cvt_pk_bf16_f32 v65, v2, v3
v_exp_f32 v4, v4
v_mfma_f32_16x16x32_bf16 v[20:23], a[220:223], a[156:159], v[20:23]
v_exp_f32 v5, v5
v_exp_f32 v6, v6
v_cvt_pk_bf16_f32 v72, v4, v5
v_exp_f32 v7, v7
v_mfma_f32_16x16x32_bf16 v[24:27], a[208:211], a[160:163], v[24:27]
v_add_f32 v32, v176, v128
v_add_f32 v33, v177, v128
v_add_f32 v34, v178, v128
v_mfma_f32_16x16x32_bf16 v[24:27], a[212:215], a[164:167], v[24:27]
v_add_f32 v35, v179, v128
v_add_f32 v8, v8, v136
v_add_f32 v9, v9, v136
v_add_f32 v10, v10, v136
v_mfma_f32_16x16x32_bf16 v[24:27], a[216:219], a[168:171], v[24:27]
v_add_f32 v11, v11, v136
v_cvt_pk_bf16_f32 v73, v6, v7
v_exp_f32 v8, v8
v_mfma_f32_16x16x32_bf16 v[24:27], a[220:223], a[172:175], v[24:27]
v_exp_f32 v9, v9
v_exp_f32 v10, v10
v_cvt_pk_bf16_f32 v80, v8, v9
v_exp_f32 v11, v11
v_mfma_f32_16x16x32_bf16 v[28:31], a[208:211], a[176:179], v[28:31]
v_add_f32 v36, v172, v132
v_add_f32 v37, v173, v132
v_add_f32 v38, v174, v132
v_mfma_f32_16x16x32_bf16 v[28:31], a[212:215], a[180:183], v[28:31]
v_add_f32 v39, v175, v132
v_add_f32 v12, v12, v140
v_add_f32 v13, v13, v140
v_add_f32 v14, v14, v140
v_mfma_f32_16x16x32_bf16 v[28:31], a[216:219], a[184:187], v[28:31]
v_add_f32 v15, v15, v140
v_cvt_pk_bf16_f32 v81, v10, v11
v_exp_f32 v12, v12
v_mfma_f32_16x16x32_bf16 v[28:31], a[220:223], a[188:191], v[28:31]
v_exp_f32 v13, v13
v_exp_f32 v14, v14
v_cvt_pk_bf16_f32 v88, v12, v13
v_exp_f32 v15, v15
s_waitcnt lgkmcnt(3)
v_mfma_f32_16x16x32_bf16 v[32:35], a[192:195], a[128:131], v[32:35]
ds_read_b128 a[208:211], v240 offset:12288
ds_read_b128 a[212:215], v240 offset:13312
ds_read_b128 a[216:219], v240 offset:14336
s_waitcnt lgkmcnt(5)
v_mfma_f32_16x16x32_bf16 v[32:35], a[196:199], a[132:135], v[32:35]
ds_read_b128 a[220:223], v240 offset:15360
v_add_f32 v40, v196, v136
v_add_f32 v41, v197, v136
v_add_f32 v42, v198, v136
s_waitcnt lgkmcnt(5)
v_mfma_f32_16x16x32_bf16 v[32:35], a[200:203], a[136:139], v[32:35]
v_add_f32 v43, v199, v136
v_cvt_pk_bf16_f32 v89, v14, v15
v_exp_f32 v16, v16
s_waitcnt lgkmcnt(4)
v_mfma_f32_16x16x32_bf16 v[32:35], a[204:207], a[140:143], v[32:35]
v_exp_f32 v17, v17
v_exp_f32 v18, v18
v_cvt_pk_bf16_f32 v66, v16, v17
v_exp_f32 v19, v19
v_mfma_f32_16x16x32_bf16 v[36:39], a[192:195], a[144:147], v[36:39]
v_add_f32 v44, v192, v140
v_add_f32 v45, v193, v140
v_mfma_f32_16x16x32_bf16 v[36:39], a[196:199], a[148:151], v[36:39]
v_add_f32 v46, v194, v140
v_add_f32 v47, v195, v140
v_cvt_pk_bf16_f32 v67, v18, v19
v_mfma_f32_16x16x32_bf16 v[36:39], a[200:203], a[152:155], v[36:39]
v_exp_f32 v20, v20
v_exp_f32 v21, v21
v_mfma_f32_16x16x32_bf16 v[36:39], a[204:207], a[156:159], v[36:39]
v_exp_f32 v22, v22
v_cvt_pk_bf16_f32 v74, v20, v21
v_exp_f32 v23, v23
v_mfma_f32_16x16x32_bf16 v[40:43], a[192:195], a[160:163], v[40:43]
v_add_f32 v48, v180, v128
v_add_f32 v49, v181, v128
)ASM"
R"ASM(v_mfma_f32_16x16x32_bf16 v[40:43], a[196:199], a[164:167], v[40:43]
v_add_f32 v50, v182, v128
v_add_f32 v51, v183, v128
v_cvt_pk_bf16_f32 v75, v22, v23
v_mfma_f32_16x16x32_bf16 v[40:43], a[200:203], a[168:171], v[40:43]
v_exp_f32 v24, v24
v_exp_f32 v25, v25
v_mfma_f32_16x16x32_bf16 v[40:43], a[204:207], a[172:175], v[40:43]
v_exp_f32 v26, v26
v_cvt_pk_bf16_f32 v82, v24, v25
v_exp_f32 v27, v27
v_mfma_f32_16x16x32_bf16 v[44:47], a[192:195], a[176:179], v[44:47]
v_add_f32 v52, v176, v132
v_add_f32 v53, v177, v132
v_mfma_f32_16x16x32_bf16 v[44:47], a[196:199], a[180:183], v[44:47]
v_add_f32 v54, v178, v132
v_add_f32 v55, v179, v132
v_cvt_pk_bf16_f32 v83, v26, v27
v_mfma_f32_16x16x32_bf16 v[44:47], a[200:203], a[184:187], v[44:47]
v_exp_f32 v28, v28
v_exp_f32 v29, v29
v_mfma_f32_16x16x32_bf16 v[44:47], a[204:207], a[188:191], v[44:47]
v_exp_f32 v30, v30
v_cvt_pk_bf16_f32 v90, v28, v29
v_exp_f32 v31, v31
s_waitcnt lgkmcnt(3)
v_mfma_f32_16x16x32_bf16 v[48:51], a[208:211], a[128:131], v[48:51]
v_add_f32 v56, v200, v136
v_add_f32 v57, v201, v136
s_waitcnt lgkmcnt(2)
v_mfma_f32_16x16x32_bf16 v[48:51], a[212:215], a[132:135], v[48:51]
v_add_f32 v58, v202, v136
v_add_f32 v59, v203, v136
v_cvt_pk_bf16_f32 v91, v30, v31
s_waitcnt lgkmcnt(1)
v_mfma_f32_16x16x32_bf16 v[48:51], a[216:219], a[136:139], v[48:51]
v_exp_f32 v32, v32
v_exp_f32 v33, v33
s_waitcnt lgkmcnt(0)
v_mfma_f32_16x16x32_bf16 v[48:51], a[220:223], a[140:143], v[48:51]
v_exp_f32 v34, v34
v_cvt_pk_bf16_f32 v68, v32, v33
v_exp_f32 v35, v35
v_mfma_f32_16x16x32_bf16 v[52:55], a[208:211], a[144:147], v[52:55]
v_add_f32 v60, v196, v140
v_add_f32 v61, v197, v140
v_mfma_f32_16x16x32_bf16 v[52:55], a[212:215], a[148:151], v[52:55]
v_add_f32 v62, v198, v140
v_add_f32 v63, v199, v140
v_cvt_pk_bf16_f32 v69, v34, v35
v_mfma_f32_16x16x32_bf16 v[52:55], a[216:219], a[152:155], v[52:55]
v_exp_f32 v36, v36
v_exp_f32 v37, v37
v_mfma_f32_16x16x32_bf16 v[52:55], a[220:223], a[156:159], v[52:55]
v_exp_f32 v38, v38
v_cvt_pk_bf16_f32 v76, v36, v37
v_exp_f32 v39, v39
v_mfma_f32_16x16x32_bf16 v[56:59], a[208:211], a[160:163], v[56:59]
v_cvt_pk_bf16_f32 v77, v38, v39
v_mfma_f32_16x16x32_bf16 v[56:59], a[212:215], a[164:167], v[56:59]
v_exp_f32 v40, v40
v_exp_f32 v41, v41
v_mfma_f32_16x16x32_bf16 v[56:59], a[216:219], a[168:171], v[56:59]
v_exp_f32 v42, v42
v_mfma_f32_16x16x32_bf16 v[56:59], a[220:223], a[172:175], v[56:59]
v_cvt_pk_bf16_f32 v84, v40, v41
v_exp_f32 v43, v43
v_mfma_f32_16x16x32_bf16 v[60:63], a[208:211], a[176:179], v[60:63]
v_cvt_pk_bf16_f32 v85, v42, v43
v_mfma_f32_16x16x32_bf16 v[60:63], a[212:215], a[180:183], v[60:63]
v_exp_f32 v44, v44
v_exp_f32 v45, v45
v_mfma_f32_16x16x32_bf16 v[60:63], a[216:219], a[184:187], v[60:63]
v_exp_f32 v46, v46
v_mfma_f32_16x16x32_bf16 v[60:63], a[220:223], a[188:191], v[60:63]
v_cvt_pk_bf16_f32 v92, v44, v45
v_exp_f32 v47, v47
s_sub_u32 s51, s51, 32768
s_sub_u32 s52, s52, 32768
s_xor_b32 s54, s54, 32768
s_xor_b32 s55, s55, 32768
v_cvt_pk_bf16_f32 v93, v46, v47
v_exp_f32 v48, v48
v_exp_f32 v49, v49
v_exp_f32 v50, v50
v_cvt_pk_bf16_f32 v70, v48, v49
v_exp_f32 v51, v51
s_nop 0
v_cvt_pk_bf16_f32 v71, v50, v51
v_exp_f32 v52, v52
v_exp_f32 v53, v53
v_exp_f32 v54, v54
v_cvt_pk_bf16_f32 v78, v52, v53
v_exp_f32 v55, v55
s_nop 0
v_cvt_pk_bf16_f32 v79, v54, v55
v_exp_f32 v56, v56
v_exp_f32 v57, v57
v_exp_f32 v58, v58
v_cvt_pk_bf16_f32 v86, v56, v57
v_exp_f32 v59, v59
s_nop 0
v_cvt_pk_bf16_f32 v87, v58, v59
v_exp_f32 v60, v60
v_exp_f32 v61, v61
v_exp_f32 v62, v62
v_cvt_pk_bf16_f32 v94, v60, v61
v_exp_f32 v63, v63
s_nop 0
v_cvt_pk_bf16_f32 v95, v62, v63
s_waitcnt vmcnt(0) lgkmcnt(0)
s_barrier
v_xor_b32 v240, 0x8000, v240
s_lshl_b32 s85, s72, 5
s_add_u32 s85, s85, s71
s_lshl_b32 s85, s85, 3
s_add_u32 s85, s85, 0x5703000
s_cmp_lg_u32 s68, 0
s_cbranch_scc1 mla_fused_f_pub4
v_and_b32 v218, 63, %[tid]
v_cmp_eq_u32 vcc, 0, v218
v_mov_b32 v216, 0x7ffffff0
v_mov_b32 v217, 0
v_cndmask_b32 v218, v216, v217, vcc
v_mov_b32 v216, s98
v_mov_b32 v217, s99
buffer_store_dwordx2 v[216:217], v218, s[36:39], s85 offen sc1
mla_fused_f_pub4:
ds_read_b128 a[192:195], v240 offset:0
ds_read_b128 a[196:199], v240 offset:1024
ds_read_b128 a[200:203], v240 offset:2048
ds_read_b128 a[204:207], v240 offset:3072
mla_fused_b_loop:
s_sub_u32 s50, s50, 1
s_waitcnt lgkmcnt(3)
v_mfma_f32_16x16x32_bf16 v[0:3], a[192:195], a[128:131], v[128:131]
s_cmp_eq_u32 s50, 0
s_cselect_b32 s74, s53, s51
s_cselect_b32 s83, s59, 0
s_waitcnt lgkmcnt(2)
v_mfma_f32_16x16x32_bf16 v[0:3], a[196:199], a[132:135], v[0:3]
s_cmp_eq_u32 s83, 1
s_cselect_b32 s42, 0, 0x5712000
ds_read_b128 a[208:211], v240 offset:4096
s_waitcnt lgkmcnt(2)
v_mfma_f32_16x16x32_bf16 v[0:3], a[200:203], a[136:139], v[0:3]
s_add_u32 m0, s54, 0
s_add_u32 s82, s74, 0
s_waitcnt lgkmcnt(1)
v_mfma_f32_16x16x32_bf16 v[0:3], a[204:207], a[140:143], v[0:3]
v_mfma_f32_16x16x32_bf16 v[4:7], a[192:195], a[144:147], v[132:135]
ds_read_b128 a[212:215], v240 offset:5120
v_mfma_f32_16x16x32_bf16 v[4:7], a[196:199], a[148:151], v[4:7]
buffer_load_dwordx4 v252, s[40:43], s82 offen offset:0 sc1 lds
v_mfma_f32_16x16x32_bf16 v[4:7], a[200:203], a[152:155], v[4:7]
ds_read_b128 a[216:219], v240 offset:6144
v_mfma_f32_16x16x32_bf16 v[4:7], a[204:207], a[156:159], v[4:7]
buffer_load_dwordx4 v252, s[40:43], s82 offen offset:1024 sc1 lds
v_mfma_f32_16x16x32_bf16 v[8:11], a[192:195], a[160:163], v[136:139]
v_exp_f32 v0, v0
v_mfma_f32_16x16x32_bf16 v[8:11], a[196:199], a[164:167], v[8:11]
v_mfma_f32_16x16x32_bf16 v[8:11], a[200:203], a[168:171], v[8:11]
v_exp_f32 v1, v1
v_mfma_f32_16x16x32_bf16 v[8:11], a[204:207], a[172:175], v[8:11]
buffer_load_dwordx4 v252, s[40:43], s82 offen offset:2048 sc1 lds
ds_read_b128 a[220:223], v240 offset:7168
v_mfma_f32_16x16x32_bf16 v[12:15], a[192:195], a[176:179], v[140:143]
v_exp_f32 v2, v2
v_mfma_f32_16x16x32_bf16 v[12:15], a[196:199], a[180:183], v[12:15]
buffer_load_dwordx4 v252, s[40:43], s82 offen offset:3072 sc1 lds
v_mfma_f32_16x16x32_bf16 v[12:15], a[200:203], a[184:187], v[12:15]
v_exp_f32 v3, v3
v_mfma_f32_16x16x32_bf16 v[12:15], a[204:207], a[188:191], v[12:15]
v_cvt_pk_bf16_f32 v96, v0, v1
v_cvt_pk_bf16_f32 v97, v2, v3
s_waitcnt lgkmcnt(3)
v_mfma_f32_16x16x32_bf16 v[16:19], a[208:211], a[128:131], v[128:131]
v_exp_f32 v4, v4
s_waitcnt lgkmcnt(2)
v_mfma_f32_16x16x32_bf16 v[16:19], a[212:215], a[132:135], v[16:19]
ds_read_b128 a[192:195], v240 offset:8192
s_waitcnt lgkmcnt(2)
v_mfma_f32_16x16x32_bf16 v[16:19], a[216:219], a[136:139], v[16:19]
v_exp_f32 v5, v5
s_add_u32 m0, s54, 4096
s_add_u32 s82, s74, 4096
s_waitcnt lgkmcnt(1)
v_mfma_f32_16x16x32_bf16 v[16:19], a[220:223], a[140:143], v[16:19]
ds_read_b128 a[196:199], v240 offset:9216
v_cvt_pk_bf16_f32 v104, v4, v5
v_mfma_f32_16x16x32_bf16 v[20:23], a[208:211], a[144:147], v[132:135]
v_exp_f32 v6, v6
v_mfma_f32_16x16x32_bf16 v[20:23], a[212:215], a[148:151], v[20:23]
buffer_load_dwordx4 v252, s[40:43], s82 offen offset:0 sc1 lds
v_mfma_f32_16x16x32_bf16 v[20:23], a[216:219], a[152:155], v[20:23]
v_exp_f32 v7, v7
v_mfma_f32_16x16x32_bf16 v[20:23], a[220:223], a[156:159], v[20:23]
buffer_load_dwordx4 v252, s[40:43], s82 offen offset:1024 sc1 lds
v_mfma_f32_16x16x32_bf16 v[24:27], a[208:211], a[160:163], v[136:139]
v_exp_f32 v8, v8
v_mfma_f32_16x16x32_bf16 v[24:27], a[212:215], a[164:167], v[24:27]
ds_read_b128 a[200:203], v240 offset:10240
v_cvt_pk_bf16_f32 v105, v6, v7
v_mfma_f32_16x16x32_bf16 v[24:27], a[216:219], a[168:171], v[24:27]
v_exp_f32 v9, v9
v_mfma_f32_16x16x32_bf16 v[24:27], a[220:223], a[172:175], v[24:27]
buffer_load_dwordx4 v252, s[40:43], s82 offen offset:2048 sc1 lds
ds_read_b128 a[204:207], v240 offset:11264
v_mfma_f32_16x16x32_bf16 v[28:31], a[208:211], a[176:179], v[140:143]
v_exp_f32 v10, v10
v_mfma_f32_16x16x32_bf16 v[28:31], a[212:215], a[180:183], v[28:31]
buffer_load_dwordx4 v252, s[40:43], s82 offen offset:3072 sc1 lds
v_mfma_f32_16x16x32_bf16 v[28:31], a[216:219], a[184:187], v[28:31]
v_exp_f32 v11, v11
v_mfma_f32_16x16x32_bf16 v[28:31], a[220:223], a[188:191], v[28:31]
v_cvt_pk_bf16_f32 v112, v8, v9
v_cvt_pk_bf16_f32 v113, v10, v11
s_waitcnt lgkmcnt(3)
v_mfma_f32_16x16x32_bf16 v[32:35], a[192:195], a[128:131], v[128:131]
v_exp_f32 v12, v12
s_waitcnt lgkmcnt(2)
v_mfma_f32_16x16x32_bf16 v[32:35], a[196:199], a[132:135], v[32:35]
ds_read_b128 a[208:211], v240 offset:12288
s_waitcnt lgkmcnt(2)
v_mfma_f32_16x16x32_bf16 v[32:35], a[200:203], a[136:139], v[32:35]
v_exp_f32 v13, v13
s_add_u32 m0, s55, 0
s_add_u32 s82, s52, 0
s_waitcnt lgkmcnt(1)
v_mfma_f32_16x16x32_bf16 v[32:35], a[204:207], a[140:143], v[32:35]
ds_read_b128 a[212:215], v240 offset:13312
v_cvt_pk_bf16_f32 v120, v12, v13
v_mfma_f32_16x16x32_bf16 v[36:39], a[192:195], a[144:147], v[132:135]
v_exp_f32 v14, v14
v_mfma_f32_16x16x32_bf16 v[36:39], a[196:199], a[148:151], v[36:39]
buffer_load_dwordx4 v252, s[36:39], s82 offen offset:0 sc1 lds
v_mfma_f32_16x16x32_bf16 v[36:39], a[200:203], a[152:155], v[36:39]
v_exp_f32 v15, v15
v_mfma_f32_16x16x32_bf16 v[36:39], a[204:207], a[156:159], v[36:39]
buffer_load_dwordx4 v252, s[36:39], s82 offen offset:1024 sc1 lds
v_mfma_f32_16x16x32_bf16 v[40:43], a[192:195], a[160:163], v[136:139]
v_exp_f32 v16, v16
v_mfma_f32_16x16x32_bf16 v[40:43], a[196:199], a[164:167], v[40:43]
ds_read_b128 a[216:219], v240 offset:14336
v_cvt_pk_bf16_f32 v121, v14, v15
v_mfma_f32_16x16x32_bf16 v[40:43], a[200:203], a[168:171], v[40:43]
v_exp_f32 v17, v17
v_mfma_f32_16x16x32_bf16 v[40:43], a[204:207], a[172:175], v[40:43]
buffer_load_dwordx4 v252, s[36:39], s82 offen offset:2048 sc1 lds
ds_read_b128 a[220:223], v240 offset:15360
v_mfma_f32_16x16x32_bf16 v[44:47], a[192:195], a[176:179], v[140:143]
v_exp_f32 v18, v18
v_mfma_f32_16x16x32_bf16 v[44:47], a[196:199], a[180:183], v[44:47]
buffer_load_dwordx4 v252, s[36:39], s82 offen offset:3072 sc1 lds
v_mfma_f32_16x16x32_bf16 v[44:47], a[200:203], a[184:187], v[44:47]
v_exp_f32 v19, v19
v_mfma_f32_16x16x32_bf16 v[44:47], a[204:207], a[188:191], v[44:47]
ds_read_b128 a[224:227], v241 offset:0
v_cvt_pk_bf16_f32 v98, v16, v17
s_waitcnt lgkmcnt(4)
v_mfma_f32_16x16x32_bf16 v[48:51], a[208:211], a[128:131], v[128:131]
v_exp_f32 v20, v20
s_waitcnt lgkmcnt(3)
v_mfma_f32_16x16x32_bf16 v[48:51], a[212:215], a[132:135], v[48:51]
v_cvt_pk_bf16_f32 v99, v18, v19
s_waitcnt lgkmcnt(2)
v_mfma_f32_16x16x32_bf16 v[48:51], a[216:219], a[136:139], v[48:51]
v_exp_f32 v21, v21
s_add_u32 m0, s55, 4096
s_add_u32 s82, s52, 4096
s_waitcnt lgkmcnt(1)
v_mfma_f32_16x16x32_bf16 v[48:51], a[220:223], a[140:143], v[48:51]
ds_read_b128 a[228:231], v241 offset:8192
v_cvt_pk_bf16_f32 v106, v20, v21
v_mfma_f32_16x16x32_bf16 v[52:55], a[208:211], a[144:147], v[132:135]
v_exp_f32 v22, v22
v_mfma_f32_16x16x32_bf16 v[52:55], a[212:215], a[148:151], v[52:55]
buffer_load_dwordx4 v252, s[36:39], s82 offen offset:0 sc1 lds
v_mfma_f32_16x16x32_bf16 v[52:55], a[216:219], a[152:155], v[52:55]
v_exp_f32 v23, v23
v_mfma_f32_16x16x32_bf16 v[52:55], a[220:223], a[156:159], v[52:55]
buffer_load_dwordx4 v252, s[36:39], s82 offen offset:1024 sc1 lds
v_mfma_f32_16x16x32_bf16 v[56:59], a[208:211], a[160:163], v[136:139]
v_exp_f32 v24, v24
v_mfma_f32_16x16x32_bf16 v[56:59], a[212:215], a[164:167], v[56:59]
ds_read_b128 a[232:235], v241 offset:1024
v_cvt_pk_bf16_f32 v107, v22, v23
v_mfma_f32_16x16x32_bf16 v[56:59], a[216:219], a[168:171], v[56:59]
v_exp_f32 v25, v25
v_mfma_f32_16x16x32_bf16 v[56:59], a[220:223], a[172:175], v[56:59]
buffer_load_dwordx4 v252, s[36:39], s82 offen offset:2048 sc1 lds
ds_read_b128 a[236:239], v241 offset:9216
v_mfma_f32_16x16x32_bf16 v[60:63], a[208:211], a[176:179], v[140:143]
v_exp_f32 v26, v26
v_mfma_f32_16x16x32_bf16 v[60:63], a[212:215], a[180:183], v[60:63]
v_cvt_pk_bf16_f32 v114, v24, v25
v_mfma_f32_16x16x32_bf16 v[60:63], a[216:219], a[184:187], v[60:63]
v_exp_f32 v27, v27
v_mfma_f32_16x16x32_bf16 v[60:63], a[220:223], a[188:191], v[60:63]
buffer_load_dwordx4 v252, s[36:39], s82 offen offset:3072 sc1 lds
s_sub_u32 s51, s51, 32768
s_waitcnt lgkmcnt(3)
v_mfma_f32_16x16x32_bf16 a[0:3], a[224:227], v[64:67], a[0:3]
)ASM"
R"ASM(v_exp_f32 v28, v28
s_sub_u32 s52, s52, 32768
s_xor_b32 s54, s54, 32768
s_waitcnt lgkmcnt(2)
v_mfma_f32_16x16x32_bf16 a[0:3], a[228:231], v[68:71], a[0:3]
s_xor_b32 s55, s55, 32768
ds_read_b128 a[240:243], v241 offset:2048
v_cvt_pk_bf16_f32 v115, v26, v27
v_mfma_f32_16x16x32_bf16 a[4:7], a[224:227], v[72:75], a[4:7]
v_exp_f32 v29, v29
v_mfma_f32_16x16x32_bf16 a[4:7], a[228:231], v[76:79], a[4:7]
v_cvt_pk_bf16_f32 v122, v28, v29
v_mfma_f32_16x16x32_bf16 a[8:11], a[224:227], v[80:83], a[8:11]
v_exp_f32 v30, v30
v_mfma_f32_16x16x32_bf16 a[8:11], a[228:231], v[84:87], a[8:11]
ds_read_b128 a[244:247], v241 offset:10240
v_mfma_f32_16x16x32_bf16 a[12:15], a[224:227], v[88:91], a[12:15]
v_exp_f32 v31, v31
v_mfma_f32_16x16x32_bf16 a[12:15], a[228:231], v[92:95], a[12:15]
v_cvt_pk_bf16_f32 v123, v30, v31
s_waitcnt lgkmcnt(3)
v_mfma_f32_16x16x32_bf16 a[16:19], a[232:235], v[64:67], a[16:19]
v_exp_f32 v32, v32
s_waitcnt lgkmcnt(2)
v_mfma_f32_16x16x32_bf16 a[16:19], a[236:239], v[68:71], a[16:19]
ds_read_b128 a[248:251], v241 offset:3072
v_mfma_f32_16x16x32_bf16 a[20:23], a[232:235], v[72:75], a[20:23]
v_exp_f32 v33, v33
v_mfma_f32_16x16x32_bf16 a[20:23], a[236:239], v[76:79], a[20:23]
v_cvt_pk_bf16_f32 v100, v32, v33
v_mfma_f32_16x16x32_bf16 a[24:27], a[232:235], v[80:83], a[24:27]
v_exp_f32 v34, v34
v_mfma_f32_16x16x32_bf16 a[24:27], a[236:239], v[84:87], a[24:27]
ds_read_b128 a[252:255], v241 offset:11264
v_mfma_f32_16x16x32_bf16 a[28:31], a[232:235], v[88:91], a[28:31]
v_exp_f32 v35, v35
v_mfma_f32_16x16x32_bf16 a[28:31], a[236:239], v[92:95], a[28:31]
v_cvt_pk_bf16_f32 v101, v34, v35
s_waitcnt lgkmcnt(3)
v_mfma_f32_16x16x32_bf16 a[32:35], a[240:243], v[64:67], a[32:35]
v_exp_f32 v36, v36
s_waitcnt lgkmcnt(2)
v_mfma_f32_16x16x32_bf16 a[32:35], a[244:247], v[68:71], a[32:35]
ds_read_b128 a[224:227], v241 offset:4096
v_mfma_f32_16x16x32_bf16 a[36:39], a[240:243], v[72:75], a[36:39]
v_exp_f32 v37, v37
v_mfma_f32_16x16x32_bf16 a[36:39], a[244:247], v[76:79], a[36:39]
v_cvt_pk_bf16_f32 v108, v36, v37
v_mfma_f32_16x16x32_bf16 a[40:43], a[240:243], v[80:83], a[40:43]
v_exp_f32 v38, v38
v_mfma_f32_16x16x32_bf16 a[40:43], a[244:247], v[84:87], a[40:43]
ds_read_b128 a[228:231], v241 offset:12288
v_mfma_f32_16x16x32_bf16 a[44:47], a[240:243], v[88:91], a[44:47]
v_exp_f32 v39, v39
v_mfma_f32_16x16x32_bf16 a[44:47], a[244:247], v[92:95], a[44:47]
v_cvt_pk_bf16_f32 v109, v38, v39
s_waitcnt lgkmcnt(3)
v_mfma_f32_16x16x32_bf16 a[48:51], a[248:251], v[64:67], a[48:51]
v_exp_f32 v40, v40
s_waitcnt lgkmcnt(2)
v_mfma_f32_16x16x32_bf16 a[48:51], a[252:255], v[68:71], a[48:51]
ds_read_b128 a[232:235], v241 offset:5120
v_mfma_f32_16x16x32_bf16 a[52:55], a[248:251], v[72:75], a[52:55]
v_exp_f32 v41, v41
v_mfma_f32_16x16x32_bf16 a[52:55], a[252:255], v[76:79], a[52:55]
v_cvt_pk_bf16_f32 v116, v40, v41
v_mfma_f32_16x16x32_bf16 a[56:59], a[248:251], v[80:83], a[56:59]
v_exp_f32 v42, v42
v_mfma_f32_16x16x32_bf16 a[56:59], a[252:255], v[84:87], a[56:59]
ds_read_b128 a[236:239], v241 offset:13312
v_mfma_f32_16x16x32_bf16 a[60:63], a[248:251], v[88:91], a[60:63]
v_exp_f32 v43, v43
v_mfma_f32_16x16x32_bf16 a[60:63], a[252:255], v[92:95], a[60:63]
v_cvt_pk_bf16_f32 v117, v42, v43
s_waitcnt lgkmcnt(3)
v_mfma_f32_16x16x32_bf16 a[64:67], a[224:227], v[64:67], a[64:67]
v_exp_f32 v44, v44
s_waitcnt lgkmcnt(2)
v_mfma_f32_16x16x32_bf16 a[64:67], a[228:231], v[68:71], a[64:67]
ds_read_b128 a[240:243], v241 offset:6144
v_mfma_f32_16x16x32_bf16 a[68:71], a[224:227], v[72:75], a[68:71]
v_exp_f32 v45, v45
v_mfma_f32_16x16x32_bf16 a[68:71], a[228:231], v[76:79], a[68:71]
v_cvt_pk_bf16_f32 v124, v44, v45
v_mfma_f32_16x16x32_bf16 a[72:75], a[224:227], v[80:83], a[72:75]
v_exp_f32 v46, v46
v_mfma_f32_16x16x32_bf16 a[72:75], a[228:231], v[84:87], a[72:75]
ds_read_b128 a[244:247], v241 offset:14336
v_mfma_f32_16x16x32_bf16 a[76:79], a[224:227], v[88:91], a[76:79]
v_exp_f32 v47, v47
v_mfma_f32_16x16x32_bf16 a[76:79], a[228:231], v[92:95], a[76:79]
v_cvt_pk_bf16_f32 v125, v46, v47
s_waitcnt lgkmcnt(3)
v_mfma_f32_16x16x32_bf16 a[80:83], a[232:235], v[64:67], a[80:83]
v_exp_f32 v48, v48
s_waitcnt lgkmcnt(2)
v_mfma_f32_16x16x32_bf16 a[80:83], a[236:239], v[68:71], a[80:83]
ds_read_b128 a[248:251], v241 offset:7168
v_mfma_f32_16x16x32_bf16 a[84:87], a[232:235], v[72:75], a[84:87]
v_exp_f32 v49, v49
v_mfma_f32_16x16x32_bf16 a[84:87], a[236:239], v[76:79], a[84:87]
v_cvt_pk_bf16_f32 v102, v48, v49
v_mfma_f32_16x16x32_bf16 a[88:91], a[232:235], v[80:83], a[88:91]
v_exp_f32 v50, v50
v_mfma_f32_16x16x32_bf16 a[88:91], a[236:239], v[84:87], a[88:91]
ds_read_b128 a[252:255], v241 offset:15360
v_mfma_f32_16x16x32_bf16 a[92:95], a[232:235], v[88:91], a[92:95]
v_exp_f32 v51, v51
v_mfma_f32_16x16x32_bf16 a[92:95], a[236:239], v[92:95], a[92:95]
v_cvt_pk_bf16_f32 v103, v50, v51
s_waitcnt lgkmcnt(3)
v_mfma_f32_16x16x32_bf16 a[96:99], a[240:243], v[64:67], a[96:99]
v_exp_f32 v52, v52
s_waitcnt lgkmcnt(2)
v_mfma_f32_16x16x32_bf16 a[96:99], a[244:247], v[68:71], a[96:99]
v_mfma_f32_16x16x32_bf16 a[100:103], a[240:243], v[72:75], a[100:103]
v_exp_f32 v53, v53
v_xor_b32 v241, 0x8000, v241
v_mfma_f32_16x16x32_bf16 a[100:103], a[244:247], v[76:79], a[100:103]
v_cvt_pk_bf16_f32 v110, v52, v53
s_waitcnt vmcnt(0) lgkmcnt(0)
s_barrier
v_xor_b32 v240, 0x8000, v240
v_mfma_f32_16x16x32_bf16 a[104:107], a[240:243], v[80:83], a[104:107]
v_exp_f32 v54, v54
v_mfma_f32_16x16x32_bf16 a[104:107], a[244:247], v[84:87], a[104:107]
ds_read_b128 a[192:195], v240 offset:0
v_mfma_f32_16x16x32_bf16 a[108:111], a[240:243], v[88:91], a[108:111]
v_exp_f32 v55, v55
v_mfma_f32_16x16x32_bf16 a[108:111], a[244:247], v[92:95], a[108:111]
ds_read_b128 a[196:199], v240 offset:1024
v_cvt_pk_bf16_f32 v111, v54, v55
v_mfma_f32_16x16x32_bf16 a[112:115], a[248:251], v[64:67], a[112:115]
v_exp_f32 v56, v56
v_mfma_f32_16x16x32_bf16 a[112:115], a[252:255], v[68:71], a[112:115]
v_mfma_f32_16x16x32_bf16 a[116:119], a[248:251], v[72:75], a[116:119]
v_exp_f32 v57, v57
v_mfma_f32_16x16x32_bf16 a[116:119], a[252:255], v[76:79], a[116:119]
ds_read_b128 a[200:203], v240 offset:2048
v_cvt_pk_bf16_f32 v118, v56, v57
v_mfma_f32_16x16x32_bf16 a[120:123], a[248:251], v[80:83], a[120:123]
v_exp_f32 v58, v58
v_mfma_f32_16x16x32_bf16 a[120:123], a[252:255], v[84:87], a[120:123]
v_mfma_f32_16x16x32_bf16 a[124:127], a[248:251], v[88:91], a[124:127]
v_exp_f32 v59, v59
v_mfma_f32_16x16x32_bf16 a[124:127], a[252:255], v[92:95], a[124:127]
ds_read_b128 a[204:207], v240 offset:3072
v_cvt_pk_bf16_f32 v119, v58, v59
v_mfma_f32_16x16x32_bf16 v[144:147], v[160:163], v[64:67], v[144:147]
v_exp_f32 v60, v60
v_mfma_f32_16x16x32_bf16 v[144:147], v[160:163], v[68:71], v[144:147]
v_mfma_f32_16x16x32_bf16 v[148:151], v[160:163], v[72:75], v[148:151]
v_exp_f32 v61, v61
v_mfma_f32_16x16x32_bf16 v[148:151], v[160:163], v[76:79], v[148:151]
v_cvt_pk_bf16_f32 v126, v60, v61
v_mfma_f32_16x16x32_bf16 v[152:155], v[160:163], v[80:83], v[152:155]
v_exp_f32 v62, v62
v_mfma_f32_16x16x32_bf16 v[152:155], v[160:163], v[84:87], v[152:155]
v_mfma_f32_16x16x32_bf16 v[156:159], v[160:163], v[88:91], v[156:159]
v_exp_f32 v63, v63
v_mfma_f32_16x16x32_bf16 v[156:159], v[160:163], v[92:95], v[156:159]
v_cvt_pk_bf16_f32 v127, v62, v63
s_cmp_eq_u32 s50, 0
s_cbranch_scc1 mla_fused_b_drain1
s_sub_u32 s50, s50, 1
s_waitcnt lgkmcnt(3)
v_mfma_f32_16x16x32_bf16 v[0:3], a[192:195], a[128:131], v[128:131]
s_cmp_eq_u32 s50, 0
s_cselect_b32 s74, s53, s51
s_cselect_b32 s83, s59, 0
s_waitcnt lgkmcnt(2)
v_mfma_f32_16x16x32_bf16 v[0:3], a[196:199], a[132:135], v[0:3]
s_cmp_eq_u32 s83, 1
s_cselect_b32 s42, 0, 0x5712000
ds_read_b128 a[208:211], v240 offset:4096
s_waitcnt lgkmcnt(2)
v_mfma_f32_16x16x32_bf16 v[0:3], a[200:203], a[136:139], v[0:3]
s_add_u32 m0, s54, 0
s_add_u32 s82, s74, 0
s_waitcnt lgkmcnt(1)
v_mfma_f32_16x16x32_bf16 v[0:3], a[204:207], a[140:143], v[0:3]
v_mfma_f32_16x16x32_bf16 v[4:7], a[192:195], a[144:147], v[132:135]
ds_read_b128 a[212:215], v240 offset:5120
v_mfma_f32_16x16x32_bf16 v[4:7], a[196:199], a[148:151], v[4:7]
buffer_load_dwordx4 v252, s[40:43], s82 offen offset:0 sc1 lds
v_mfma_f32_16x16x32_bf16 v[4:7], a[200:203], a[152:155], v[4:7]
ds_read_b128 a[216:219], v240 offset:6144
v_mfma_f32_16x16x32_bf16 v[4:7], a[204:207], a[156:159], v[4:7]
buffer_load_dwordx4 v252, s[40:43], s82 offen offset:1024 sc1 lds
v_mfma_f32_16x16x32_bf16 v[8:11], a[192:195], a[160:163], v[136:139]
v_exp_f32 v0, v0
v_mfma_f32_16x16x32_bf16 v[8:11], a[196:199], a[164:167], v[8:11]
v_mfma_f32_16x16x32_bf16 v[8:11], a[200:203], a[168:171], v[8:11]
v_exp_f32 v1, v1
v_mfma_f32_16x16x32_bf16 v[8:11], a[204:207], a[172:175], v[8:11]
buffer_load_dwordx4 v252, s[40:43], s82 offen offset:2048 sc1 lds
ds_read_b128 a[220:223], v240 offset:7168
v_mfma_f32_16x16x32_bf16 v[12:15], a[192:195], a[176:179], v[140:143]
v_exp_f32 v2, v2
v_mfma_f32_16x16x32_bf16 v[12:15], a[196:199], a[180:183], v[12:15]
buffer_load_dwordx4 v252, s[40:43], s82 offen offset:3072 sc1 lds
v_mfma_f32_16x16x32_bf16 v[12:15], a[200:203], a[184:187], v[12:15]
v_exp_f32 v3, v3
v_mfma_f32_16x16x32_bf16 v[12:15], a[204:207], a[188:191], v[12:15]
v_cvt_pk_bf16_f32 v64, v0, v1
v_cvt_pk_bf16_f32 v65, v2, v3
s_waitcnt lgkmcnt(3)
v_mfma_f32_16x16x32_bf16 v[16:19], a[208:211], a[128:131], v[128:131]
v_exp_f32 v4, v4
s_waitcnt lgkmcnt(2)
v_mfma_f32_16x16x32_bf16 v[16:19], a[212:215], a[132:135], v[16:19]
ds_read_b128 a[192:195], v240 offset:8192
s_waitcnt lgkmcnt(2)
v_mfma_f32_16x16x32_bf16 v[16:19], a[216:219], a[136:139], v[16:19]
v_exp_f32 v5, v5
s_add_u32 m0, s54, 4096
s_add_u32 s82, s74, 4096
s_waitcnt lgkmcnt(1)
v_mfma_f32_16x16x32_bf16 v[16:19], a[220:223], a[140:143], v[16:19]
ds_read_b128 a[196:199], v240 offset:9216
v_cvt_pk_bf16_f32 v72, v4, v5
v_mfma_f32_16x16x32_bf16 v[20:23], a[208:211], a[144:147], v[132:135]
v_exp_f32 v6, v6
v_mfma_f32_16x16x32_bf16 v[20:23], a[212:215], a[148:151], v[20:23]
buffer_load_dwordx4 v252, s[40:43], s82 offen offset:0 sc1 lds
v_mfma_f32_16x16x32_bf16 v[20:23], a[216:219], a[152:155], v[20:23]
v_exp_f32 v7, v7
v_mfma_f32_16x16x32_bf16 v[20:23], a[220:223], a[156:159], v[20:23]
buffer_load_dwordx4 v252, s[40:43], s82 offen offset:1024 sc1 lds
v_mfma_f32_16x16x32_bf16 v[24:27], a[208:211], a[160:163], v[136:139]
v_exp_f32 v8, v8
v_mfma_f32_16x16x32_bf16 v[24:27], a[212:215], a[164:167], v[24:27]
ds_read_b128 a[200:203], v240 offset:10240
v_cvt_pk_bf16_f32 v73, v6, v7
v_mfma_f32_16x16x32_bf16 v[24:27], a[216:219], a[168:171], v[24:27]
v_exp_f32 v9, v9
v_mfma_f32_16x16x32_bf16 v[24:27], a[220:223], a[172:175], v[24:27]
buffer_load_dwordx4 v252, s[40:43], s82 offen offset:2048 sc1 lds
ds_read_b128 a[204:207], v240 offset:11264
v_mfma_f32_16x16x32_bf16 v[28:31], a[208:211], a[176:179], v[140:143]
v_exp_f32 v10, v10
v_mfma_f32_16x16x32_bf16 v[28:31], a[212:215], a[180:183], v[28:31]
buffer_load_dwordx4 v252, s[40:43], s82 offen offset:3072 sc1 lds
v_mfma_f32_16x16x32_bf16 v[28:31], a[216:219], a[184:187], v[28:31]
v_exp_f32 v11, v11
v_mfma_f32_16x16x32_bf16 v[28:31], a[220:223], a[188:191], v[28:31]
v_cvt_pk_bf16_f32 v80, v8, v9
v_cvt_pk_bf16_f32 v81, v10, v11
s_waitcnt lgkmcnt(3)
v_mfma_f32_16x16x32_bf16 v[32:35], a[192:195], a[128:131], v[128:131]
v_exp_f32 v12, v12
s_waitcnt lgkmcnt(2)
v_mfma_f32_16x16x32_bf16 v[32:35], a[196:199], a[132:135], v[32:35]
ds_read_b128 a[208:211], v240 offset:12288
s_waitcnt lgkmcnt(2)
v_mfma_f32_16x16x32_bf16 v[32:35], a[200:203], a[136:139], v[32:35]
v_exp_f32 v13, v13
s_add_u32 m0, s55, 0
s_add_u32 s82, s52, 0
s_waitcnt lgkmcnt(1)
v_mfma_f32_16x16x32_bf16 v[32:35], a[204:207], a[140:143], v[32:35]
ds_read_b128 a[212:215], v240 offset:13312
v_cvt_pk_bf16_f32 v88, v12, v13
v_mfma_f32_16x16x32_bf16 v[36:39], a[192:195], a[144:147], v[132:135]
v_exp_f32 v14, v14
v_mfma_f32_16x16x32_bf16 v[36:39], a[196:199], a[148:151], v[36:39]
buffer_load_dwordx4 v252, s[36:39], s82 offen offset:0 sc1 lds
v_mfma_f32_16x16x32_bf16 v[36:39], a[200:203], a[152:155], v[36:39]
v_exp_f32 v15, v15
v_mfma_f32_16x16x32_bf16 v[36:39], a[204:207], a[156:159], v[36:39]
buffer_load_dwordx4 v252, s[36:39], s82 offen offset:1024 sc1 lds
)ASM"
R"ASM(v_mfma_f32_16x16x32_bf16 v[40:43], a[192:195], a[160:163], v[136:139]
v_exp_f32 v16, v16
v_mfma_f32_16x16x32_bf16 v[40:43], a[196:199], a[164:167], v[40:43]
ds_read_b128 a[216:219], v240 offset:14336
v_cvt_pk_bf16_f32 v89, v14, v15
v_mfma_f32_16x16x32_bf16 v[40:43], a[200:203], a[168:171], v[40:43]
v_exp_f32 v17, v17
v_mfma_f32_16x16x32_bf16 v[40:43], a[204:207], a[172:175], v[40:43]
buffer_load_dwordx4 v252, s[36:39], s82 offen offset:2048 sc1 lds
ds_read_b128 a[220:223], v240 offset:15360
v_mfma_f32_16x16x32_bf16 v[44:47], a[192:195], a[176:179], v[140:143]
v_exp_f32 v18, v18
v_mfma_f32_16x16x32_bf16 v[44:47], a[196:199], a[180:183], v[44:47]
buffer_load_dwordx4 v252, s[36:39], s82 offen offset:3072 sc1 lds
v_mfma_f32_16x16x32_bf16 v[44:47], a[200:203], a[184:187], v[44:47]
v_exp_f32 v19, v19
v_mfma_f32_16x16x32_bf16 v[44:47], a[204:207], a[188:191], v[44:47]
ds_read_b128 a[224:227], v241 offset:0
v_cvt_pk_bf16_f32 v66, v16, v17
s_waitcnt lgkmcnt(4)
v_mfma_f32_16x16x32_bf16 v[48:51], a[208:211], a[128:131], v[128:131]
v_exp_f32 v20, v20
s_waitcnt lgkmcnt(3)
v_mfma_f32_16x16x32_bf16 v[48:51], a[212:215], a[132:135], v[48:51]
v_cvt_pk_bf16_f32 v67, v18, v19
s_waitcnt lgkmcnt(2)
v_mfma_f32_16x16x32_bf16 v[48:51], a[216:219], a[136:139], v[48:51]
v_exp_f32 v21, v21
s_add_u32 m0, s55, 4096
s_add_u32 s82, s52, 4096
s_waitcnt lgkmcnt(1)
v_mfma_f32_16x16x32_bf16 v[48:51], a[220:223], a[140:143], v[48:51]
ds_read_b128 a[228:231], v241 offset:8192
v_cvt_pk_bf16_f32 v74, v20, v21
v_mfma_f32_16x16x32_bf16 v[52:55], a[208:211], a[144:147], v[132:135]
v_exp_f32 v22, v22
v_mfma_f32_16x16x32_bf16 v[52:55], a[212:215], a[148:151], v[52:55]
buffer_load_dwordx4 v252, s[36:39], s82 offen offset:0 sc1 lds
v_mfma_f32_16x16x32_bf16 v[52:55], a[216:219], a[152:155], v[52:55]
v_exp_f32 v23, v23
v_mfma_f32_16x16x32_bf16 v[52:55], a[220:223], a[156:159], v[52:55]
buffer_load_dwordx4 v252, s[36:39], s82 offen offset:1024 sc1 lds
v_mfma_f32_16x16x32_bf16 v[56:59], a[208:211], a[160:163], v[136:139]
v_exp_f32 v24, v24
v_mfma_f32_16x16x32_bf16 v[56:59], a[212:215], a[164:167], v[56:59]
ds_read_b128 a[232:235], v241 offset:1024
v_cvt_pk_bf16_f32 v75, v22, v23
v_mfma_f32_16x16x32_bf16 v[56:59], a[216:219], a[168:171], v[56:59]
v_exp_f32 v25, v25
v_mfma_f32_16x16x32_bf16 v[56:59], a[220:223], a[172:175], v[56:59]
buffer_load_dwordx4 v252, s[36:39], s82 offen offset:2048 sc1 lds
ds_read_b128 a[236:239], v241 offset:9216
v_mfma_f32_16x16x32_bf16 v[60:63], a[208:211], a[176:179], v[140:143]
v_exp_f32 v26, v26
v_mfma_f32_16x16x32_bf16 v[60:63], a[212:215], a[180:183], v[60:63]
v_cvt_pk_bf16_f32 v82, v24, v25
v_mfma_f32_16x16x32_bf16 v[60:63], a[216:219], a[184:187], v[60:63]
v_exp_f32 v27, v27
v_mfma_f32_16x16x32_bf16 v[60:63], a[220:223], a[188:191], v[60:63]
buffer_load_dwordx4 v252, s[36:39], s82 offen offset:3072 sc1 lds
s_sub_u32 s51, s51, 32768
s_waitcnt lgkmcnt(3)
v_mfma_f32_16x16x32_bf16 a[0:3], a[224:227], v[96:99], a[0:3]
v_exp_f32 v28, v28
s_sub_u32 s52, s52, 32768
s_xor_b32 s54, s54, 32768
s_waitcnt lgkmcnt(2)
v_mfma_f32_16x16x32_bf16 a[0:3], a[228:231], v[100:103], a[0:3]
s_xor_b32 s55, s55, 32768
ds_read_b128 a[240:243], v241 offset:2048
v_cvt_pk_bf16_f32 v83, v26, v27
v_mfma_f32_16x16x32_bf16 a[4:7], a[224:227], v[104:107], a[4:7]
v_exp_f32 v29, v29
v_mfma_f32_16x16x32_bf16 a[4:7], a[228:231], v[108:111], a[4:7]
v_cvt_pk_bf16_f32 v90, v28, v29
v_mfma_f32_16x16x32_bf16 a[8:11], a[224:227], v[112:115], a[8:11]
v_exp_f32 v30, v30
v_mfma_f32_16x16x32_bf16 a[8:11], a[228:231], v[116:119], a[8:11]
ds_read_b128 a[244:247], v241 offset:10240
v_mfma_f32_16x16x32_bf16 a[12:15], a[224:227], v[120:123], a[12:15]
v_exp_f32 v31, v31
v_mfma_f32_16x16x32_bf16 a[12:15], a[228:231], v[124:127], a[12:15]
v_cvt_pk_bf16_f32 v91, v30, v31
s_waitcnt lgkmcnt(3)
v_mfma_f32_16x16x32_bf16 a[16:19], a[232:235], v[96:99], a[16:19]
v_exp_f32 v32, v32
s_waitcnt lgkmcnt(2)
v_mfma_f32_16x16x32_bf16 a[16:19], a[236:239], v[100:103], a[16:19]
ds_read_b128 a[248:251], v241 offset:3072
v_mfma_f32_16x16x32_bf16 a[20:23], a[232:235], v[104:107], a[20:23]
v_exp_f32 v33, v33
v_mfma_f32_16x16x32_bf16 a[20:23], a[236:239], v[108:111], a[20:23]
v_cvt_pk_bf16_f32 v68, v32, v33
v_mfma_f32_16x16x32_bf16 a[24:27], a[232:235], v[112:115], a[24:27]
v_exp_f32 v34, v34
v_mfma_f32_16x16x32_bf16 a[24:27], a[236:239], v[116:119], a[24:27]
ds_read_b128 a[252:255], v241 offset:11264
v_mfma_f32_16x16x32_bf16 a[28:31], a[232:235], v[120:123], a[28:31]
v_exp_f32 v35, v35
v_mfma_f32_16x16x32_bf16 a[28:31], a[236:239], v[124:127], a[28:31]
v_cvt_pk_bf16_f32 v69, v34, v35
s_waitcnt lgkmcnt(3)
v_mfma_f32_16x16x32_bf16 a[32:35], a[240:243], v[96:99], a[32:35]
v_exp_f32 v36, v36
s_waitcnt lgkmcnt(2)
v_mfma_f32_16x16x32_bf16 a[32:35], a[244:247], v[100:103], a[32:35]
ds_read_b128 a[224:227], v241 offset:4096
v_mfma_f32_16x16x32_bf16 a[36:39], a[240:243], v[104:107], a[36:39]
v_exp_f32 v37, v37
v_mfma_f32_16x16x32_bf16 a[36:39], a[244:247], v[108:111], a[36:39]
v_cvt_pk_bf16_f32 v76, v36, v37
v_mfma_f32_16x16x32_bf16 a[40:43], a[240:243], v[112:115], a[40:43]
v_exp_f32 v38, v38
v_mfma_f32_16x16x32_bf16 a[40:43], a[244:247], v[116:119], a[40:43]
ds_read_b128 a[228:231], v241 offset:12288
v_mfma_f32_16x16x32_bf16 a[44:47], a[240:243], v[120:123], a[44:47]
v_exp_f32 v39, v39
v_mfma_f32_16x16x32_bf16 a[44:47], a[244:247], v[124:127], a[44:47]
v_cvt_pk_bf16_f32 v77, v38, v39
s_waitcnt lgkmcnt(3)
v_mfma_f32_16x16x32_bf16 a[48:51], a[248:251], v[96:99], a[48:51]
v_exp_f32 v40, v40
s_waitcnt lgkmcnt(2)
v_mfma_f32_16x16x32_bf16 a[48:51], a[252:255], v[100:103], a[48:51]
ds_read_b128 a[232:235], v241 offset:5120
v_mfma_f32_16x16x32_bf16 a[52:55], a[248:251], v[104:107], a[52:55]
v_exp_f32 v41, v41
v_mfma_f32_16x16x32_bf16 a[52:55], a[252:255], v[108:111], a[52:55]
v_cvt_pk_bf16_f32 v84, v40, v41
v_mfma_f32_16x16x32_bf16 a[56:59], a[248:251], v[112:115], a[56:59]
v_exp_f32 v42, v42
v_mfma_f32_16x16x32_bf16 a[56:59], a[252:255], v[116:119], a[56:59]
ds_read_b128 a[236:239], v241 offset:13312
v_mfma_f32_16x16x32_bf16 a[60:63], a[248:251], v[120:123], a[60:63]
v_exp_f32 v43, v43
v_mfma_f32_16x16x32_bf16 a[60:63], a[252:255], v[124:127], a[60:63]
v_cvt_pk_bf16_f32 v85, v42, v43
s_waitcnt lgkmcnt(3)
v_mfma_f32_16x16x32_bf16 a[64:67], a[224:227], v[96:99], a[64:67]
v_exp_f32 v44, v44
s_waitcnt lgkmcnt(2)
v_mfma_f32_16x16x32_bf16 a[64:67], a[228:231], v[100:103], a[64:67]
ds_read_b128 a[240:243], v241 offset:6144
v_mfma_f32_16x16x32_bf16 a[68:71], a[224:227], v[104:107], a[68:71]
v_exp_f32 v45, v45
v_mfma_f32_16x16x32_bf16 a[68:71], a[228:231], v[108:111], a[68:71]
v_cvt_pk_bf16_f32 v92, v44, v45
v_mfma_f32_16x16x32_bf16 a[72:75], a[224:227], v[112:115], a[72:75]
v_exp_f32 v46, v46
v_mfma_f32_16x16x32_bf16 a[72:75], a[228:231], v[116:119], a[72:75]
ds_read_b128 a[244:247], v241 offset:14336
v_mfma_f32_16x16x32_bf16 a[76:79], a[224:227], v[120:123], a[76:79]
v_exp_f32 v47, v47
v_mfma_f32_16x16x32_bf16 a[76:79], a[228:231], v[124:127], a[76:79]
v_cvt_pk_bf16_f32 v93, v46, v47
s_waitcnt lgkmcnt(3)
v_mfma_f32_16x16x32_bf16 a[80:83], a[232:235], v[96:99], a[80:83]
v_exp_f32 v48, v48
s_waitcnt lgkmcnt(2)
v_mfma_f32_16x16x32_bf16 a[80:83], a[236:239], v[100:103], a[80:83]
ds_read_b128 a[248:251], v241 offset:7168
v_mfma_f32_16x16x32_bf16 a[84:87], a[232:235], v[104:107], a[84:87]
v_exp_f32 v49, v49
v_mfma_f32_16x16x32_bf16 a[84:87], a[236:239], v[108:111], a[84:87]
v_cvt_pk_bf16_f32 v70, v48, v49
v_mfma_f32_16x16x32_bf16 a[88:91], a[232:235], v[112:115], a[88:91]
v_exp_f32 v50, v50
v_mfma_f32_16x16x32_bf16 a[88:91], a[236:239], v[116:119], a[88:91]
ds_read_b128 a[252:255], v241 offset:15360
v_mfma_f32_16x16x32_bf16 a[92:95], a[232:235], v[120:123], a[92:95]
v_exp_f32 v51, v51
v_mfma_f32_16x16x32_bf16 a[92:95], a[236:239], v[124:127], a[92:95]
v_cvt_pk_bf16_f32 v71, v50, v51
s_waitcnt lgkmcnt(3)
v_mfma_f32_16x16x32_bf16 a[96:99], a[240:243], v[96:99], a[96:99]
v_exp_f32 v52, v52
s_waitcnt lgkmcnt(2)
v_mfma_f32_16x16x32_bf16 a[96:99], a[244:247], v[100:103], a[96:99]
v_mfma_f32_16x16x32_bf16 a[100:103], a[240:243], v[104:107], a[100:103]
v_exp_f32 v53, v53
v_xor_b32 v241, 0x8000, v241
v_mfma_f32_16x16x32_bf16 a[100:103], a[244:247], v[108:111], a[100:103]
v_cvt_pk_bf16_f32 v78, v52, v53
s_waitcnt vmcnt(0) lgkmcnt(0)
s_barrier
v_xor_b32 v240, 0x8000, v240
v_mfma_f32_16x16x32_bf16 a[104:107], a[240:243], v[112:115], a[104:107]
v_exp_f32 v54, v54
v_mfma_f32_16x16x32_bf16 a[104:107], a[244:247], v[116:119], a[104:107]
ds_read_b128 a[192:195], v240 offset:0
v_mfma_f32_16x16x32_bf16 a[108:111], a[240:243], v[120:123], a[108:111]
v_exp_f32 v55, v55
v_mfma_f32_16x16x32_bf16 a[108:111], a[244:247], v[124:127], a[108:111]
ds_read_b128 a[196:199], v240 offset:1024
v_cvt_pk_bf16_f32 v79, v54, v55
v_mfma_f32_16x16x32_bf16 a[112:115], a[248:251], v[96:99], a[112:115]
v_exp_f32 v56, v56
v_mfma_f32_16x16x32_bf16 a[112:115], a[252:255], v[100:103], a[112:115]
v_mfma_f32_16x16x32_bf16 a[116:119], a[248:251], v[104:107], a[116:119]
v_exp_f32 v57, v57
v_mfma_f32_16x16x32_bf16 a[116:119], a[252:255], v[108:111], a[116:119]
ds_read_b128 a[200:203], v240 offset:2048
v_cvt_pk_bf16_f32 v86, v56, v57
v_mfma_f32_16x16x32_bf16 a[120:123], a[248:251], v[112:115], a[120:123]
v_exp_f32 v58, v58
v_mfma_f32_16x16x32_bf16 a[120:123], a[252:255], v[116:119], a[120:123]
v_mfma_f32_16x16x32_bf16 a[124:127], a[248:251], v[120:123], a[124:127]
v_exp_f32 v59, v59
v_mfma_f32_16x16x32_bf16 a[124:127], a[252:255], v[124:127], a[124:127]
ds_read_b128 a[204:207], v240 offset:3072
v_cvt_pk_bf16_f32 v87, v58, v59
v_mfma_f32_16x16x32_bf16 v[144:147], v[160:163], v[96:99], v[144:147]
v_exp_f32 v60, v60
v_mfma_f32_16x16x32_bf16 v[144:147], v[160:163], v[100:103], v[144:147]
v_mfma_f32_16x16x32_bf16 v[148:151], v[160:163], v[104:107], v[148:151]
v_exp_f32 v61, v61
v_mfma_f32_16x16x32_bf16 v[148:151], v[160:163], v[108:111], v[148:151]
v_cvt_pk_bf16_f32 v94, v60, v61
v_mfma_f32_16x16x32_bf16 v[152:155], v[160:163], v[112:115], v[152:155]
v_exp_f32 v62, v62
v_mfma_f32_16x16x32_bf16 v[152:155], v[160:163], v[116:119], v[152:155]
v_mfma_f32_16x16x32_bf16 v[156:159], v[160:163], v[120:123], v[156:159]
v_exp_f32 v63, v63
v_mfma_f32_16x16x32_bf16 v[156:159], v[160:163], v[124:127], v[156:159]
v_cvt_pk_bf16_f32 v95, v62, v63
s_cmp_lg_u32 s50, 0
s_cbranch_scc1 mla_fused_b_loop
mla_fused_b_drain0:
ds_read_b128 a[224:227], v241 offset:0
ds_read_b128 a[228:231], v241 offset:8192
ds_read_b128 a[232:235], v241 offset:1024
ds_read_b128 a[236:239], v241 offset:9216
s_cmp_lg_u32 s59, 0
s_cbranch_scc1 mla_fused_f_ww7
v_and_b32 v219, 63, %[tid]
v_lshlrev_b32 v219, 7, v219
s_bfe_u32 s82, s89, 0x20001
s_lshr_b32 s83, s89, 3
s_mov_b64 s[44:45], %[wo]
s_and_b32 s45, s45, 0xffff
s_mov_b32 s46, 0x400000
s_mov_b32 s47, 0x20000
s_lshl_b32 s84, s82, 20
s_lshl_b32 s85, s83, 15
s_add_u32 s84, s84, s85
s_lshl_b32 s85, s68, 13
s_add_u32 s84, s84, s85
buffer_load_dword v224, v219, s[44:47], s84 offen
s_cmp_ge_u32 s68, 2
s_cbranch_scc1 mla_fused_f_ww7
s_mov_b64 s[44:45], %[w2]
s_and_b32 s45, s45, 0xffff
s_mov_b32 s46, 0x1c0000
s_mov_b32 s47, 0x20000
s_mul_i32 s84, s82, 458752
s_mul_i32 s85, s83, 14336
s_add_u32 s84, s84, s85
s_lshl_b32 s85, s68, 13
s_add_u32 s84, s84, s85
buffer_load_dword v225, v219, s[44:47], s84 offen
mla_fused_f_ww7:
s_sub_u32 s82, s54, s75
s_sub_u32 s83, s55, s75
s_sub_u32 s83, s83, 32768
s_cmp_eq_u32 s69, 0
s_cselect_b32 s84, s82, s83
s_cselect_b32 s85, s83, s82
s_lshl_b32 s82, s68, 14
s_add_u32 s84, s84, s82
v_add_u32 v243, s84, v252
s_xor_b32 s82, s82, 32768
s_add_u32 s85, s85, s82
v_add_u32 v244, s85, v252
s_waitcnt lgkmcnt(3)
v_mfma_f32_16x16x32_bf16 a[0:3], a[224:227], v[64:67], a[0:3]
s_waitcnt lgkmcnt(2)
v_mfma_f32_16x16x32_bf16 a[0:3], a[228:231], v[68:71], a[0:3]
ds_read_b128 a[240:243], v241 offset:2048
v_mfma_f32_16x16x32_bf16 a[4:7], a[224:227], v[72:75], a[4:7]
ds_read_b128 a[128:131], v230 offset:0
v_mfma_f32_16x16x32_bf16 a[4:7], a[228:231], v[76:79], a[4:7]
v_mfma_f32_16x16x32_bf16 a[8:11], a[224:227], v[80:83], a[8:11]
v_mfma_f32_16x16x32_bf16 a[8:11], a[228:231], v[84:87], a[8:11]
)ASM"
R"ASM(ds_read_b128 a[244:247], v241 offset:10240
ds_read_b128 a[132:135], v230 offset:1024
v_mfma_f32_16x16x32_bf16 a[12:15], a[224:227], v[88:91], a[12:15]
v_mfma_f32_16x16x32_bf16 a[12:15], a[228:231], v[92:95], a[12:15]
s_waitcnt lgkmcnt(5)
v_mfma_f32_16x16x32_bf16 a[16:19], a[232:235], v[64:67], a[16:19]
ds_read_b128 a[136:139], v230 offset:2048
v_accvgpr_read_b32 v0, a0
v_accvgpr_read_b32 v1, a1
s_waitcnt lgkmcnt(5)
v_mfma_f32_16x16x32_bf16 a[16:19], a[236:239], v[68:71], a[16:19]
ds_read_b128 a[248:251], v241 offset:3072
v_accvgpr_read_b32 v2, a2
v_accvgpr_read_b32 v3, a3
v_mfma_f32_16x16x32_bf16 a[20:23], a[232:235], v[72:75], a[20:23]
v_accvgpr_read_b32 v4, a4
v_accvgpr_read_b32 v5, a5
v_mfma_f32_16x16x32_bf16 a[20:23], a[236:239], v[76:79], a[20:23]
ds_read_b128 a[140:143], v230 offset:3072
v_accvgpr_read_b32 v6, a6
v_accvgpr_read_b32 v7, a7
v_mfma_f32_16x16x32_bf16 a[24:27], a[232:235], v[80:83], a[24:27]
ds_write_b128 v243, a[8:11] offset:0
v_mfma_f32_16x16x32_bf16 a[24:27], a[236:239], v[84:87], a[24:27]
ds_read_b128 a[252:255], v241 offset:11264
v_mfma_f32_16x16x32_bf16 a[28:31], a[232:235], v[88:91], a[28:31]
ds_read_b128 a[144:147], v230 offset:4096
ds_write_b128 v243, a[12:15] offset:1024
v_mfma_f32_16x16x32_bf16 a[28:31], a[236:239], v[92:95], a[28:31]
s_waitcnt lgkmcnt(10)
v_mfma_f32_16x16x32_bf16 a[32:35], a[240:243], v[64:67], a[32:35]
v_accvgpr_read_b32 v8, a16
v_accvgpr_read_b32 v9, a17
s_waitcnt lgkmcnt(8)
v_mfma_f32_16x16x32_bf16 a[32:35], a[244:247], v[68:71], a[32:35]
ds_read_b128 a[224:227], v241 offset:4096
ds_read_b128 a[148:151], v230 offset:5120
v_accvgpr_read_b32 v10, a18
v_accvgpr_read_b32 v11, a19
v_mfma_f32_16x16x32_bf16 a[36:39], a[240:243], v[72:75], a[36:39]
v_accvgpr_read_b32 v12, a20
v_accvgpr_read_b32 v13, a21
v_mfma_f32_16x16x32_bf16 a[36:39], a[244:247], v[76:79], a[36:39]
v_accvgpr_read_b32 v14, a22
v_accvgpr_read_b32 v15, a23
v_mfma_f32_16x16x32_bf16 a[40:43], a[240:243], v[80:83], a[40:43]
ds_read_b128 a[152:155], v230 offset:6144
ds_write_b128 v243, a[24:27] offset:2048
v_mfma_f32_16x16x32_bf16 a[40:43], a[244:247], v[84:87], a[40:43]
ds_read_b128 a[228:231], v241 offset:12288
v_mfma_f32_16x16x32_bf16 a[44:47], a[240:243], v[88:91], a[44:47]
ds_write_b128 v243, a[28:31] offset:3072
v_mfma_f32_16x16x32_bf16 a[44:47], a[244:247], v[92:95], a[44:47]
ds_read_b128 a[156:159], v230 offset:7168
s_waitcnt lgkmcnt(12)
v_mfma_f32_16x16x32_bf16 a[48:51], a[248:251], v[64:67], a[48:51]
v_accvgpr_read_b32 v16, a32
v_accvgpr_read_b32 v17, a33
s_waitcnt lgkmcnt(9)
v_mfma_f32_16x16x32_bf16 a[48:51], a[252:255], v[68:71], a[48:51]
ds_read_b128 a[232:235], v241 offset:5120
v_accvgpr_read_b32 v18, a34
v_accvgpr_read_b32 v19, a35
v_mfma_f32_16x16x32_bf16 a[52:55], a[248:251], v[72:75], a[52:55]
ds_read_b128 a[160:163], v231 offset:0
v_accvgpr_read_b32 v20, a36
v_accvgpr_read_b32 v21, a37
v_mfma_f32_16x16x32_bf16 a[52:55], a[252:255], v[76:79], a[52:55]
v_accvgpr_read_b32 v22, a38
v_accvgpr_read_b32 v23, a39
v_mfma_f32_16x16x32_bf16 a[56:59], a[248:251], v[80:83], a[56:59]
ds_write_b128 v243, a[40:43] offset:4096
v_mfma_f32_16x16x32_bf16 a[56:59], a[252:255], v[84:87], a[56:59]
ds_read_b128 a[236:239], v241 offset:13312
ds_read_b128 a[164:167], v231 offset:1024
v_mfma_f32_16x16x32_bf16 a[60:63], a[248:251], v[88:91], a[60:63]
ds_write_b128 v243, a[44:47] offset:5120
v_mfma_f32_16x16x32_bf16 a[60:63], a[252:255], v[92:95], a[60:63]
s_waitcnt lgkmcnt(12)
v_mfma_f32_16x16x32_bf16 a[64:67], a[224:227], v[64:67], a[64:67]
ds_read_b128 a[168:171], v231 offset:2048
v_accvgpr_read_b32 v24, a48
v_accvgpr_read_b32 v25, a49
s_waitcnt lgkmcnt(9)
v_mfma_f32_16x16x32_bf16 a[64:67], a[228:231], v[68:71], a[64:67]
ds_read_b128 a[240:243], v241 offset:6144
v_accvgpr_read_b32 v26, a50
v_accvgpr_read_b32 v27, a51
v_mfma_f32_16x16x32_bf16 a[68:71], a[224:227], v[72:75], a[68:71]
v_accvgpr_read_b32 v28, a52
v_accvgpr_read_b32 v29, a53
v_mfma_f32_16x16x32_bf16 a[68:71], a[228:231], v[76:79], a[68:71]
ds_read_b128 a[172:175], v231 offset:3072
v_accvgpr_read_b32 v30, a54
v_accvgpr_read_b32 v31, a55
v_mfma_f32_16x16x32_bf16 a[72:75], a[224:227], v[80:83], a[72:75]
ds_write_b128 v243, a[56:59] offset:6144
v_mfma_f32_16x16x32_bf16 a[72:75], a[228:231], v[84:87], a[72:75]
ds_read_b128 a[244:247], v241 offset:14336
v_mfma_f32_16x16x32_bf16 a[76:79], a[224:227], v[88:91], a[76:79]
ds_read_b128 a[176:179], v231 offset:4096
ds_write_b128 v243, a[60:63] offset:7168
v_mfma_f32_16x16x32_bf16 a[76:79], a[228:231], v[92:95], a[76:79]
s_waitcnt lgkmcnt(12)
v_mfma_f32_16x16x32_bf16 a[80:83], a[232:235], v[64:67], a[80:83]
v_accvgpr_read_b32 v32, a64
v_accvgpr_read_b32 v33, a65
s_waitcnt lgkmcnt(9)
v_mfma_f32_16x16x32_bf16 a[80:83], a[236:239], v[68:71], a[80:83]
ds_read_b128 a[248:251], v241 offset:7168
ds_read_b128 a[180:183], v231 offset:5120
v_accvgpr_read_b32 v34, a66
v_accvgpr_read_b32 v35, a67
v_mfma_f32_16x16x32_bf16 a[84:87], a[232:235], v[72:75], a[84:87]
v_accvgpr_read_b32 v36, a68
v_accvgpr_read_b32 v37, a69
v_mfma_f32_16x16x32_bf16 a[84:87], a[236:239], v[76:79], a[84:87]
v_accvgpr_read_b32 v38, a70
v_accvgpr_read_b32 v39, a71
v_mfma_f32_16x16x32_bf16 a[88:91], a[232:235], v[80:83], a[88:91]
ds_read_b128 a[184:187], v231 offset:6144
ds_write_b128 v243, a[72:75] offset:8192
v_mfma_f32_16x16x32_bf16 a[88:91], a[236:239], v[84:87], a[88:91]
ds_read_b128 a[252:255], v241 offset:15360
v_mfma_f32_16x16x32_bf16 a[92:95], a[232:235], v[88:91], a[92:95]
ds_write_b128 v243, a[76:79] offset:9216
v_mfma_f32_16x16x32_bf16 a[92:95], a[236:239], v[92:95], a[92:95]
ds_read_b128 a[188:191], v231 offset:7168
s_waitcnt lgkmcnt(12)
v_mfma_f32_16x16x32_bf16 a[96:99], a[240:243], v[64:67], a[96:99]
v_accvgpr_read_b32 v40, a80
v_accvgpr_read_b32 v41, a81
s_waitcnt lgkmcnt(9)
v_mfma_f32_16x16x32_bf16 a[96:99], a[244:247], v[68:71], a[96:99]
v_accvgpr_read_b32 v42, a82
v_accvgpr_read_b32 v43, a83
v_mfma_f32_16x16x32_bf16 a[100:103], a[240:243], v[72:75], a[100:103]
v_accvgpr_read_b32 v44, a84
v_accvgpr_read_b32 v45, a85
v_xor_b32 v241, 0x8000, v241
v_mfma_f32_16x16x32_bf16 a[100:103], a[244:247], v[76:79], a[100:103]
v_accvgpr_read_b32 v46, a86
v_accvgpr_read_b32 v47, a87
v_mfma_f32_16x16x32_bf16 a[104:107], a[240:243], v[80:83], a[104:107]
ds_write_b128 v243, a[88:91] offset:10240
v_mfma_f32_16x16x32_bf16 a[104:107], a[244:247], v[84:87], a[104:107]
v_mfma_f32_16x16x32_bf16 a[108:111], a[240:243], v[88:91], a[108:111]
ds_write_b128 v243, a[92:95] offset:11264
v_mfma_f32_16x16x32_bf16 a[108:111], a[244:247], v[92:95], a[108:111]
s_waitcnt lgkmcnt(8)
v_mfma_f32_16x16x32_bf16 a[112:115], a[248:251], v[64:67], a[112:115]
v_accvgpr_read_b32 v48, a96
v_accvgpr_read_b32 v49, a97
s_waitcnt lgkmcnt(4)
v_mfma_f32_16x16x32_bf16 a[112:115], a[252:255], v[68:71], a[112:115]
v_accvgpr_read_b32 v50, a98
v_accvgpr_read_b32 v51, a99
v_mfma_f32_16x16x32_bf16 a[116:119], a[248:251], v[72:75], a[116:119]
v_accvgpr_read_b32 v52, a100
v_accvgpr_read_b32 v53, a101
v_mfma_f32_16x16x32_bf16 a[116:119], a[252:255], v[76:79], a[116:119]
v_accvgpr_read_b32 v54, a102
v_accvgpr_read_b32 v55, a103
v_mfma_f32_16x16x32_bf16 a[120:123], a[248:251], v[80:83], a[120:123]
ds_write_b128 v243, a[104:107] offset:12288
v_mfma_f32_16x16x32_bf16 a[120:123], a[252:255], v[84:87], a[120:123]
v_mfma_f32_16x16x32_bf16 a[124:127], a[248:251], v[88:91], a[124:127]
ds_write_b128 v243, a[108:111] offset:13312
v_mfma_f32_16x16x32_bf16 a[124:127], a[252:255], v[92:95], a[124:127]
v_mfma_f32_16x16x32_bf16 v[144:147], v[160:163], v[64:67], v[144:147]
v_accvgpr_read_b32 v56, a112
v_accvgpr_read_b32 v57, a113
v_mfma_f32_16x16x32_bf16 v[144:147], v[160:163], v[68:71], v[144:147]
v_accvgpr_read_b32 v58, a114
v_accvgpr_read_b32 v59, a115
v_mfma_f32_16x16x32_bf16 v[148:151], v[160:163], v[72:75], v[148:151]
v_accvgpr_read_b32 v60, a116
v_accvgpr_read_b32 v61, a117
v_mfma_f32_16x16x32_bf16 v[148:151], v[160:163], v[76:79], v[148:151]
v_accvgpr_read_b32 v62, a118
v_accvgpr_read_b32 v63, a119
v_mfma_f32_16x16x32_bf16 v[152:155], v[160:163], v[80:83], v[152:155]
ds_write_b128 v243, a[120:123] offset:14336
v_mfma_f32_16x16x32_bf16 v[152:155], v[160:163], v[84:87], v[152:155]
v_mfma_f32_16x16x32_bf16 v[156:159], v[160:163], v[88:91], v[156:159]
ds_write_b128 v243, a[124:127] offset:15360
v_mfma_f32_16x16x32_bf16 v[156:159], v[160:163], v[92:95], v[156:159]
s_waitcnt lgkmcnt(0)
s_barrier
s_nop 2
ds_write_b32 v249, v152 offset:1024
s_nop 1
ds_write_b32 v249, v156 offset:2048
s_branch mla_fused_b_comb
mla_fused_b_drain1:
ds_read_b128 a[224:227], v241 offset:0
ds_read_b128 a[228:231], v241 offset:8192
ds_read_b128 a[232:235], v241 offset:1024
ds_read_b128 a[236:239], v241 offset:9216
s_cmp_lg_u32 s59, 0
s_cbranch_scc1 mla_fused_f_ww8
v_and_b32 v219, 63, %[tid]
v_lshlrev_b32 v219, 7, v219
s_bfe_u32 s82, s89, 0x20001
s_lshr_b32 s83, s89, 3
s_mov_b64 s[44:45], %[wo]
s_and_b32 s45, s45, 0xffff
s_mov_b32 s46, 0x400000
s_mov_b32 s47, 0x20000
s_lshl_b32 s84, s82, 20
s_lshl_b32 s85, s83, 15
s_add_u32 s84, s84, s85
s_lshl_b32 s85, s68, 13
s_add_u32 s84, s84, s85
buffer_load_dword v224, v219, s[44:47], s84 offen
s_cmp_ge_u32 s68, 2
s_cbranch_scc1 mla_fused_f_ww8
s_mov_b64 s[44:45], %[w2]
s_and_b32 s45, s45, 0xffff
s_mov_b32 s46, 0x1c0000
s_mov_b32 s47, 0x20000
s_mul_i32 s84, s82, 458752
s_mul_i32 s85, s83, 14336
s_add_u32 s84, s84, s85
s_lshl_b32 s85, s68, 13
s_add_u32 s84, s84, s85
buffer_load_dword v225, v219, s[44:47], s84 offen
mla_fused_f_ww8:
s_sub_u32 s82, s54, s75
s_sub_u32 s83, s55, s75
s_sub_u32 s83, s83, 32768
s_cmp_eq_u32 s69, 0
s_cselect_b32 s84, s82, s83
s_cselect_b32 s85, s83, s82
s_lshl_b32 s82, s68, 14
s_add_u32 s84, s84, s82
v_add_u32 v243, s84, v252
s_xor_b32 s82, s82, 32768
s_add_u32 s85, s85, s82
v_add_u32 v244, s85, v252
s_waitcnt lgkmcnt(3)
v_mfma_f32_16x16x32_bf16 a[0:3], a[224:227], v[96:99], a[0:3]
s_waitcnt lgkmcnt(2)
v_mfma_f32_16x16x32_bf16 a[0:3], a[228:231], v[100:103], a[0:3]
ds_read_b128 a[240:243], v241 offset:2048
v_mfma_f32_16x16x32_bf16 a[4:7], a[224:227], v[104:107], a[4:7]
ds_read_b128 a[128:131], v230 offset:0
v_mfma_f32_16x16x32_bf16 a[4:7], a[228:231], v[108:111], a[4:7]
v_mfma_f32_16x16x32_bf16 a[8:11], a[224:227], v[112:115], a[8:11]
v_mfma_f32_16x16x32_bf16 a[8:11], a[228:231], v[116:119], a[8:11]
ds_read_b128 a[244:247], v241 offset:10240
ds_read_b128 a[132:135], v230 offset:1024
v_mfma_f32_16x16x32_bf16 a[12:15], a[224:227], v[120:123], a[12:15]
v_mfma_f32_16x16x32_bf16 a[12:15], a[228:231], v[124:127], a[12:15]
s_waitcnt lgkmcnt(5)
v_mfma_f32_16x16x32_bf16 a[16:19], a[232:235], v[96:99], a[16:19]
ds_read_b128 a[136:139], v230 offset:2048
v_accvgpr_read_b32 v0, a0
v_accvgpr_read_b32 v1, a1
s_waitcnt lgkmcnt(5)
v_mfma_f32_16x16x32_bf16 a[16:19], a[236:239], v[100:103], a[16:19]
ds_read_b128 a[248:251], v241 offset:3072
v_accvgpr_read_b32 v2, a2
v_accvgpr_read_b32 v3, a3
v_mfma_f32_16x16x32_bf16 a[20:23], a[232:235], v[104:107], a[20:23]
v_accvgpr_read_b32 v4, a4
v_accvgpr_read_b32 v5, a5
v_mfma_f32_16x16x32_bf16 a[20:23], a[236:239], v[108:111], a[20:23]
ds_read_b128 a[140:143], v230 offset:3072
v_accvgpr_read_b32 v6, a6
v_accvgpr_read_b32 v7, a7
v_mfma_f32_16x16x32_bf16 a[24:27], a[232:235], v[112:115], a[24:27]
ds_write_b128 v243, a[8:11] offset:0
v_mfma_f32_16x16x32_bf16 a[24:27], a[236:239], v[116:119], a[24:27]
ds_read_b128 a[252:255], v241 offset:11264
v_mfma_f32_16x16x32_bf16 a[28:31], a[232:235], v[120:123], a[28:31]
ds_read_b128 a[144:147], v230 offset:4096
ds_write_b128 v243, a[12:15] offset:1024
v_mfma_f32_16x16x32_bf16 a[28:31], a[236:239], v[124:127], a[28:31]
s_waitcnt lgkmcnt(10)
v_mfma_f32_16x16x32_bf16 a[32:35], a[240:243], v[96:99], a[32:35]
v_accvgpr_read_b32 v8, a16
v_accvgpr_read_b32 v9, a17
s_waitcnt lgkmcnt(8)
v_mfma_f32_16x16x32_bf16 a[32:35], a[244:247], v[100:103], a[32:35]
ds_read_b128 a[224:227], v241 offset:4096
ds_read_b128 a[148:151], v230 offset:5120
v_accvgpr_read_b32 v10, a18
v_accvgpr_read_b32 v11, a19
v_mfma_f32_16x16x32_bf16 a[36:39], a[240:243], v[104:107], a[36:39]
v_accvgpr_read_b32 v12, a20
v_accvgpr_read_b32 v13, a21
)ASM"
R"ASM(v_mfma_f32_16x16x32_bf16 a[36:39], a[244:247], v[108:111], a[36:39]
v_accvgpr_read_b32 v14, a22
v_accvgpr_read_b32 v15, a23
v_mfma_f32_16x16x32_bf16 a[40:43], a[240:243], v[112:115], a[40:43]
ds_read_b128 a[152:155], v230 offset:6144
ds_write_b128 v243, a[24:27] offset:2048
v_mfma_f32_16x16x32_bf16 a[40:43], a[244:247], v[116:119], a[40:43]
ds_read_b128 a[228:231], v241 offset:12288
v_mfma_f32_16x16x32_bf16 a[44:47], a[240:243], v[120:123], a[44:47]
ds_write_b128 v243, a[28:31] offset:3072
v_mfma_f32_16x16x32_bf16 a[44:47], a[244:247], v[124:127], a[44:47]
ds_read_b128 a[156:159], v230 offset:7168
s_waitcnt lgkmcnt(12)
v_mfma_f32_16x16x32_bf16 a[48:51], a[248:251], v[96:99], a[48:51]
v_accvgpr_read_b32 v16, a32
v_accvgpr_read_b32 v17, a33
s_waitcnt lgkmcnt(9)
v_mfma_f32_16x16x32_bf16 a[48:51], a[252:255], v[100:103], a[48:51]
ds_read_b128 a[232:235], v241 offset:5120
v_accvgpr_read_b32 v18, a34
v_accvgpr_read_b32 v19, a35
v_mfma_f32_16x16x32_bf16 a[52:55], a[248:251], v[104:107], a[52:55]
ds_read_b128 a[160:163], v231 offset:0
v_accvgpr_read_b32 v20, a36
v_accvgpr_read_b32 v21, a37
v_mfma_f32_16x16x32_bf16 a[52:55], a[252:255], v[108:111], a[52:55]
v_accvgpr_read_b32 v22, a38
v_accvgpr_read_b32 v23, a39
v_mfma_f32_16x16x32_bf16 a[56:59], a[248:251], v[112:115], a[56:59]
ds_write_b128 v243, a[40:43] offset:4096
v_mfma_f32_16x16x32_bf16 a[56:59], a[252:255], v[116:119], a[56:59]
ds_read_b128 a[236:239], v241 offset:13312
ds_read_b128 a[164:167], v231 offset:1024
v_mfma_f32_16x16x32_bf16 a[60:63], a[248:251], v[120:123], a[60:63]
ds_write_b128 v243, a[44:47] offset:5120
v_mfma_f32_16x16x32_bf16 a[60:63], a[252:255], v[124:127], a[60:63]
s_waitcnt lgkmcnt(12)
v_mfma_f32_16x16x32_bf16 a[64:67], a[224:227], v[96:99], a[64:67]
ds_read_b128 a[168:171], v231 offset:2048
v_accvgpr_read_b32 v24, a48
v_accvgpr_read_b32 v25, a49
s_waitcnt lgkmcnt(9)
v_mfma_f32_16x16x32_bf16 a[64:67], a[228:231], v[100:103], a[64:67]
ds_read_b128 a[240:243], v241 offset:6144
v_accvgpr_read_b32 v26, a50
v_accvgpr_read_b32 v27, a51
v_mfma_f32_16x16x32_bf16 a[68:71], a[224:227], v[104:107], a[68:71]
v_accvgpr_read_b32 v28, a52
v_accvgpr_read_b32 v29, a53
v_mfma_f32_16x16x32_bf16 a[68:71], a[228:231], v[108:111], a[68:71]
ds_read_b128 a[172:175], v231 offset:3072
v_accvgpr_read_b32 v30, a54
v_accvgpr_read_b32 v31, a55
v_mfma_f32_16x16x32_bf16 a[72:75], a[224:227], v[112:115], a[72:75]
ds_write_b128 v243, a[56:59] offset:6144
v_mfma_f32_16x16x32_bf16 a[72:75], a[228:231], v[116:119], a[72:75]
ds_read_b128 a[244:247], v241 offset:14336
v_mfma_f32_16x16x32_bf16 a[76:79], a[224:227], v[120:123], a[76:79]
ds_read_b128 a[176:179], v231 offset:4096
ds_write_b128 v243, a[60:63] offset:7168
v_mfma_f32_16x16x32_bf16 a[76:79], a[228:231], v[124:127], a[76:79]
s_waitcnt lgkmcnt(12)
v_mfma_f32_16x16x32_bf16 a[80:83], a[232:235], v[96:99], a[80:83]
v_accvgpr_read_b32 v32, a64
v_accvgpr_read_b32 v33, a65
s_waitcnt lgkmcnt(9)
v_mfma_f32_16x16x32_bf16 a[80:83], a[236:239], v[100:103], a[80:83]
ds_read_b128 a[248:251], v241 offset:7168
ds_read_b128 a[180:183], v231 offset:5120
v_accvgpr_read_b32 v34, a66
v_accvgpr_read_b32 v35, a67
v_mfma_f32_16x16x32_bf16 a[84:87], a[232:235], v[104:107], a[84:87]
v_accvgpr_read_b32 v36, a68
v_accvgpr_read_b32 v37, a69
v_mfma_f32_16x16x32_bf16 a[84:87], a[236:239], v[108:111], a[84:87]
v_accvgpr_read_b32 v38, a70
v_accvgpr_read_b32 v39, a71
v_mfma_f32_16x16x32_bf16 a[88:91], a[232:235], v[112:115], a[88:91]
ds_read_b128 a[184:187], v231 offset:6144
ds_write_b128 v243, a[72:75] offset:8192
v_mfma_f32_16x16x32_bf16 a[88:91], a[236:239], v[116:119], a[88:91]
ds_read_b128 a[252:255], v241 offset:15360
v_mfma_f32_16x16x32_bf16 a[92:95], a[232:235], v[120:123], a[92:95]
ds_write_b128 v243, a[76:79] offset:9216
v_mfma_f32_16x16x32_bf16 a[92:95], a[236:239], v[124:127], a[92:95]
ds_read_b128 a[188:191], v231 offset:7168
s_waitcnt lgkmcnt(12)
v_mfma_f32_16x16x32_bf16 a[96:99], a[240:243], v[96:99], a[96:99]
v_accvgpr_read_b32 v40, a80
v_accvgpr_read_b32 v41, a81
s_waitcnt lgkmcnt(9)
v_mfma_f32_16x16x32_bf16 a[96:99], a[244:247], v[100:103], a[96:99]
v_accvgpr_read_b32 v42, a82
v_accvgpr_read_b32 v43, a83
v_mfma_f32_16x16x32_bf16 a[100:103], a[240:243], v[104:107], a[100:103]
v_accvgpr_read_b32 v44, a84
v_accvgpr_read_b32 v45, a85
v_xor_b32 v241, 0x8000, v241
v_mfma_f32_16x16x32_bf16 a[100:103], a[244:247], v[108:111], a[100:103]
v_accvgpr_read_b32 v46, a86
v_accvgpr_read_b32 v47, a87
v_mfma_f32_16x16x32_bf16 a[104:107], a[240:243], v[112:115], a[104:107]
ds_write_b128 v243, a[88:91] offset:10240
v_mfma_f32_16x16x32_bf16 a[104:107], a[244:247], v[116:119], a[104:107]
v_mfma_f32_16x16x32_bf16 a[108:111], a[240:243], v[120:123], a[108:111]
ds_write_b128 v243, a[92:95] offset:11264
v_mfma_f32_16x16x32_bf16 a[108:111], a[244:247], v[124:127], a[108:111]
s_waitcnt lgkmcnt(8)
v_mfma_f32_16x16x32_bf16 a[112:115], a[248:251], v[96:99], a[112:115]
v_accvgpr_read_b32 v48, a96
v_accvgpr_read_b32 v49, a97
s_waitcnt lgkmcnt(4)
v_mfma_f32_16x16x32_bf16 a[112:115], a[252:255], v[100:103], a[112:115]
v_accvgpr_read_b32 v50, a98
v_accvgpr_read_b32 v51, a99
v_mfma_f32_16x16x32_bf16 a[116:119], a[248:251], v[104:107], a[116:119]
v_accvgpr_read_b32 v52, a100
v_accvgpr_read_b32 v53, a101
v_mfma_f32_16x16x32_bf16 a[116:119], a[252:255], v[108:111], a[116:119]
v_accvgpr_read_b32 v54, a102
v_accvgpr_read_b32 v55, a103
v_mfma_f32_16x16x32_bf16 a[120:123], a[248:251], v[112:115], a[120:123]
ds_write_b128 v243, a[104:107] offset:12288
v_mfma_f32_16x16x32_bf16 a[120:123], a[252:255], v[116:119], a[120:123]
v_mfma_f32_16x16x32_bf16 a[124:127], a[248:251], v[120:123], a[124:127]
ds_write_b128 v243, a[108:111] offset:13312
v_mfma_f32_16x16x32_bf16 a[124:127], a[252:255], v[124:127], a[124:127]
v_mfma_f32_16x16x32_bf16 v[144:147], v[160:163], v[96:99], v[144:147]
v_accvgpr_read_b32 v56, a112
v_accvgpr_read_b32 v57, a113
v_mfma_f32_16x16x32_bf16 v[144:147], v[160:163], v[100:103], v[144:147]
v_accvgpr_read_b32 v58, a114
v_accvgpr_read_b32 v59, a115
v_mfma_f32_16x16x32_bf16 v[148:151], v[160:163], v[104:107], v[148:151]
v_accvgpr_read_b32 v60, a116
v_accvgpr_read_b32 v61, a117
v_mfma_f32_16x16x32_bf16 v[148:151], v[160:163], v[108:111], v[148:151]
v_accvgpr_read_b32 v62, a118
v_accvgpr_read_b32 v63, a119
v_mfma_f32_16x16x32_bf16 v[152:155], v[160:163], v[112:115], v[152:155]
ds_write_b128 v243, a[120:123] offset:14336
v_mfma_f32_16x16x32_bf16 v[152:155], v[160:163], v[116:119], v[152:155]
v_mfma_f32_16x16x32_bf16 v[156:159], v[160:163], v[120:123], v[156:159]
ds_write_b128 v243, a[124:127] offset:15360
v_mfma_f32_16x16x32_bf16 v[156:159], v[160:163], v[124:127], v[156:159]
s_waitcnt lgkmcnt(0)
s_barrier
s_nop 2
ds_write_b32 v249, v152 offset:1024
s_nop 1
ds_write_b32 v249, v156 offset:2048
mla_fused_b_comb:
s_waitcnt lgkmcnt(0)
s_barrier
ds_read_b32 v212, v250 offset:1024
ds_read_b32 v213, v250 offset:2048
ds_read_b128 v[64:67], v244 offset:0
ds_read_b128 v[72:75], v244 offset:2048
ds_read_b128 v[68:71], v244 offset:1024
ds_read_b128 v[76:79], v244 offset:3072
ds_read_b128 v[80:83], v244 offset:4096
ds_read_b128 v[88:91], v244 offset:6144
ds_read_b128 v[84:87], v244 offset:5120
ds_read_b128 v[92:95], v244 offset:7168
ds_read_b128 v[96:99], v244 offset:8192
ds_read_b128 v[104:107], v244 offset:10240
ds_read_b128 v[100:103], v244 offset:9216
ds_read_b128 v[108:111], v244 offset:11264
ds_read_b128 v[112:115], v244 offset:12288
ds_read_b128 v[120:123], v244 offset:14336
ds_read_b128 v[116:119], v244 offset:13312
ds_read_b128 v[124:127], v244 offset:15360
s_mul_i32 s58, s48, 184
s_lshl_b32 s82, s71, 2
s_add_u32 s58, s58, s82
s_add_u32 s58, s58, 56
s_lshl_b32 s58, s58, 3
s_lshl_b32 s82, s70, 2
s_add_u32 s58, s58, s82
s_lshl_b32 s82, s69, 1
s_add_u32 s58, s58, s82
s_lshl_b32 s58, s58, 10
s_add_u32 s58, s58, 0x3000000
s_waitcnt lgkmcnt(15)
v_add_f32 v208, v144, v212
v_add_f32 v210, v148, v213
v_cmp_ngt_f32 vcc, 0x5d800000, v208
v_rcp_f32 v208, v208
s_nop 2
s_cmp_lg_u64 vcc, 0
s_cselect_b32 s82, 1, 0
s_lshl_b32 s82, s82, s59
s_or_b32 s73, s73, s82
v_cmp_ngt_f32 vcc, 0x5d800000, v210
v_rcp_f32 v210, v210
s_nop 2
s_cmp_lg_u64 vcc, 0
s_cselect_b32 s82, 1, 0
s_lshl_b32 s82, s82, s59
s_or_b32 s73, s73, s82
v_mov_b32 v209, v208
v_mov_b32 v211, v210
v_pk_add_f32 v[0:1], v[0:1], v[64:65]
v_pk_add_f32 v[2:3], v[2:3], v[66:67]
s_waitcnt lgkmcnt(14)
v_pk_add_f32 v[8:9], v[8:9], v[72:73]
v_pk_add_f32 v[10:11], v[10:11], v[74:75]
v_pk_mul_f32 v[0:1], v[0:1], v[208:209]
v_pk_mul_f32 v[2:3], v[2:3], v[208:209]
v_pk_mul_f32 v[8:9], v[8:9], v[208:209]
v_pk_mul_f32 v[10:11], v[10:11], v[208:209]
v_cvt_pk_bf16_f32 v0, v0, v1
v_cvt_pk_bf16_f32 v1, v2, v3
v_cvt_pk_bf16_f32 v2, v8, v9
v_cvt_pk_bf16_f32 v3, v10, v11
s_add_u32 s82, s58, 0
buffer_store_dwordx4 v[0:3], v252, s[36:39], s82 offen offset:0 sc1
s_waitcnt lgkmcnt(13)
v_pk_add_f32 v[4:5], v[4:5], v[68:69]
v_pk_add_f32 v[6:7], v[6:7], v[70:71]
s_waitcnt lgkmcnt(12)
v_pk_add_f32 v[12:13], v[12:13], v[76:77]
v_pk_add_f32 v[14:15], v[14:15], v[78:79]
v_pk_mul_f32 v[4:5], v[4:5], v[210:211]
v_pk_mul_f32 v[6:7], v[6:7], v[210:211]
v_pk_mul_f32 v[12:13], v[12:13], v[210:211]
v_pk_mul_f32 v[14:15], v[14:15], v[210:211]
v_cvt_pk_bf16_f32 v4, v4, v5
v_cvt_pk_bf16_f32 v5, v6, v7
v_cvt_pk_bf16_f32 v6, v12, v13
v_cvt_pk_bf16_f32 v7, v14, v15
s_add_u32 s82, s58, 0
buffer_store_dwordx4 v[4:7], v252, s[36:39], s82 offen offset:1024 sc1
s_waitcnt lgkmcnt(11)
v_pk_add_f32 v[16:17], v[16:17], v[80:81]
v_pk_add_f32 v[18:19], v[18:19], v[82:83]
s_waitcnt lgkmcnt(10)
v_pk_add_f32 v[24:25], v[24:25], v[88:89]
v_pk_add_f32 v[26:27], v[26:27], v[90:91]
v_pk_mul_f32 v[16:17], v[16:17], v[208:209]
v_pk_mul_f32 v[18:19], v[18:19], v[208:209]
v_pk_mul_f32 v[24:25], v[24:25], v[208:209]
v_pk_mul_f32 v[26:27], v[26:27], v[208:209]
v_cvt_pk_bf16_f32 v16, v16, v17
v_cvt_pk_bf16_f32 v17, v18, v19
v_cvt_pk_bf16_f32 v18, v24, v25
v_cvt_pk_bf16_f32 v19, v26, v27
s_add_u32 s82, s58, 8192
buffer_store_dwordx4 v[16:19], v252, s[36:39], s82 offen offset:0 sc1
s_waitcnt lgkmcnt(9)
v_pk_add_f32 v[20:21], v[20:21], v[84:85]
v_pk_add_f32 v[22:23], v[22:23], v[86:87]
s_waitcnt lgkmcnt(8)
v_pk_add_f32 v[28:29], v[28:29], v[92:93]
v_pk_add_f32 v[30:31], v[30:31], v[94:95]
v_pk_mul_f32 v[20:21], v[20:21], v[210:211]
v_pk_mul_f32 v[22:23], v[22:23], v[210:211]
v_pk_mul_f32 v[28:29], v[28:29], v[210:211]
v_pk_mul_f32 v[30:31], v[30:31], v[210:211]
v_cvt_pk_bf16_f32 v20, v20, v21
v_cvt_pk_bf16_f32 v21, v22, v23
v_cvt_pk_bf16_f32 v22, v28, v29
v_cvt_pk_bf16_f32 v23, v30, v31
s_add_u32 s82, s58, 8192
buffer_store_dwordx4 v[20:23], v252, s[36:39], s82 offen offset:1024 sc1
s_waitcnt lgkmcnt(7)
v_pk_add_f32 v[32:33], v[32:33], v[96:97]
v_pk_add_f32 v[34:35], v[34:35], v[98:99]
s_waitcnt lgkmcnt(6)
v_pk_add_f32 v[40:41], v[40:41], v[104:105]
v_pk_add_f32 v[42:43], v[42:43], v[106:107]
v_pk_mul_f32 v[32:33], v[32:33], v[208:209]
v_pk_mul_f32 v[34:35], v[34:35], v[208:209]
v_pk_mul_f32 v[40:41], v[40:41], v[208:209]
v_pk_mul_f32 v[42:43], v[42:43], v[208:209]
v_cvt_pk_bf16_f32 v32, v32, v33
v_cvt_pk_bf16_f32 v33, v34, v35
v_cvt_pk_bf16_f32 v34, v40, v41
v_cvt_pk_bf16_f32 v35, v42, v43
s_add_u32 s82, s58, 16384
buffer_store_dwordx4 v[32:35], v252, s[36:39], s82 offen offset:0 sc1
s_waitcnt lgkmcnt(5)
v_pk_add_f32 v[36:37], v[36:37], v[100:101]
v_pk_add_f32 v[38:39], v[38:39], v[102:103]
s_waitcnt lgkmcnt(4)
v_pk_add_f32 v[44:45], v[44:45], v[108:109]
v_pk_add_f32 v[46:47], v[46:47], v[110:111]
v_pk_mul_f32 v[36:37], v[36:37], v[210:211]
v_pk_mul_f32 v[38:39], v[38:39], v[210:211]
v_pk_mul_f32 v[44:45], v[44:45], v[210:211]
v_pk_mul_f32 v[46:47], v[46:47], v[210:211]
v_cvt_pk_bf16_f32 v36, v36, v37
v_cvt_pk_bf16_f32 v37, v38, v39
v_cvt_pk_bf16_f32 v38, v44, v45
v_cvt_pk_bf16_f32 v39, v46, v47
s_add_u32 s82, s58, 16384
buffer_store_dwordx4 v[36:39], v252, s[36:39], s82 offen offset:1024 sc1
s_waitcnt lgkmcnt(3)
v_pk_add_f32 v[48:49], v[48:49], v[112:113]
v_pk_add_f32 v[50:51], v[50:51], v[114:115]
s_waitcnt lgkmcnt(2)
v_pk_add_f32 v[56:57], v[56:57], v[120:121]
v_pk_add_f32 v[58:59], v[58:59], v[122:123]
)ASM"
R"ASM(v_pk_mul_f32 v[48:49], v[48:49], v[208:209]
v_pk_mul_f32 v[50:51], v[50:51], v[208:209]
v_pk_mul_f32 v[56:57], v[56:57], v[208:209]
v_pk_mul_f32 v[58:59], v[58:59], v[208:209]
v_cvt_pk_bf16_f32 v48, v48, v49
v_cvt_pk_bf16_f32 v49, v50, v51
v_cvt_pk_bf16_f32 v50, v56, v57
v_cvt_pk_bf16_f32 v51, v58, v59
s_add_u32 s82, s58, 24576
buffer_store_dwordx4 v[48:51], v252, s[36:39], s82 offen offset:0 sc1
s_waitcnt lgkmcnt(1)
v_pk_add_f32 v[52:53], v[52:53], v[116:117]
v_pk_add_f32 v[54:55], v[54:55], v[118:119]
s_waitcnt lgkmcnt(0)
v_pk_add_f32 v[60:61], v[60:61], v[124:125]
v_pk_add_f32 v[62:63], v[62:63], v[126:127]
v_pk_mul_f32 v[52:53], v[52:53], v[210:211]
v_pk_mul_f32 v[54:55], v[54:55], v[210:211]
v_pk_mul_f32 v[60:61], v[60:61], v[210:211]
v_pk_mul_f32 v[62:63], v[62:63], v[210:211]
v_cvt_pk_bf16_f32 v52, v52, v53
v_cvt_pk_bf16_f32 v53, v54, v55
v_cvt_pk_bf16_f32 v54, v60, v61
v_cvt_pk_bf16_f32 v55, v62, v63
s_add_u32 s82, s58, 24576
buffer_store_dwordx4 v[52:55], v252, s[36:39], s82 offen offset:1024 sc1
s_cmp_eq_u32 s59, 1
s_cbranch_scc1 mla_fused_b_end
s_mov_b32 s59, 1
s_mov_b32 s48, s49
s_lshl_b32 s84, s71, 19
s_add_u32 s84, s84, s75
s_lshl_b32 s83, s48, 15
s_add_u32 s83, s83, s84
s_add_u32 s52, s83, 0x2000000
s_add_u32 s51, s83, 0x1000000
s_sub_u32 s51, s51, 32768
s_waitcnt lgkmcnt(0)
s_barrier
s_branch mla_fused_b_init
mla_fused_b_init:
s_cmp_lg_u32 s68, 0
s_cbranch_scc1 mla_fused_f_pi5
v_and_b32 v218, 63, %[tid]
v_cmp_gt_u32 vcc, 7, v218
v_lshlrev_b32 v218, 3, v218
s_nop 2
s_mov_b64 s[92:93], vcc
v_mov_b32 v216, 0x7ffffff0
v_cndmask_b32 v218, v216, v218, vcc
buffer_load_dwordx2 v[216:217], v218, s[36:39], s77 offen sc1
mla_fused_f_pi5:
s_getpc_b64 s[44:45]
mla_fused_h_pc:
s_add_u32 s44, s44, mla_fused_c_start-mla_fused_h_pc
s_addc_u32 s45, s45, 0
s_and_b32 s45, s45, 0xffff
s_mov_b32 s46, mla_fused_b_pfend-mla_fused_c_start
s_mov_b32 s47, 0x20000
v_lshlrev_b32 v219, 6, %[tid]
buffer_load_dword v221, v219, s[44:47], 0 offen
s_mov_b32 s50, s48
s_cmp_eq_u32 s50, 0
s_cselect_b32 s74, s53, s51
s_cselect_b32 s83, s59, 0
s_cmp_eq_u32 s83, 1
s_cselect_b32 s42, 0, 0x5712000
v_mfma_f32_16x16x32_bf16 v[0:3], a[192:195], a[128:131], v[168:171]
v_mfma_f32_16x16x32_bf16 v[0:3], a[196:199], a[132:135], v[0:3]
ds_read_b128 a[208:211], v240 offset:4096
v_mfma_f32_16x16x32_bf16 v[0:3], a[200:203], a[136:139], v[0:3]
v_mfma_f32_16x16x32_bf16 v[0:3], a[204:207], a[140:143], v[0:3]
s_add_u32 m0, s54, 0
s_add_u32 s82, s74, 0
buffer_load_dwordx4 v252, s[40:43], s82 offen offset:0 sc1 lds
v_mfma_f32_16x16x32_bf16 v[4:7], a[192:195], a[144:147], v[164:167]
v_mfma_f32_16x16x32_bf16 v[4:7], a[196:199], a[148:151], v[4:7]
ds_read_b128 a[212:215], v240 offset:5120
v_mfma_f32_16x16x32_bf16 v[4:7], a[200:203], a[152:155], v[4:7]
v_mfma_f32_16x16x32_bf16 v[4:7], a[204:207], a[156:159], v[4:7]
buffer_load_dwordx4 v252, s[40:43], s82 offen offset:1024 sc1 lds
v_mfma_f32_16x16x32_bf16 v[8:11], a[192:195], a[160:163], v[188:191]
v_mfma_f32_16x16x32_bf16 v[8:11], a[196:199], a[164:167], v[8:11]
ds_read_b128 a[216:219], v240 offset:6144
v_mfma_f32_16x16x32_bf16 v[8:11], a[200:203], a[168:171], v[8:11]
v_mfma_f32_16x16x32_bf16 v[8:11], a[204:207], a[172:175], v[8:11]
buffer_load_dwordx4 v252, s[40:43], s82 offen offset:2048 sc1 lds
v_mfma_f32_16x16x32_bf16 v[12:15], a[192:195], a[176:179], v[184:187]
v_mfma_f32_16x16x32_bf16 v[12:15], a[196:199], a[180:183], v[12:15]
ds_read_b128 a[220:223], v240 offset:7168
v_mfma_f32_16x16x32_bf16 v[12:15], a[200:203], a[184:187], v[12:15]
v_mfma_f32_16x16x32_bf16 v[12:15], a[204:207], a[188:191], v[12:15]
buffer_load_dwordx4 v252, s[40:43], s82 offen offset:3072 sc1 lds
v_mfma_f32_32x32x16_bf16 a[0:15], v[160:163], v[204:207], 0
s_add_u32 m0, s54, 4096
s_add_u32 s82, s74, 4096
buffer_load_dwordx4 v252, s[40:43], s82 offen offset:0 sc1 lds
v_mfma_f32_32x32x16_bf16 a[16:31], v[160:163], v[204:207], 0
buffer_load_dwordx4 v252, s[40:43], s82 offen offset:1024 sc1 lds
buffer_load_dwordx4 v252, s[40:43], s82 offen offset:2048 sc1 lds
v_max3_f32 v208, v0, v1, v2
v_max3_f32 v209, v4, v5, v6
v_max3_f32 v210, v8, v9, v10
v_max3_f32 v211, v12, v13, v14
v_mfma_f32_32x32x16_bf16 a[32:47], v[160:163], v[204:207], 0
buffer_load_dwordx4 v252, s[40:43], s82 offen offset:3072 sc1 lds
v_max_f32 v208, v208, v3
v_max_f32 v209, v209, v7
v_max_f32 v210, v210, v11
v_max_f32 v211, v211, v15
v_mov_b32 v212, v208
v_mov_b32 v213, v209
v_mov_b32 v214, v210
v_mov_b32 v215, v211
v_mfma_f32_32x32x16_bf16 a[48:63], v[160:163], v[204:207], 0
s_add_u32 m0, s55, 0
s_add_u32 s82, s52, 0
buffer_load_dwordx4 v252, s[36:39], s82 offen offset:0 sc1 lds
v_permlane16_swap_b32 v208, v212
v_permlane16_swap_b32 v209, v213
v_permlane16_swap_b32 v210, v214
v_permlane16_swap_b32 v211, v215
v_max_f32 v208, v208, v212
v_max_f32 v209, v209, v213
v_max_f32 v210, v210, v214
v_max_f32 v211, v211, v215
v_mov_b32 v212, v208
v_mov_b32 v213, v209
v_mov_b32 v214, v210
v_mov_b32 v215, v211
v_mfma_f32_32x32x16_bf16 a[64:79], v[160:163], v[204:207], 0
buffer_load_dwordx4 v252, s[36:39], s82 offen offset:1024 sc1 lds
v_permlane32_swap_b32 v208, v212
v_permlane32_swap_b32 v209, v213
v_permlane32_swap_b32 v210, v214
v_permlane32_swap_b32 v211, v215
v_max_f32 v208, v208, v212
v_max_f32 v209, v209, v213
v_max_f32 v210, v210, v214
v_max_f32 v211, v211, v215
ds_write_b128 v249, v[208:211]
v_mfma_f32_32x32x16_bf16 a[80:95], v[160:163], v[204:207], 0
buffer_load_dwordx4 v252, s[36:39], s82 offen offset:2048 sc1 lds
v_mfma_f32_32x32x16_bf16 a[96:111], v[160:163], v[204:207], 0
buffer_load_dwordx4 v252, s[36:39], s82 offen offset:3072 sc1 lds
s_waitcnt lgkmcnt(0)
s_barrier
ds_read_b128 v[212:215], v250
v_mfma_f32_32x32x16_bf16 a[112:127], v[160:163], v[204:207], 0
v_mfma_f32_32x32x16_bf16 v[144:159], v[160:163], v[204:207], 0
s_waitcnt lgkmcnt(0)
v_max_f32 v208, v208, v214
v_max_f32 v209, v209, v215
v_max_f32 v210, v210, v212
v_max_f32 v211, v211, v213
v_sub_f32 v128, 0, v208
v_sub_f32 v132, 0, v209
v_sub_f32 v136, 0, v210
v_sub_f32 v140, 0, v211
v_add_f32 v16, v172, v128
v_add_f32 v17, v173, v128
v_add_f32 v18, v174, v128
v_add_f32 v19, v175, v128
v_add_f32 v20, v168, v132
v_add_f32 v21, v169, v132
v_add_f32 v22, v170, v132
v_add_f32 v23, v171, v132
v_mov_b32 v129, v128
v_mov_b32 v130, v128
v_mov_b32 v131, v128
v_mov_b32 v133, v132
v_mov_b32 v134, v132
v_mov_b32 v135, v132
v_mov_b32 v137, v136
v_mov_b32 v138, v136
v_mov_b32 v139, v136
v_mov_b32 v141, v140
v_mov_b32 v142, v140
v_mov_b32 v143, v140
v_mfma_f32_16x16x32_bf16 v[16:19], a[208:211], a[128:131], v[16:19]
s_add_u32 m0, s55, 4096
s_add_u32 s82, s52, 4096
buffer_load_dwordx4 v252, s[36:39], s82 offen offset:0 sc1 lds
ds_read_b128 a[192:195], v240 offset:8192
ds_read_b128 a[196:199], v240 offset:9216
v_mfma_f32_16x16x32_bf16 v[16:19], a[212:215], a[132:135], v[16:19]
ds_read_b128 a[200:203], v240 offset:10240
ds_read_b128 a[204:207], v240 offset:11264
v_add_f32 v24, v192, v136
v_add_f32 v25, v193, v136
v_add_f32 v26, v194, v136
v_mfma_f32_16x16x32_bf16 v[16:19], a[216:219], a[136:139], v[16:19]
v_add_f32 v27, v195, v136
v_add_f32 v0, v0, v128
v_add_f32 v1, v1, v128
v_add_f32 v2, v2, v128
v_add_f32 v3, v3, v128
v_mfma_f32_16x16x32_bf16 v[16:19], a[220:223], a[140:143], v[16:19]
v_exp_f32 v0, v0
v_exp_f32 v1, v1
v_exp_f32 v2, v2
v_cvt_pk_bf16_f32 v64, v0, v1
v_exp_f32 v3, v3
v_mfma_f32_16x16x32_bf16 v[20:23], a[208:211], a[144:147], v[20:23]
buffer_load_dwordx4 v252, s[36:39], s82 offen offset:1024 sc1 lds
v_add_f32 v28, v188, v140
v_add_f32 v29, v189, v140
v_mfma_f32_16x16x32_bf16 v[20:23], a[212:215], a[148:151], v[20:23]
v_add_f32 v30, v190, v140
v_add_f32 v31, v191, v140
v_add_f32 v4, v4, v132
v_add_f32 v5, v5, v132
v_mfma_f32_16x16x32_bf16 v[20:23], a[216:219], a[152:155], v[20:23]
v_add_f32 v6, v6, v132
v_add_f32 v7, v7, v132
v_cvt_pk_bf16_f32 v65, v2, v3
v_exp_f32 v4, v4
v_mfma_f32_16x16x32_bf16 v[20:23], a[220:223], a[156:159], v[20:23]
v_exp_f32 v5, v5
v_exp_f32 v6, v6
v_cvt_pk_bf16_f32 v72, v4, v5
v_exp_f32 v7, v7
v_mfma_f32_16x16x32_bf16 v[24:27], a[208:211], a[160:163], v[24:27]
buffer_load_dwordx4 v252, s[36:39], s82 offen offset:2048 sc1 lds
v_add_f32 v32, v176, v128
v_add_f32 v33, v177, v128
v_mfma_f32_16x16x32_bf16 v[24:27], a[212:215], a[164:167], v[24:27]
v_add_f32 v34, v178, v128
v_add_f32 v35, v179, v128
v_add_f32 v8, v8, v136
v_add_f32 v9, v9, v136
v_mfma_f32_16x16x32_bf16 v[24:27], a[216:219], a[168:171], v[24:27]
v_add_f32 v10, v10, v136
v_add_f32 v11, v11, v136
v_cvt_pk_bf16_f32 v73, v6, v7
v_exp_f32 v8, v8
v_mfma_f32_16x16x32_bf16 v[24:27], a[220:223], a[172:175], v[24:27]
v_exp_f32 v9, v9
v_exp_f32 v10, v10
v_cvt_pk_bf16_f32 v80, v8, v9
v_exp_f32 v11, v11
v_mfma_f32_16x16x32_bf16 v[28:31], a[208:211], a[176:179], v[28:31]
buffer_load_dwordx4 v252, s[36:39], s82 offen offset:3072 sc1 lds
v_add_f32 v36, v172, v132
v_add_f32 v37, v173, v132
v_mfma_f32_16x16x32_bf16 v[28:31], a[212:215], a[180:183], v[28:31]
v_add_f32 v38, v174, v132
v_add_f32 v39, v175, v132
v_add_f32 v12, v12, v140
v_add_f32 v13, v13, v140
v_mfma_f32_16x16x32_bf16 v[28:31], a[216:219], a[184:187], v[28:31]
v_add_f32 v14, v14, v140
v_add_f32 v15, v15, v140
v_cvt_pk_bf16_f32 v81, v10, v11
v_exp_f32 v12, v12
v_mfma_f32_16x16x32_bf16 v[28:31], a[220:223], a[188:191], v[28:31]
v_exp_f32 v13, v13
v_exp_f32 v14, v14
v_cvt_pk_bf16_f32 v88, v12, v13
v_exp_f32 v15, v15
s_waitcnt lgkmcnt(3)
v_mfma_f32_16x16x32_bf16 v[32:35], a[192:195], a[128:131], v[32:35]
ds_read_b128 a[208:211], v240 offset:12288
ds_read_b128 a[212:215], v240 offset:13312
ds_read_b128 a[216:219], v240 offset:14336
s_waitcnt lgkmcnt(5)
v_mfma_f32_16x16x32_bf16 v[32:35], a[196:199], a[132:135], v[32:35]
ds_read_b128 a[220:223], v240 offset:15360
v_add_f32 v40, v196, v136
v_add_f32 v41, v197, v136
v_add_f32 v42, v198, v136
s_waitcnt lgkmcnt(5)
v_mfma_f32_16x16x32_bf16 v[32:35], a[200:203], a[136:139], v[32:35]
v_add_f32 v43, v199, v136
v_cvt_pk_bf16_f32 v89, v14, v15
v_exp_f32 v16, v16
s_waitcnt lgkmcnt(4)
v_mfma_f32_16x16x32_bf16 v[32:35], a[204:207], a[140:143], v[32:35]
v_exp_f32 v17, v17
v_exp_f32 v18, v18
v_cvt_pk_bf16_f32 v66, v16, v17
v_exp_f32 v19, v19
v_mfma_f32_16x16x32_bf16 v[36:39], a[192:195], a[144:147], v[36:39]
v_add_f32 v44, v192, v140
v_add_f32 v45, v193, v140
v_mfma_f32_16x16x32_bf16 v[36:39], a[196:199], a[148:151], v[36:39]
v_add_f32 v46, v194, v140
v_add_f32 v47, v195, v140
v_cvt_pk_bf16_f32 v67, v18, v19
v_mfma_f32_16x16x32_bf16 v[36:39], a[200:203], a[152:155], v[36:39]
v_exp_f32 v20, v20
v_exp_f32 v21, v21
v_mfma_f32_16x16x32_bf16 v[36:39], a[204:207], a[156:159], v[36:39]
v_exp_f32 v22, v22
v_cvt_pk_bf16_f32 v74, v20, v21
v_exp_f32 v23, v23
v_mfma_f32_16x16x32_bf16 v[40:43], a[192:195], a[160:163], v[40:43]
v_add_f32 v48, v180, v128
v_add_f32 v49, v181, v128
v_mfma_f32_16x16x32_bf16 v[40:43], a[196:199], a[164:167], v[40:43]
v_add_f32 v50, v182, v128
v_add_f32 v51, v183, v128
v_cvt_pk_bf16_f32 v75, v22, v23
v_mfma_f32_16x16x32_bf16 v[40:43], a[200:203], a[168:171], v[40:43]
v_exp_f32 v24, v24
v_exp_f32 v25, v25
v_mfma_f32_16x16x32_bf16 v[40:43], a[204:207], a[172:175], v[40:43]
v_exp_f32 v26, v26
v_cvt_pk_bf16_f32 v82, v24, v25
v_exp_f32 v27, v27
v_mfma_f32_16x16x32_bf16 v[44:47], a[192:195], a[176:179], v[44:47]
v_add_f32 v52, v176, v132
v_add_f32 v53, v177, v132
v_mfma_f32_16x16x32_bf16 v[44:47], a[196:199], a[180:183], v[44:47]
v_add_f32 v54, v178, v132
v_add_f32 v55, v179, v132
v_cvt_pk_bf16_f32 v83, v26, v27
v_mfma_f32_16x16x32_bf16 v[44:47], a[200:203], a[184:187], v[44:47]
v_exp_f32 v28, v28
v_exp_f32 v29, v29
v_mfma_f32_16x16x32_bf16 v[44:47], a[204:207], a[188:191], v[44:47]
v_exp_f32 v30, v30
v_cvt_pk_bf16_f32 v90, v28, v29
v_exp_f32 v31, v31
s_waitcnt lgkmcnt(3)
v_mfma_f32_16x16x32_bf16 v[48:51], a[208:211], a[128:131], v[48:51]
v_add_f32 v56, v200, v136
v_add_f32 v57, v201, v136
s_waitcnt lgkmcnt(2)
v_mfma_f32_16x16x32_bf16 v[48:51], a[212:215], a[132:135], v[48:51]
v_add_f32 v58, v202, v136
v_add_f32 v59, v203, v136
v_cvt_pk_bf16_f32 v91, v30, v31
s_waitcnt lgkmcnt(1)
v_mfma_f32_16x16x32_bf16 v[48:51], a[216:219], a[136:139], v[48:51]
)ASM"
R"ASM(v_exp_f32 v32, v32
v_exp_f32 v33, v33
s_waitcnt lgkmcnt(0)
v_mfma_f32_16x16x32_bf16 v[48:51], a[220:223], a[140:143], v[48:51]
v_exp_f32 v34, v34
v_cvt_pk_bf16_f32 v68, v32, v33
v_exp_f32 v35, v35
v_mfma_f32_16x16x32_bf16 v[52:55], a[208:211], a[144:147], v[52:55]
v_add_f32 v60, v196, v140
v_add_f32 v61, v197, v140
v_mfma_f32_16x16x32_bf16 v[52:55], a[212:215], a[148:151], v[52:55]
v_add_f32 v62, v198, v140
v_add_f32 v63, v199, v140
v_cvt_pk_bf16_f32 v69, v34, v35
v_mfma_f32_16x16x32_bf16 v[52:55], a[216:219], a[152:155], v[52:55]
v_exp_f32 v36, v36
v_exp_f32 v37, v37
v_mfma_f32_16x16x32_bf16 v[52:55], a[220:223], a[156:159], v[52:55]
v_exp_f32 v38, v38
v_cvt_pk_bf16_f32 v76, v36, v37
v_exp_f32 v39, v39
v_mfma_f32_16x16x32_bf16 v[56:59], a[208:211], a[160:163], v[56:59]
v_cvt_pk_bf16_f32 v77, v38, v39
v_mfma_f32_16x16x32_bf16 v[56:59], a[212:215], a[164:167], v[56:59]
v_exp_f32 v40, v40
v_exp_f32 v41, v41
v_mfma_f32_16x16x32_bf16 v[56:59], a[216:219], a[168:171], v[56:59]
v_exp_f32 v42, v42
v_mfma_f32_16x16x32_bf16 v[56:59], a[220:223], a[172:175], v[56:59]
v_cvt_pk_bf16_f32 v84, v40, v41
v_exp_f32 v43, v43
v_mfma_f32_16x16x32_bf16 v[60:63], a[208:211], a[176:179], v[60:63]
v_cvt_pk_bf16_f32 v85, v42, v43
v_mfma_f32_16x16x32_bf16 v[60:63], a[212:215], a[180:183], v[60:63]
v_exp_f32 v44, v44
v_exp_f32 v45, v45
v_mfma_f32_16x16x32_bf16 v[60:63], a[216:219], a[184:187], v[60:63]
v_exp_f32 v46, v46
v_mfma_f32_16x16x32_bf16 v[60:63], a[220:223], a[188:191], v[60:63]
v_cvt_pk_bf16_f32 v92, v44, v45
v_exp_f32 v47, v47
s_sub_u32 s51, s51, 32768
s_sub_u32 s52, s52, 32768
s_xor_b32 s54, s54, 32768
s_xor_b32 s55, s55, 32768
v_cvt_pk_bf16_f32 v93, v46, v47
v_exp_f32 v48, v48
v_exp_f32 v49, v49
v_exp_f32 v50, v50
v_cvt_pk_bf16_f32 v70, v48, v49
v_exp_f32 v51, v51
s_nop 0
v_cvt_pk_bf16_f32 v71, v50, v51
v_exp_f32 v52, v52
v_exp_f32 v53, v53
v_exp_f32 v54, v54
v_cvt_pk_bf16_f32 v78, v52, v53
v_exp_f32 v55, v55
s_nop 0
v_cvt_pk_bf16_f32 v79, v54, v55
v_exp_f32 v56, v56
v_exp_f32 v57, v57
v_exp_f32 v58, v58
v_cvt_pk_bf16_f32 v86, v56, v57
v_exp_f32 v59, v59
s_nop 0
v_cvt_pk_bf16_f32 v87, v58, v59
v_exp_f32 v60, v60
v_exp_f32 v61, v61
v_exp_f32 v62, v62
v_cvt_pk_bf16_f32 v94, v60, v61
v_exp_f32 v63, v63
s_nop 0
v_cvt_pk_bf16_f32 v95, v62, v63
s_waitcnt vmcnt(0) lgkmcnt(0)
s_barrier
v_xor_b32 v240, 0x8000, v240
s_cmp_lg_u32 s68, 0
s_cbranch_scc1 mla_fused_f_pc6x
s_mov_b32 s90, 262144
s_waitcnt vmcnt(0)
s_branch mla_fused_f_pc6c
mla_fused_f_pc6:
buffer_load_dwordx2 v[216:217], v218, s[36:39], s77 offen sc1
s_waitcnt vmcnt(0)
mla_fused_f_pc6c:
v_cmp_eq_u32 vcc, s98, v216
v_cmp_eq_u32 s[94:95], s99, v217
s_nop 3
s_and_b64 vcc, vcc, s[94:95]
s_and_b64 vcc, vcc, s[92:93]
s_cmp_eq_u64 vcc, s[92:93]
s_cbranch_scc1 mla_fused_f_pc6d
s_sub_u32 s90, s90, 1
s_cmp_eq_u32 s90, 0
s_cbranch_scc1 mla_fused_f_pc6d
s_sleep 2
s_branch mla_fused_f_pc6
mla_fused_f_pc6d:
mla_fused_f_pc6x:
ds_read_b128 a[192:195], v240 offset:0
ds_read_b128 a[196:199], v240 offset:1024
ds_read_b128 a[200:203], v240 offset:2048
ds_read_b128 a[204:207], v240 offset:3072
s_cmp_eq_u32 s50, 0
s_cbranch_scc1 mla_fused_b_drain0
s_branch mla_fused_b_loop
mla_fused_b_end:
s_mov_b32 %[flag], s73
s_mov_b32 %[tl], s98
s_mov_b32 %[th], s99
)ASM"
  : [flag]"=&s"(flag), [tl]"=&s"(tl), [th]"=&s"(th)
  : [x]"s"(x), [ckv]"s"(ckv), [wq]"s"(wq), [wuk]"s"(wuk), [wuv]"s"(wuv), [w1]"s"(w1), [b1]"s"(b1), [gw]"s"(gw), [wo]"s"(wo), [w2]"s"(w2), [ws]"s"(ws), [tid]"v"(tid), [bid]"s"(bid)
  : "v0", "v1", "v2", "v3", "v4", "v5", "v6", "v7", "v8", "v9", "v10", "v11", "v12", "v13", "v14", "v15", "v16", "v17", "v18", "v19", "v20", "v21", "v22", "v23", "v24", "v25", "v26", "v27", "v28", "v29", "v30", "v31", "v32", "v33", "v34", "v35", "v36", "v37", "v38", "v39", "v40", "v41", "v42", "v43", "v44", "v45", "v46", "v47", "v48", "v49", "v50", "v51", "v52", "v53", "v54", "v55", "v56", "v57", "v58", "v59", "v60", "v61", "v62", "v63", "v64", "v65", "v66", "v67", "v68", "v69", "v70", "v71", "v72", "v73", "v74", "v75", "v76", "v77", "v78", "v79", "v80", "v81", "v82", "v83", "v84", "v85", "v86", "v87", "v88", "v89", "v90", "v91", "v92", "v93", "v94", "v95", "v96", "v97", "v98", "v99", "v100", "v101", "v102", "v103", "v104", "v105", "v106", "v107", "v108", "v109", "v110", "v111", "v112", "v113", "v114", "v115", "v116", "v117", "v118", "v119", "v120", "v121", "v122", "v123", "v124", "v125", "v126", "v127", "v128", "v129", "v130", "v131", "v132", "v133", "v134", "v135", "v136", "v137", "v138", "v139", "v140", "v141", "v142", "v143", "v144", "v145", "v146", "v147", "v148", "v149", "v150", "v151", "v152", "v153", "v154", "v155", "v156", "v157", "v158", "v159", "v160", "v161", "v162", "v163", "v164", "v165", "v166", "v167", "v168", "v169", "v170", "v171", "v172", "v173", "v174", "v175", "v176", "v177", "v178", "v179", "v180", "v181", "v182", "v183", "v184", "v185", "v186", "v187", "v188", "v189", "v190", "v191", "v192", "v193", "v194", "v195", "v196", "v197", "v198", "v199", "v200", "v201", "v202", "v203", "v204", "v205", "v206", "v207", "v208", "v209", "v210", "v211", "v212", "v213", "v214", "v215", "v216", "v217", "v218", "v219", "v220", "v221", "v222", "v223", "v224", "v225", "v226", "v227", "v228", "v229", "v230", "v231", "v232", "v233", "v234", "v235", "v236", "v237", "v238", "v239", "v240", "v241", "v242", "v243", "v244", "v245", "v246", "v247", "v248", "v249", "v250", "v251", "v252", "v253", "v254", "a0", "a1", "a2", "a3", "a4", "a5", "a6", "a7", "a8", "a9", "a10", "a11", "a12", "a13", "a14", "a15", "a16", "a17", "a18", "a19", "a20", "a21", "a22", "a23", "a24", "a25", "a26", "a27", "a28", "a29", "a30", "a31", "a32", "a33", "a34", "a35", "a36", "a37", "a38", "a39", "a40", "a41", "a42", "a43", "a44", "a45", "a46", "a47", "a48", "a49", "a50", "a51", "a52", "a53", "a54", "a55", "a56", "a57", "a58", "a59", "a60", "a61", "a62", "a63", "a64", "a65", "a66", "a67", "a68", "a69", "a70", "a71", "a72", "a73", "a74", "a75", "a76", "a77", "a78", "a79", "a80", "a81", "a82", "a83", "a84", "a85", "a86", "a87", "a88", "a89", "a90", "a91", "a92", "a93", "a94", "a95", "a96", "a97", "a98", "a99", "a100", "a101", "a102", "a103", "a104", "a105", "a106", "a107", "a108", "a109", "a110", "a111", "a112", "a113", "a114", "a115", "a116", "a117", "a118", "a119", "a120", "a121", "a122", "a123", "a124", "a125", "a126", "a127", "a128", "a129", "a130", "a131", "a132", "a133", "a134", "a135", "a136", "a137", "a138", "a139", "a140", "a141", "a142", "a143", "a144", "a145", "a146", "a147", "a148", "a149", "a150", "a151", "a152", "a153", "a154", "a155", "a156", "a157", "a158", "a159", "a160", "a161", "a162", "a163", "a164", "a165", "a166", "a167", "a168", "a169", "a170", "a171", "a172", "a173", "a174", "a175", "a176", "a177", "a178", "a179", "a180", "a181", "a182", "a183", "a184", "a185", "a186", "a187", "a188", "a189", "a190", "a191", "a192", "a193", "a194", "a195", "a196", "a197", "a198", "a199", "a200", "a201", "a202", "a203", "a204", "a205", "a206", "a207", "a208", "a209", "a210", "a211", "a212", "a213", "a214", "a215", "a216", "a217", "a218", "a219", "a220", "a221", "a222", "a223", "a224", "a225", "a226", "a227", "a228", "a229", "a230", "a231", "a232", "a233", "a234", "a235", "a236", "a237", "a238", "a239", "a240", "a241", "a242", "a243", "a244", "a245", "a246", "a247", "a248", "a249", "a250", "a251", "a252", "a253", "a254", "a255", "s34", "s35", "s36", "s37", "s38", "s39", "s40", "s41", "s42", "s43", "s44", "s45", "s46", "s47", "s48", "s49", "s50", "s51", "s52", "s53", "s54", "s55", "s56", "s57", "s58", "s59", "s60", "s61", "s62", "s63", "s64", "s65", "s66", "s67", "s68", "s69", "s70", "s71", "s72", "s73", "s74", "s75", "s76", "s77", "s78", "s79", "s80", "s81", "s82", "s83", "s84", "s85", "s86", "s87", "s88", "s89", "s90", "s91", "s92", "s93", "s94", "s95", "s96", "s97", "s98", "s99", "vcc", "memory");
  if (__builtin_expect(flag != 0, 0)) {
    const unsigned w = tid >> 6, h = 4 * (bid & 7) + (bid >> 6), i = (bid >> 3) & 7;
    for (unsigned blk = 0; blk < 2; ++blk)
      if (flag >> blk & 1) exact_rows((const u16_t*)x, (const u16_t*)wq, (char*)ws, h, 128 * (blk ? i : 15 - i) + 64 * (w & 1) + 32 * (w >> 1), w, tid & 63);
    asm volatile("s_waitcnt vmcnt(0) lgkmcnt(0)" ::: "memory");
  }
  asm volatile(
R"ASM(s_mov_b32 s98, %[tl]
s_mov_b32 s99, %[th]
v_lshrrev_b32 v254, 6, %[tid]
v_and_b32 v253, 63, %[tid]
v_readfirstlane_b32 s68, v254
v_lshlrev_b32 v252, 4, v253
s_mov_b32 s89, %[bid]
s_nop 1
s_lshr_b32 s69, s68, 1
s_and_b32 s70, s68, 1
s_and_b32 s82, s89, 7
s_lshl_b32 s82, s82, 2
s_lshr_b32 s83, s89, 6
s_add_u32 s82, s82, s83
s_bfe_u32 s83, s89, 0x30003
s_lshl_b32 s83, s83, 5
s_add_u32 s77, s82, s83
s_lshl_b32 s77, s77, 3
s_add_u32 s77, s77, 0x5704000
s_waitcnt lgkmcnt(0)
s_barrier
mla_fused_c_start:
s_mov_b64 s[36:37], %[ws]
s_and_b32 s37, s37, 0xffff
s_mov_b32 s38, 0x5712000
s_mov_b32 s39, 0x20000
s_mov_b64 s[44:45], %[ws]
s_and_b32 s45, s45, 0xffff
s_mov_b32 s46, 0x5712000
s_mov_b32 s47, 0x20000
s_mov_b64 s[40:41], %[w2]
s_and_b32 s41, s41, 0xffff
s_mov_b32 s42, 0x1c0000
s_mov_b32 s43, 0x20000
s_mov_b64 s[62:63], %[wo]
s_and_b32 s63, s63, 0xffff
s_and_b32 s82, s89, 7
s_lshr_b32 s83, s89, 3
s_lshr_b32 s48, s82, 1
s_and_b32 s82, s82, 1
s_lshl_b32 s82, s82, 3
s_lshr_b32 s49, s83, 2
s_add_u32 s49, s49, s82
s_and_b32 s50, s83, 3
v_and_b32 v253, 63, %[tid]
v_bfe_u32 v0, v253, 2, 3
v_lshrrev_b32 v1, 2, v0
v_lshlrev_b32 v1, 1, v1
v_and_b32 v2, 3, v253
v_xor_b32 v2, v2, v1
v_lshrrev_b32 v1, 5, v253
v_lshl_add_u32 v2, v1, 2, v2
v_lshlrev_b32 v2, 4, v2
v_lshl_add_u32 v245, v0, 10, v2
s_mul_i32 s82, s49, 184
s_mul_i32 s83, s48, 14
s_add_u32 s83, s83, s82
s_lshl_b32 s83, s83, 13
s_lshl_b32 s84, s68, 12
s_add_u32 s84, s84, 0x3000000
s_add_u32 s51, s83, s84
s_lshl_b32 s83, s48, 5
s_add_u32 s83, s83, s82
s_add_u32 s83, s83, 56
s_lshl_b32 s83, s83, 13
s_add_u32 s59, s83, s84
s_lshl_b32 s84, s68, 14
s_lshl_b32 s83, s50, 8
s_add_u32 s84, s84, s83
s_mul_i32 s83, s48, 458752
s_add_u32 s52, s83, s84
s_lshl_b32 s83, s48, 20
s_add_u32 s60, s83, s84
s_mov_b32 s61, 0
s_mov_b32 s67, 0
s_lshl_b32 s82, s61, 14
s_add_u32 s51, s51, s82
s_lshl_b32 s82, s61, 16
s_add_u32 s52, s52, s82
s_lshl_b32 s82, s67, 14
s_add_u32 s59, s59, s82
s_lshl_b32 s82, s67, 16
s_add_u32 s60, s60, s82
s_mov_b32 s64, 7
s_mov_b32 s65, 114688
s_mov_b32 s66, 458752
s_lshl_b32 s57, s68, 12
s_lshl_b32 s58, s68, 11
s_add_u32 s58, s58, 16384
s_mov_b32 s54, 0
s_add_u32 s83, s58, s54
s_add_u32 s86, s48, 0
s_and_b32 s86, s86, 3
s_lshl_b32 s87, s69, 2
s_add_u32 s86, s86, s87
s_lshl_b32 s86, s86, 10
v_add_u32 v140, s86, v252
s_add_u32 s86, s48, 1
s_and_b32 s86, s86, 3
s_lshl_b32 s87, s69, 2
s_add_u32 s86, s86, s87
s_lshl_b32 s86, s86, 10
v_add_u32 v141, s86, v252
s_add_u32 m0, s83, 0
s_add_u32 s82, s52, 0
buffer_load_dwordx4 v245, s[40:43], s82 offen lds
s_add_u32 s86, s48, 2
s_and_b32 s86, s86, 3
s_lshl_b32 s87, s69, 2
s_add_u32 s86, s86, s87
s_lshl_b32 s86, s86, 10
v_add_u32 v142, s86, v252
s_add_u32 s86, s48, 3
s_and_b32 s86, s86, 3
s_lshl_b32 s87, s69, 2
s_add_u32 s86, s86, s87
s_lshl_b32 s86, s86, 10
v_add_u32 v143, s86, v252
s_add_u32 m0, s83, 1024
s_add_u32 s82, s52, 8192
buffer_load_dwordx4 v245, s[40:43], s82 offen lds
v_lshrrev_b32 v1, 4, v253
v_lshrrev_b32 v2, 1, v1
s_add_u32 m0, s83, 8192
s_add_u32 s82, s52, 128
buffer_load_dwordx4 v245, s[40:43], s82 offen lds
v_lshlrev_b32 v2, 10, v2
v_and_b32 v3, 1, v1
s_add_u32 m0, s83, 9216
s_add_u32 s82, s52, 8320
buffer_load_dwordx4 v245, s[40:43], s82 offen lds
v_lshl_add_u32 v2, v3, 8, v2
v_bfe_u32 v4, v253, 2, 2
s_add_u32 m0, s57, s54
s_mov_b32 s82, s51
buffer_load_dwordx4 v252, s[44:47], s82 offen offset:0 sc1 lds
v_lshl_add_u32 v2, v4, 6, v2
v_and_b32 v4, 3, v253
buffer_load_dwordx4 v252, s[44:47], s82 offen offset:1024 sc1 lds
v_lshl_add_u32 v2, v4, 3, v2
v_lshl_add_u32 v4, v3, 5, v2
buffer_load_dwordx4 v252, s[44:47], s82 offen offset:2048 sc1 lds
v_xor_b32 v3, 1, v3
v_lshl_add_u32 v5, v3, 5, v2
buffer_load_dwordx4 v252, s[44:47], s82 offen offset:3072 sc1 lds
s_lshl_b32 s86, s70, 13
s_add_u32 s86, s86, 16384
v_add_u32 v240, s86, v4
v_add_u32 v241, s86, v5
v_mov_b32 v243, v240
s_add_u32 s54, s54, 32768
s_cmp_eq_u32 s54, 131072
s_cselect_b32 s54, 0, s54
s_add_u32 s51, s51, 16384
s_add_u32 s52, s52, 65536
s_add_u32 s83, s58, s54
v_mov_b32 v244, v241
v_mov_b32 v144, v140
s_add_u32 m0, s83, 0
s_add_u32 s82, s52, 0
buffer_load_dwordx4 v245, s[40:43], s82 offen lds
v_mov_b32 v145, v141
v_mov_b32 v146, v142
s_add_u32 m0, s83, 1024
s_add_u32 s82, s52, 8192
buffer_load_dwordx4 v245, s[40:43], s82 offen lds
v_mov_b32 v147, v143
v_mov_b64 v[64:65], 0
s_add_u32 m0, s83, 8192
s_add_u32 s82, s52, 128
buffer_load_dwordx4 v245, s[40:43], s82 offen lds
v_mov_b64 v[66:67], 0
v_mov_b64 v[68:69], 0
s_add_u32 m0, s83, 9216
s_add_u32 s82, s52, 8320
buffer_load_dwordx4 v245, s[40:43], s82 offen lds
v_mov_b64 v[70:71], 0
v_mov_b64 v[72:73], 0
s_add_u32 m0, s57, s54
s_mov_b32 s82, s51
buffer_load_dwordx4 v252, s[44:47], s82 offen offset:0 sc1 lds
v_mov_b64 v[74:75], 0
v_mov_b64 v[76:77], 0
buffer_load_dwordx4 v252, s[44:47], s82 offen offset:1024 sc1 lds
v_mov_b64 v[78:79], 0
v_mov_b64 v[80:81], 0
buffer_load_dwordx4 v252, s[44:47], s82 offen offset:2048 sc1 lds
v_mov_b64 v[82:83], 0
v_mov_b64 v[84:85], 0
buffer_load_dwordx4 v252, s[44:47], s82 offen offset:3072 sc1 lds
v_mov_b64 v[86:87], 0
v_mov_b64 v[88:89], 0
s_add_u32 s54, s54, 32768
s_cmp_eq_u32 s54, 131072
s_cselect_b32 s54, 0, s54
s_add_u32 s51, s51, 16384
s_add_u32 s52, s52, 65536
s_waitcnt vmcnt(16)
s_barrier
s_cmp_lg_u32 s68, 0
s_cbranch_scc1 mla_fused_f_pub9
v_and_b32 v152, 63, %[tid]
v_cmp_eq_u32 vcc, 0, v152
v_mov_b32 v150, 0x7ffffff0
v_mov_b32 v151, 0
v_cndmask_b32 v152, v150, v151, vcc
v_mov_b32 v150, s98
v_mov_b32 v151, s99
buffer_store_dwordx2 v[150:151], v152, s[36:39], s77 offen sc1
mla_fused_f_pub9:
s_add_u32 s83, s58, s54
v_mov_b64 v[90:91], 0
v_mov_b64 v[92:93], 0
s_add_u32 m0, s83, 0
s_add_u32 s82, s52, 0
buffer_load_dwordx4 v245, s[40:43], s82 offen lds
v_mov_b64 v[94:95], 0
v_mov_b64 v[96:97], 0
s_add_u32 m0, s83, 1024
s_add_u32 s82, s52, 8192
buffer_load_dwordx4 v245, s[40:43], s82 offen lds
v_mov_b64 v[98:99], 0
v_mov_b64 v[100:101], 0
s_add_u32 m0, s83, 8192
s_add_u32 s82, s52, 128
buffer_load_dwordx4 v245, s[40:43], s82 offen lds
v_mov_b64 v[102:103], 0
v_mov_b64 v[104:105], 0
s_add_u32 m0, s83, 9216
s_add_u32 s82, s52, 8320
buffer_load_dwordx4 v245, s[40:43], s82 offen lds
v_mov_b64 v[106:107], 0
v_mov_b64 v[108:109], 0
s_add_u32 m0, s57, s54
s_mov_b32 s82, s51
buffer_load_dwordx4 v252, s[44:47], s82 offen offset:0 sc1 lds
v_mov_b64 v[110:111], 0
v_mov_b64 v[112:113], 0
buffer_load_dwordx4 v252, s[44:47], s82 offen offset:1024 sc1 lds
v_mov_b64 v[114:115], 0
v_mov_b64 v[116:117], 0
buffer_load_dwordx4 v252, s[44:47], s82 offen offset:2048 sc1 lds
v_mov_b64 v[118:119], 0
v_mov_b64 v[120:121], 0
buffer_load_dwordx4 v252, s[44:47], s82 offen offset:3072 sc1 lds
v_mov_b64 v[122:123], 0
v_mov_b64 v[124:125], 0
s_add_u32 s54, s54, 32768
s_cmp_eq_u32 s54, 131072
s_cselect_b32 s54, 0, s54
s_add_u32 s51, s51, 16384
s_add_u32 s52, s52, 65536
s_add_u32 s83, s58, s54
v_mov_b64 v[126:127], 0
s_add_u32 m0, s83, 0
s_add_u32 s82, s52, 0
buffer_load_dwordx4 v245, s[40:43], s82 offen lds
s_add_u32 m0, s83, 1024
s_add_u32 s82, s52, 8192
buffer_load_dwordx4 v245, s[40:43], s82 offen lds
s_add_u32 m0, s83, 8192
s_add_u32 s82, s52, 128
buffer_load_dwordx4 v245, s[40:43], s82 offen lds
s_add_u32 m0, s83, 9216
s_add_u32 s82, s52, 8320
buffer_load_dwordx4 v245, s[40:43], s82 offen lds
s_add_u32 m0, s57, s54
s_mov_b32 s82, s51
buffer_load_dwordx4 v252, s[44:47], s82 offen offset:0 sc1 lds
buffer_load_dwordx4 v252, s[44:47], s82 offen offset:1024 sc1 lds
buffer_load_dwordx4 v252, s[44:47], s82 offen offset:2048 sc1 lds
buffer_load_dwordx4 v252, s[44:47], s82 offen offset:3072 sc1 lds
s_add_u32 s54, s54, 32768
s_cmp_eq_u32 s54, 131072
s_cselect_b32 s54, 0, s54
s_add_u32 s51, s51, 16384
s_add_u32 s52, s52, 65536
s_mov_b32 s55, 0
s_sub_u32 s71, 15, s49
s_cmp_lt_u32 s49, 8
s_cselect_b32 s71, s49, s71
s_lshl_b32 s71, s71, 8
s_add_u32 s71, s71, 0x5704000
s_waitcnt vmcnt(24)
s_barrier
ds_read_b64_tr_b16 a[32:33], v243 offset:0
ds_read_b64_tr_b16 a[34:35], v243 offset:2048
ds_read_b64_tr_b16 a[36:37], v243 offset:4096
ds_read_b64_tr_b16 a[38:39], v243 offset:6144
ds_read_b128 a[0:3], v144 offset:0
ds_read_b128 a[4:7], v144 offset:8192
ds_read_b128 a[8:11], v145 offset:0
ds_read_b128 a[12:15], v145 offset:8192
ds_read_b128 a[16:19], v146 offset:0
ds_read_b128 a[20:23], v146 offset:8192
ds_read_b128 a[24:27], v147 offset:0
ds_read_b128 a[28:31], v147 offset:8192
ds_read_b64_tr_b16 a[40:41], v244 offset:0
ds_read_b64_tr_b16 a[42:43], v244 offset:2048
ds_read_b64_tr_b16 a[44:45], v244 offset:4096
ds_read_b64_tr_b16 a[46:47], v244 offset:6144
ds_read_b64_tr_b16 a[48:49], v243 offset:512
ds_read_b64_tr_b16 a[50:51], v243 offset:2560
ds_read_b64_tr_b16 a[52:53], v243 offset:4608
ds_read_b64_tr_b16 a[54:55], v243 offset:6656
ds_read_b64_tr_b16 a[56:57], v244 offset:512
ds_read_b64_tr_b16 a[58:59], v244 offset:2560
ds_read_b64_tr_b16 a[60:61], v244 offset:4608
ds_read_b64_tr_b16 a[62:63], v244 offset:6656
s_waitcnt vmcnt(16) lgkmcnt(0)
s_barrier
v_mfma_f32_16x16x32_bf16 v[0:3], a[32:35], a[0:3], 0
s_add_u32 s55, s55, 32768
s_cmp_eq_u32 s55, 131072
s_cselect_b32 s55, 0, s55
v_mfma_f32_16x16x32_bf16 v[0:3], a[36:39], a[4:7], v[0:3]
v_add_u32 v243, s55, v240
v_add_u32 v244, s55, v241
v_mfma_f32_16x16x32_bf16 v[4:7], a[32:35], a[8:11], 0
v_add_u32 v144, s55, v140
v_add_u32 v145, s55, v141
v_mfma_f32_16x16x32_bf16 v[4:7], a[36:39], a[12:15], v[4:7]
v_add_u32 v146, s55, v142
v_add_u32 v147, s55, v143
v_mfma_f32_16x16x32_bf16 v[8:11], a[32:35], a[16:19], 0
s_add_u32 s83, s58, s54
ds_read_b64_tr_b16 a[96:97], v243 offset:0
ds_read_b64_tr_b16 a[98:99], v243 offset:2048
v_mfma_f32_16x16x32_bf16 v[8:11], a[36:39], a[20:23], v[8:11]
s_add_u32 m0, s83, 0
s_add_u32 s82, s52, 0
ds_read_b64_tr_b16 a[100:101], v243 offset:4096
v_mfma_f32_16x16x32_bf16 v[12:15], a[32:35], a[24:27], 0
buffer_load_dwordx4 v245, s[40:43], s82 offen lds
v_mfma_f32_16x16x32_bf16 v[12:15], a[36:39], a[28:31], v[12:15]
ds_read_b64_tr_b16 a[102:103], v243 offset:6144
ds_read_b128 a[64:67], v144 offset:0
v_mfma_f32_16x16x32_bf16 v[16:19], a[40:43], a[0:3], 0
s_add_u32 m0, s83, 1024
s_add_u32 s82, s52, 8192
ds_read_b128 a[68:71], v144 offset:8192
v_mfma_f32_16x16x32_bf16 v[16:19], a[44:47], a[4:7], v[16:19]
buffer_load_dwordx4 v245, s[40:43], s82 offen lds
v_mfma_f32_16x16x32_bf16 v[20:23], a[40:43], a[8:11], 0
s_add_u32 m0, s83, 8192
s_add_u32 s82, s52, 128
ds_read_b128 a[72:75], v145 offset:0
v_mfma_f32_16x16x32_bf16 v[20:23], a[44:47], a[12:15], v[20:23]
ds_read_b128 a[76:79], v145 offset:8192
ds_read_b128 a[80:83], v146 offset:0
v_mfma_f32_16x16x32_bf16 v[24:27], a[40:43], a[16:19], 0
buffer_load_dwordx4 v245, s[40:43], s82 offen lds
v_mfma_f32_16x16x32_bf16 v[24:27], a[44:47], a[20:23], v[24:27]
s_add_u32 m0, s83, 9216
s_add_u32 s82, s52, 8320
ds_read_b128 a[84:87], v146 offset:8192
v_mfma_f32_16x16x32_bf16 v[28:31], a[40:43], a[24:27], 0
ds_read_b128 a[88:91], v147 offset:0
ds_read_b128 a[92:95], v147 offset:8192
v_mfma_f32_16x16x32_bf16 v[28:31], a[44:47], a[28:31], v[28:31]
buffer_load_dwordx4 v245, s[40:43], s82 offen lds
v_mfma_f32_16x16x32_bf16 v[32:35], a[48:51], a[0:3], 0
s_add_u32 m0, s57, s54
s_mov_b32 s82, s51
ds_read_b64_tr_b16 a[104:105], v244 offset:0
v_mfma_f32_16x16x32_bf16 v[32:35], a[52:55], a[4:7], v[32:35]
buffer_load_dwordx4 v252, s[44:47], s82 offen offset:0 sc1 lds
v_mfma_f32_16x16x32_bf16 v[36:39], a[48:51], a[8:11], 0
ds_read_b64_tr_b16 a[106:107], v244 offset:2048
ds_read_b64_tr_b16 a[108:109], v244 offset:4096
v_mfma_f32_16x16x32_bf16 v[36:39], a[52:55], a[12:15], v[36:39]
buffer_load_dwordx4 v252, s[44:47], s82 offen offset:1024 sc1 lds
v_mfma_f32_16x16x32_bf16 v[40:43], a[48:51], a[16:19], 0
buffer_load_dwordx4 v252, s[44:47], s82 offen offset:2048 sc1 lds
v_mfma_f32_16x16x32_bf16 v[40:43], a[52:55], a[20:23], v[40:43]
ds_read_b64_tr_b16 a[110:111], v244 offset:6144
ds_read_b64_tr_b16 a[112:113], v243 offset:512
v_mfma_f32_16x16x32_bf16 v[44:47], a[48:51], a[24:27], 0
buffer_load_dwordx4 v252, s[44:47], s82 offen offset:3072 sc1 lds
s_add_u32 s54, s54, 32768
v_mfma_f32_16x16x32_bf16 v[44:47], a[52:55], a[28:31], v[44:47]
)ASM"
R"ASM(s_cmp_eq_u32 s54, 131072
s_cselect_b32 s54, 0, s54
s_add_u32 s51, s51, 16384
v_mfma_f32_16x16x32_bf16 v[48:51], a[56:59], a[0:3], 0
s_add_u32 s52, s52, 65536
ds_read_b64_tr_b16 a[114:115], v243 offset:2560
ds_read_b64_tr_b16 a[116:117], v243 offset:4608
v_mfma_f32_16x16x32_bf16 v[48:51], a[60:63], a[4:7], v[48:51]
ds_read_b64_tr_b16 a[118:119], v243 offset:6656
ds_read_b64_tr_b16 a[120:121], v244 offset:512
v_mfma_f32_16x16x32_bf16 v[52:55], a[56:59], a[8:11], 0
ds_read_b64_tr_b16 a[122:123], v244 offset:2560
ds_read_b64_tr_b16 a[124:125], v244 offset:4608
v_mfma_f32_16x16x32_bf16 v[52:55], a[60:63], a[12:15], v[52:55]
ds_read_b64_tr_b16 a[126:127], v244 offset:6656
v_mfma_f32_16x16x32_bf16 v[56:59], a[56:59], a[16:19], 0
v_mfma_f32_16x16x32_bf16 v[56:59], a[60:63], a[20:23], v[56:59]
v_mfma_f32_16x16x32_bf16 v[60:63], a[56:59], a[24:27], 0
v_mfma_f32_16x16x32_bf16 v[60:63], a[60:63], a[28:31], v[60:63]
s_waitcnt vmcnt(16) lgkmcnt(0)
s_barrier
v_mfma_f32_16x16x32_bf16 v[0:3], a[96:99], a[64:67], v[0:3]
s_add_u32 s55, s55, 32768
s_cmp_eq_u32 s55, 131072
s_cselect_b32 s55, 0, s55
v_mfma_f32_16x16x32_bf16 v[0:3], a[100:103], a[68:71], v[0:3]
v_add_u32 v243, s55, v240
v_add_u32 v244, s55, v241
v_mfma_f32_16x16x32_bf16 v[4:7], a[96:99], a[72:75], v[4:7]
v_add_u32 v144, s55, v140
v_add_u32 v145, s55, v141
v_mfma_f32_16x16x32_bf16 v[4:7], a[100:103], a[76:79], v[4:7]
v_add_u32 v146, s55, v142
v_add_u32 v147, s55, v143
v_mfma_f32_16x16x32_bf16 v[8:11], a[96:99], a[80:83], v[8:11]
s_add_u32 s83, s58, s54
ds_read_b64_tr_b16 a[32:33], v243 offset:0
ds_read_b64_tr_b16 a[34:35], v243 offset:2048
v_mfma_f32_16x16x32_bf16 v[8:11], a[100:103], a[84:87], v[8:11]
s_add_u32 m0, s83, 0
s_add_u32 s82, s52, 0
ds_read_b64_tr_b16 a[36:37], v243 offset:4096
v_mfma_f32_16x16x32_bf16 v[12:15], a[96:99], a[88:91], v[12:15]
buffer_load_dwordx4 v245, s[40:43], s82 offen lds
v_mfma_f32_16x16x32_bf16 v[12:15], a[100:103], a[92:95], v[12:15]
ds_read_b64_tr_b16 a[38:39], v243 offset:6144
ds_read_b128 a[0:3], v144 offset:0
v_mfma_f32_16x16x32_bf16 v[16:19], a[104:107], a[64:67], v[16:19]
s_add_u32 m0, s83, 1024
s_add_u32 s82, s52, 8192
ds_read_b128 a[4:7], v144 offset:8192
v_mfma_f32_16x16x32_bf16 v[16:19], a[108:111], a[68:71], v[16:19]
buffer_load_dwordx4 v245, s[40:43], s82 offen lds
v_mfma_f32_16x16x32_bf16 v[20:23], a[104:107], a[72:75], v[20:23]
s_add_u32 m0, s83, 8192
s_add_u32 s82, s52, 128
ds_read_b128 a[8:11], v145 offset:0
v_mfma_f32_16x16x32_bf16 v[20:23], a[108:111], a[76:79], v[20:23]
ds_read_b128 a[12:15], v145 offset:8192
ds_read_b128 a[16:19], v146 offset:0
v_mfma_f32_16x16x32_bf16 v[24:27], a[104:107], a[80:83], v[24:27]
buffer_load_dwordx4 v245, s[40:43], s82 offen lds
v_mfma_f32_16x16x32_bf16 v[24:27], a[108:111], a[84:87], v[24:27]
s_add_u32 m0, s83, 9216
s_add_u32 s82, s52, 8320
ds_read_b128 a[20:23], v146 offset:8192
v_mfma_f32_16x16x32_bf16 v[28:31], a[104:107], a[88:91], v[28:31]
ds_read_b128 a[24:27], v147 offset:0
ds_read_b128 a[28:31], v147 offset:8192
v_mfma_f32_16x16x32_bf16 v[28:31], a[108:111], a[92:95], v[28:31]
buffer_load_dwordx4 v245, s[40:43], s82 offen lds
v_mfma_f32_16x16x32_bf16 v[32:35], a[112:115], a[64:67], v[32:35]
s_add_u32 m0, s57, s54
s_mov_b32 s82, s51
ds_read_b64_tr_b16 a[40:41], v244 offset:0
v_mfma_f32_16x16x32_bf16 v[32:35], a[116:119], a[68:71], v[32:35]
buffer_load_dwordx4 v252, s[44:47], s82 offen offset:0 sc1 lds
v_mfma_f32_16x16x32_bf16 v[36:39], a[112:115], a[72:75], v[36:39]
ds_read_b64_tr_b16 a[42:43], v244 offset:2048
ds_read_b64_tr_b16 a[44:45], v244 offset:4096
v_mfma_f32_16x16x32_bf16 v[36:39], a[116:119], a[76:79], v[36:39]
buffer_load_dwordx4 v252, s[44:47], s82 offen offset:1024 sc1 lds
v_mfma_f32_16x16x32_bf16 v[40:43], a[112:115], a[80:83], v[40:43]
buffer_load_dwordx4 v252, s[44:47], s82 offen offset:2048 sc1 lds
v_mfma_f32_16x16x32_bf16 v[40:43], a[116:119], a[84:87], v[40:43]
ds_read_b64_tr_b16 a[46:47], v244 offset:6144
ds_read_b64_tr_b16 a[48:49], v243 offset:512
v_mfma_f32_16x16x32_bf16 v[44:47], a[112:115], a[88:91], v[44:47]
buffer_load_dwordx4 v252, s[44:47], s82 offen offset:3072 sc1 lds
s_add_u32 s54, s54, 32768
v_mfma_f32_16x16x32_bf16 v[44:47], a[116:119], a[92:95], v[44:47]
s_cmp_eq_u32 s54, 131072
s_cselect_b32 s54, 0, s54
s_add_u32 s51, s51, 16384
v_mfma_f32_16x16x32_bf16 v[48:51], a[120:123], a[64:67], v[48:51]
s_add_u32 s52, s52, 65536
ds_read_b64_tr_b16 a[50:51], v243 offset:2560
ds_read_b64_tr_b16 a[52:53], v243 offset:4608
v_mfma_f32_16x16x32_bf16 v[48:51], a[124:127], a[68:71], v[48:51]
ds_read_b64_tr_b16 a[54:55], v243 offset:6656
ds_read_b64_tr_b16 a[56:57], v244 offset:512
v_mfma_f32_16x16x32_bf16 v[52:55], a[120:123], a[72:75], v[52:55]
ds_read_b64_tr_b16 a[58:59], v244 offset:2560
ds_read_b64_tr_b16 a[60:61], v244 offset:4608
v_mfma_f32_16x16x32_bf16 v[52:55], a[124:127], a[76:79], v[52:55]
ds_read_b64_tr_b16 a[62:63], v244 offset:6656
v_mfma_f32_16x16x32_bf16 v[56:59], a[120:123], a[80:83], v[56:59]
v_mfma_f32_16x16x32_bf16 v[56:59], a[124:127], a[84:87], v[56:59]
v_mfma_f32_16x16x32_bf16 v[60:63], a[120:123], a[88:91], v[60:63]
v_mfma_f32_16x16x32_bf16 v[60:63], a[124:127], a[92:95], v[60:63]
s_waitcnt vmcnt(16) lgkmcnt(0)
s_barrier
s_cmp_lg_u32 s68, 0
s_cbranch_scc1 mla_fused_c_pi10
v_and_b32 v152, 63, %[tid]
v_cmp_gt_u32 vcc, 32, v152
v_lshlrev_b32 v152, 3, v152
s_nop 2
s_mov_b64 s[92:93], vcc
v_mov_b32 v150, 0x7ffffff0
v_cndmask_b32 v152, v150, v152, vcc
buffer_load_dwordx2 v[150:151], v152, s[36:39], s71 offen sc1
mla_fused_c_pi10:
v_mfma_f32_16x16x32_bf16 v[0:3], a[32:35], a[0:3], v[0:3]
s_add_u32 s55, s55, 32768
s_cmp_eq_u32 s55, 131072
s_cselect_b32 s55, 0, s55
v_mfma_f32_16x16x32_bf16 v[0:3], a[36:39], a[4:7], v[0:3]
v_add_u32 v243, s55, v240
v_add_u32 v244, s55, v241
v_mfma_f32_16x16x32_bf16 v[4:7], a[32:35], a[8:11], v[4:7]
v_add_u32 v144, s55, v140
v_add_u32 v145, s55, v141
v_mfma_f32_16x16x32_bf16 v[4:7], a[36:39], a[12:15], v[4:7]
v_add_u32 v146, s55, v142
v_add_u32 v147, s55, v143
v_mfma_f32_16x16x32_bf16 v[8:11], a[32:35], a[16:19], v[8:11]
s_add_u32 s83, s58, s54
ds_read_b64_tr_b16 a[96:97], v243 offset:0
ds_read_b64_tr_b16 a[98:99], v243 offset:2048
v_mfma_f32_16x16x32_bf16 v[8:11], a[36:39], a[20:23], v[8:11]
s_add_u32 m0, s83, 0
s_add_u32 s82, s52, 0
ds_read_b64_tr_b16 a[100:101], v243 offset:4096
v_mfma_f32_16x16x32_bf16 v[12:15], a[32:35], a[24:27], v[12:15]
buffer_load_dwordx4 v245, s[40:43], s82 offen lds
v_mfma_f32_16x16x32_bf16 v[12:15], a[36:39], a[28:31], v[12:15]
ds_read_b64_tr_b16 a[102:103], v243 offset:6144
ds_read_b128 a[64:67], v144 offset:0
v_mfma_f32_16x16x32_bf16 v[16:19], a[40:43], a[0:3], v[16:19]
s_add_u32 m0, s83, 1024
s_add_u32 s82, s52, 8192
ds_read_b128 a[68:71], v144 offset:8192
v_mfma_f32_16x16x32_bf16 v[16:19], a[44:47], a[4:7], v[16:19]
buffer_load_dwordx4 v245, s[40:43], s82 offen lds
v_mfma_f32_16x16x32_bf16 v[20:23], a[40:43], a[8:11], v[20:23]
s_add_u32 m0, s83, 8192
s_add_u32 s82, s52, 128
ds_read_b128 a[72:75], v145 offset:0
v_mfma_f32_16x16x32_bf16 v[20:23], a[44:47], a[12:15], v[20:23]
ds_read_b128 a[76:79], v145 offset:8192
ds_read_b128 a[80:83], v146 offset:0
v_mfma_f32_16x16x32_bf16 v[24:27], a[40:43], a[16:19], v[24:27]
buffer_load_dwordx4 v245, s[40:43], s82 offen lds
v_mfma_f32_16x16x32_bf16 v[24:27], a[44:47], a[20:23], v[24:27]
s_add_u32 m0, s83, 9216
s_add_u32 s82, s52, 8320
ds_read_b128 a[84:87], v146 offset:8192
v_mfma_f32_16x16x32_bf16 v[28:31], a[40:43], a[24:27], v[28:31]
ds_read_b128 a[88:91], v147 offset:0
ds_read_b128 a[92:95], v147 offset:8192
v_mfma_f32_16x16x32_bf16 v[28:31], a[44:47], a[28:31], v[28:31]
buffer_load_dwordx4 v245, s[40:43], s82 offen lds
v_mfma_f32_16x16x32_bf16 v[32:35], a[48:51], a[0:3], v[32:35]
s_add_u32 m0, s57, s54
s_mov_b32 s82, s51
ds_read_b64_tr_b16 a[104:105], v244 offset:0
v_mfma_f32_16x16x32_bf16 v[32:35], a[52:55], a[4:7], v[32:35]
buffer_load_dwordx4 v252, s[44:47], s82 offen offset:0 sc1 lds
v_mfma_f32_16x16x32_bf16 v[36:39], a[48:51], a[8:11], v[36:39]
ds_read_b64_tr_b16 a[106:107], v244 offset:2048
ds_read_b64_tr_b16 a[108:109], v244 offset:4096
v_mfma_f32_16x16x32_bf16 v[36:39], a[52:55], a[12:15], v[36:39]
buffer_load_dwordx4 v252, s[44:47], s82 offen offset:1024 sc1 lds
v_mfma_f32_16x16x32_bf16 v[40:43], a[48:51], a[16:19], v[40:43]
buffer_load_dwordx4 v252, s[44:47], s82 offen offset:2048 sc1 lds
v_mfma_f32_16x16x32_bf16 v[40:43], a[52:55], a[20:23], v[40:43]
ds_read_b64_tr_b16 a[110:111], v244 offset:6144
ds_read_b64_tr_b16 a[112:113], v243 offset:512
ds_read_b64_tr_b16 a[114:115], v243 offset:2560
v_mfma_f32_16x16x32_bf16 v[44:47], a[48:51], a[24:27], v[44:47]
buffer_load_dwordx4 v252, s[44:47], s82 offen offset:3072 sc1 lds
s_add_u32 s54, s54, 32768
v_mfma_f32_16x16x32_bf16 v[44:47], a[52:55], a[28:31], v[44:47]
s_cmp_eq_u32 s54, 131072
s_cselect_b32 s54, 0, s54
s_mov_b32 s51, s59
ds_read_b64_tr_b16 a[116:117], v243 offset:4608
v_mfma_f32_16x16x32_bf16 v[48:51], a[56:59], a[0:3], v[48:51]
s_mov_b32 s52, s60
s_mov_b32 s40, s62
s_mov_b32 s41, s63
v_mfma_f32_16x16x32_bf16 v[48:51], a[60:63], a[4:7], v[48:51]
s_mov_b32 s42, 0x400000
ds_read_b64_tr_b16 a[118:119], v243 offset:6656
ds_read_b64_tr_b16 a[120:121], v244 offset:512
v_mfma_f32_16x16x32_bf16 v[52:55], a[56:59], a[8:11], v[52:55]
ds_read_b64_tr_b16 a[122:123], v244 offset:2560
ds_read_b64_tr_b16 a[124:125], v244 offset:4608
v_mfma_f32_16x16x32_bf16 v[52:55], a[60:63], a[12:15], v[52:55]
ds_read_b64_tr_b16 a[126:127], v244 offset:6656
v_mfma_f32_16x16x32_bf16 v[56:59], a[56:59], a[16:19], v[56:59]
v_mfma_f32_16x16x32_bf16 v[56:59], a[60:63], a[20:23], v[56:59]
v_mfma_f32_16x16x32_bf16 v[60:63], a[56:59], a[24:27], v[60:63]
v_mfma_f32_16x16x32_bf16 v[60:63], a[60:63], a[28:31], v[60:63]
s_cmp_lg_u32 s68, 0
s_cbranch_scc1 mla_fused_c_pc11x
s_mov_b32 s90, 262144
s_waitcnt vmcnt(8)
s_branch mla_fused_c_pc11c
mla_fused_c_pc11:
buffer_load_dwordx2 v[150:151], v152, s[36:39], s71 offen sc1
s_waitcnt vmcnt(0)
mla_fused_c_pc11c:
v_cmp_eq_u32 vcc, s98, v150
v_cmp_eq_u32 s[94:95], s99, v151
s_nop 3
s_and_b64 vcc, vcc, s[94:95]
s_and_b64 vcc, vcc, s[92:93]
s_cmp_eq_u64 vcc, s[92:93]
s_cbranch_scc1 mla_fused_c_pc11d
s_sub_u32 s90, s90, 1
s_cmp_eq_u32 s90, 0
s_cbranch_scc1 mla_fused_c_pc11d
s_sleep 2
s_branch mla_fused_c_pc11
mla_fused_c_pc11d:
mla_fused_c_pc11x:
s_waitcnt vmcnt(16) lgkmcnt(0)
s_barrier
v_mfma_f32_16x16x32_bf16 v[0:3], a[96:99], a[64:67], v[0:3]
s_add_u32 s55, s55, 32768
s_cmp_eq_u32 s55, 131072
s_cselect_b32 s55, 0, s55
v_mfma_f32_16x16x32_bf16 v[0:3], a[100:103], a[68:71], v[0:3]
v_add_u32 v243, s55, v240
v_add_u32 v244, s55, v241
v_mfma_f32_16x16x32_bf16 v[4:7], a[96:99], a[72:75], v[4:7]
v_add_u32 v144, s55, v140
v_add_u32 v145, s55, v141
v_mfma_f32_16x16x32_bf16 v[4:7], a[100:103], a[76:79], v[4:7]
v_add_u32 v146, s55, v142
v_add_u32 v147, s55, v143
v_mfma_f32_16x16x32_bf16 v[8:11], a[96:99], a[80:83], v[8:11]
s_add_u32 s83, s58, s54
ds_read_b64_tr_b16 a[32:33], v243 offset:0
ds_read_b64_tr_b16 a[34:35], v243 offset:2048
v_mfma_f32_16x16x32_bf16 v[8:11], a[100:103], a[84:87], v[8:11]
s_add_u32 m0, s83, 0
s_add_u32 s82, s52, 0
ds_read_b64_tr_b16 a[36:37], v243 offset:4096
v_mfma_f32_16x16x32_bf16 v[12:15], a[96:99], a[88:91], v[12:15]
buffer_load_dwordx4 v245, s[40:43], s82 offen lds
v_mfma_f32_16x16x32_bf16 v[12:15], a[100:103], a[92:95], v[12:15]
ds_read_b64_tr_b16 a[38:39], v243 offset:6144
ds_read_b128 a[0:3], v144 offset:0
v_mfma_f32_16x16x32_bf16 v[16:19], a[104:107], a[64:67], v[16:19]
s_add_u32 m0, s83, 1024
s_add_u32 s82, s52, 8192
ds_read_b128 a[4:7], v144 offset:8192
v_mfma_f32_16x16x32_bf16 v[16:19], a[108:111], a[68:71], v[16:19]
buffer_load_dwordx4 v245, s[40:43], s82 offen lds
v_mfma_f32_16x16x32_bf16 v[20:23], a[104:107], a[72:75], v[20:23]
s_add_u32 m0, s83, 8192
s_add_u32 s82, s52, 128
ds_read_b128 a[8:11], v145 offset:0
v_mfma_f32_16x16x32_bf16 v[20:23], a[108:111], a[76:79], v[20:23]
ds_read_b128 a[12:15], v145 offset:8192
ds_read_b128 a[16:19], v146 offset:0
v_mfma_f32_16x16x32_bf16 v[24:27], a[104:107], a[80:83], v[24:27]
)ASM"
R"ASM(buffer_load_dwordx4 v245, s[40:43], s82 offen lds
v_mfma_f32_16x16x32_bf16 v[24:27], a[108:111], a[84:87], v[24:27]
s_add_u32 m0, s83, 9216
s_add_u32 s82, s52, 8320
ds_read_b128 a[20:23], v146 offset:8192
v_mfma_f32_16x16x32_bf16 v[28:31], a[104:107], a[88:91], v[28:31]
ds_read_b128 a[24:27], v147 offset:0
ds_read_b128 a[28:31], v147 offset:8192
v_mfma_f32_16x16x32_bf16 v[28:31], a[108:111], a[92:95], v[28:31]
buffer_load_dwordx4 v245, s[40:43], s82 offen lds
v_mfma_f32_16x16x32_bf16 v[32:35], a[112:115], a[64:67], v[32:35]
s_add_u32 m0, s57, s54
s_mov_b32 s82, s51
ds_read_b64_tr_b16 a[40:41], v244 offset:0
v_mfma_f32_16x16x32_bf16 v[32:35], a[116:119], a[68:71], v[32:35]
buffer_load_dwordx4 v252, s[44:47], s82 offen offset:0 sc1 lds
v_mfma_f32_16x16x32_bf16 v[36:39], a[112:115], a[72:75], v[36:39]
ds_read_b64_tr_b16 a[42:43], v244 offset:2048
ds_read_b64_tr_b16 a[44:45], v244 offset:4096
v_mfma_f32_16x16x32_bf16 v[36:39], a[116:119], a[76:79], v[36:39]
buffer_load_dwordx4 v252, s[44:47], s82 offen offset:1024 sc1 lds
v_mfma_f32_16x16x32_bf16 v[40:43], a[112:115], a[80:83], v[40:43]
buffer_load_dwordx4 v252, s[44:47], s82 offen offset:2048 sc1 lds
v_mfma_f32_16x16x32_bf16 v[40:43], a[116:119], a[84:87], v[40:43]
ds_read_b64_tr_b16 a[46:47], v244 offset:6144
ds_read_b64_tr_b16 a[48:49], v243 offset:512
v_mfma_f32_16x16x32_bf16 v[44:47], a[112:115], a[88:91], v[44:47]
buffer_load_dwordx4 v252, s[44:47], s82 offen offset:3072 sc1 lds
s_add_u32 s54, s54, 32768
v_mfma_f32_16x16x32_bf16 v[44:47], a[116:119], a[92:95], v[44:47]
s_cmp_eq_u32 s54, 131072
s_cselect_b32 s54, 0, s54
s_add_u32 s51, s51, 16384
v_mfma_f32_16x16x32_bf16 v[48:51], a[120:123], a[64:67], v[48:51]
s_add_u32 s52, s52, 65536
ds_read_b64_tr_b16 a[50:51], v243 offset:2560
ds_read_b64_tr_b16 a[52:53], v243 offset:4608
v_mfma_f32_16x16x32_bf16 v[48:51], a[124:127], a[68:71], v[48:51]
ds_read_b64_tr_b16 a[54:55], v243 offset:6656
ds_read_b64_tr_b16 a[56:57], v244 offset:512
v_mfma_f32_16x16x32_bf16 v[52:55], a[120:123], a[72:75], v[52:55]
ds_read_b64_tr_b16 a[58:59], v244 offset:2560
ds_read_b64_tr_b16 a[60:61], v244 offset:4608
v_mfma_f32_16x16x32_bf16 v[52:55], a[124:127], a[76:79], v[52:55]
ds_read_b64_tr_b16 a[62:63], v244 offset:6656
v_mfma_f32_16x16x32_bf16 v[56:59], a[120:123], a[80:83], v[56:59]
v_mfma_f32_16x16x32_bf16 v[56:59], a[124:127], a[84:87], v[56:59]
v_mfma_f32_16x16x32_bf16 v[60:63], a[120:123], a[88:91], v[60:63]
v_mfma_f32_16x16x32_bf16 v[60:63], a[124:127], a[92:95], v[60:63]
s_waitcnt vmcnt(16) lgkmcnt(0)
s_barrier
s_or_b32 s84, s89, s68
s_cmp_lg_u32 s84, 0
s_cbranch_scc1 mla_fused_c_ei12
v_and_b32 v153, 63, %[tid]
v_lshlrev_b32 v153, 5, v153
s_mov_b32 s84, 0x5702100
buffer_load_dwordx4 v[156:159], v153, s[36:39], s84 offen sc1
buffer_load_dwordx4 v[246:249], v153, s[36:39], s84 offen offset:16 sc1
mla_fused_c_ei12:
v_mfma_f32_16x16x32_bf16 v[0:3], a[32:35], a[0:3], v[0:3]
s_add_u32 s55, s55, 32768
s_cmp_eq_u32 s55, 131072
s_cselect_b32 s55, 0, s55
v_mfma_f32_16x16x32_bf16 v[0:3], a[36:39], a[4:7], v[0:3]
v_add_u32 v243, s55, v240
v_add_u32 v244, s55, v241
v_mfma_f32_16x16x32_bf16 v[4:7], a[32:35], a[8:11], v[4:7]
v_add_u32 v144, s55, v140
v_add_u32 v145, s55, v141
v_mfma_f32_16x16x32_bf16 v[4:7], a[36:39], a[12:15], v[4:7]
v_add_u32 v146, s55, v142
v_add_u32 v147, s55, v143
v_mfma_f32_16x16x32_bf16 v[8:11], a[32:35], a[16:19], v[8:11]
s_add_u32 s83, s58, s54
ds_read_b64_tr_b16 a[96:97], v243 offset:0
ds_read_b64_tr_b16 a[98:99], v243 offset:2048
v_mfma_f32_16x16x32_bf16 v[8:11], a[36:39], a[20:23], v[8:11]
s_add_u32 m0, s83, 0
s_add_u32 s82, s52, 0
ds_read_b64_tr_b16 a[100:101], v243 offset:4096
v_mfma_f32_16x16x32_bf16 v[12:15], a[32:35], a[24:27], v[12:15]
buffer_load_dwordx4 v245, s[40:43], s82 offen lds
v_mfma_f32_16x16x32_bf16 v[12:15], a[36:39], a[28:31], v[12:15]
ds_read_b64_tr_b16 a[102:103], v243 offset:6144
ds_read_b128 a[64:67], v144 offset:0
v_mfma_f32_16x16x32_bf16 v[16:19], a[40:43], a[0:3], v[16:19]
s_add_u32 m0, s83, 1024
s_add_u32 s82, s52, 8192
ds_read_b128 a[68:71], v144 offset:8192
v_mfma_f32_16x16x32_bf16 v[16:19], a[44:47], a[4:7], v[16:19]
buffer_load_dwordx4 v245, s[40:43], s82 offen lds
v_mfma_f32_16x16x32_bf16 v[20:23], a[40:43], a[8:11], v[20:23]
s_add_u32 m0, s83, 8192
s_add_u32 s82, s52, 128
ds_read_b128 a[72:75], v145 offset:0
v_mfma_f32_16x16x32_bf16 v[20:23], a[44:47], a[12:15], v[20:23]
ds_read_b128 a[76:79], v145 offset:8192
ds_read_b128 a[80:83], v146 offset:0
v_mfma_f32_16x16x32_bf16 v[24:27], a[40:43], a[16:19], v[24:27]
buffer_load_dwordx4 v245, s[40:43], s82 offen lds
v_mfma_f32_16x16x32_bf16 v[24:27], a[44:47], a[20:23], v[24:27]
s_add_u32 m0, s83, 9216
s_add_u32 s82, s52, 8320
ds_read_b128 a[84:87], v146 offset:8192
v_mfma_f32_16x16x32_bf16 v[28:31], a[40:43], a[24:27], v[28:31]
ds_read_b128 a[88:91], v147 offset:0
ds_read_b128 a[92:95], v147 offset:8192
v_mfma_f32_16x16x32_bf16 v[28:31], a[44:47], a[28:31], v[28:31]
buffer_load_dwordx4 v245, s[40:43], s82 offen lds
v_mfma_f32_16x16x32_bf16 v[32:35], a[48:51], a[0:3], v[32:35]
s_add_u32 m0, s57, s54
s_mov_b32 s82, s51
ds_read_b64_tr_b16 a[104:105], v244 offset:0
v_mfma_f32_16x16x32_bf16 v[32:35], a[52:55], a[4:7], v[32:35]
buffer_load_dwordx4 v252, s[44:47], s82 offen offset:0 sc1 lds
v_mfma_f32_16x16x32_bf16 v[36:39], a[48:51], a[8:11], v[36:39]
ds_read_b64_tr_b16 a[106:107], v244 offset:2048
ds_read_b64_tr_b16 a[108:109], v244 offset:4096
v_mfma_f32_16x16x32_bf16 v[36:39], a[52:55], a[12:15], v[36:39]
buffer_load_dwordx4 v252, s[44:47], s82 offen offset:1024 sc1 lds
v_mfma_f32_16x16x32_bf16 v[40:43], a[48:51], a[16:19], v[40:43]
buffer_load_dwordx4 v252, s[44:47], s82 offen offset:2048 sc1 lds
v_mfma_f32_16x16x32_bf16 v[40:43], a[52:55], a[20:23], v[40:43]
ds_read_b64_tr_b16 a[110:111], v244 offset:6144
ds_read_b64_tr_b16 a[112:113], v243 offset:512
v_mfma_f32_16x16x32_bf16 v[44:47], a[48:51], a[24:27], v[44:47]
buffer_load_dwordx4 v252, s[44:47], s82 offen offset:3072 sc1 lds
s_add_u32 s54, s54, 32768
v_mfma_f32_16x16x32_bf16 v[44:47], a[52:55], a[28:31], v[44:47]
s_cmp_eq_u32 s54, 131072
s_cselect_b32 s54, 0, s54
s_add_u32 s51, s51, 16384
v_mfma_f32_16x16x32_bf16 v[48:51], a[56:59], a[0:3], v[48:51]
s_add_u32 s52, s52, 65536
ds_read_b64_tr_b16 a[114:115], v243 offset:2560
ds_read_b64_tr_b16 a[116:117], v243 offset:4608
v_mfma_f32_16x16x32_bf16 v[48:51], a[60:63], a[4:7], v[48:51]
ds_read_b64_tr_b16 a[118:119], v243 offset:6656
ds_read_b64_tr_b16 a[120:121], v244 offset:512
v_mfma_f32_16x16x32_bf16 v[52:55], a[56:59], a[8:11], v[52:55]
ds_read_b64_tr_b16 a[122:123], v244 offset:2560
ds_read_b64_tr_b16 a[124:125], v244 offset:4608
v_mfma_f32_16x16x32_bf16 v[52:55], a[60:63], a[12:15], v[52:55]
ds_read_b64_tr_b16 a[126:127], v244 offset:6656
v_mfma_f32_16x16x32_bf16 v[56:59], a[56:59], a[16:19], v[56:59]
v_mfma_f32_16x16x32_bf16 v[56:59], a[60:63], a[20:23], v[56:59]
v_mfma_f32_16x16x32_bf16 v[60:63], a[56:59], a[24:27], v[60:63]
v_mfma_f32_16x16x32_bf16 v[60:63], a[60:63], a[28:31], v[60:63]
s_or_b32 s84, s89, s68
s_cmp_lg_u32 s84, 0
s_cbranch_scc1 mla_fused_c_ec13x
s_mov_b32 s90, 262144
s_mov_b32 s84, 0x5702100
s_waitcnt vmcnt(8)
s_branch mla_fused_c_ec13c
mla_fused_c_ec13:
buffer_load_dwordx4 v[156:159], v153, s[36:39], s84 offen sc1
buffer_load_dwordx4 v[246:249], v153, s[36:39], s84 offen offset:16 sc1
s_waitcnt vmcnt(0)
mla_fused_c_ec13c:
v_xor_b32 v156, s98, v156
v_xor_b32 v157, s99, v157
v_xor_b32 v158, s98, v158
v_xor_b32 v159, s99, v159
v_xor_b32 v246, s98, v246
v_xor_b32 v247, s99, v247
v_xor_b32 v248, s98, v248
v_xor_b32 v249, s99, v249
v_or3_b32 v156, v156, v157, v158
v_or3_b32 v246, v246, v247, v248
v_or3_b32 v156, v156, v159, v249
v_or_b32 v156, v156, v246
v_cmp_ne_u32 vcc, 0, v156
s_nop 3
s_cmp_eq_u64 vcc, 0
s_cbranch_scc1 mla_fused_c_ec13d
s_sub_u32 s90, s90, 1
s_cmp_eq_u32 s90, 0
s_cbranch_scc1 mla_fused_c_ec13d
s_sleep 2
s_branch mla_fused_c_ec13
mla_fused_c_ec13d:
s_mov_b32 s84, 0x5702000
s_cmp_lg_u32 s68, 0
s_cbranch_scc1 mla_fused_c_pub14
v_and_b32 v153, 63, %[tid]
v_cmp_eq_u32 vcc, 0, v153
v_mov_b32 v156, 0x7ffffff0
v_mov_b32 v157, 0
v_cndmask_b32 v153, v156, v157, vcc
v_mov_b32 v156, s98
v_mov_b32 v157, s99
buffer_store_dwordx2 v[156:157], v153, s[36:39], s84 offen sc1
mla_fused_c_pub14:
mla_fused_c_ec13x:
s_waitcnt vmcnt(16) lgkmcnt(0)
s_barrier
v_mfma_f32_16x16x32_bf16 v[0:3], a[96:99], a[64:67], v[0:3]
s_add_u32 s55, s55, 32768
s_cmp_eq_u32 s55, 131072
s_cselect_b32 s55, 0, s55
v_mfma_f32_16x16x32_bf16 v[0:3], a[100:103], a[68:71], v[0:3]
v_add_u32 v243, s55, v240
v_add_u32 v244, s55, v241
v_mfma_f32_16x16x32_bf16 v[4:7], a[96:99], a[72:75], v[4:7]
v_add_u32 v144, s55, v140
v_add_u32 v145, s55, v141
v_mfma_f32_16x16x32_bf16 v[4:7], a[100:103], a[76:79], v[4:7]
v_add_u32 v146, s55, v142
v_add_u32 v147, s55, v143
v_mfma_f32_16x16x32_bf16 v[8:11], a[96:99], a[80:83], v[8:11]
s_add_u32 s83, s58, s54
ds_read_b64_tr_b16 a[32:33], v243 offset:0
ds_read_b64_tr_b16 a[34:35], v243 offset:2048
v_mfma_f32_16x16x32_bf16 v[8:11], a[100:103], a[84:87], v[8:11]
s_add_u32 m0, s83, 0
s_add_u32 s82, s52, 0
ds_read_b64_tr_b16 a[36:37], v243 offset:4096
v_mfma_f32_16x16x32_bf16 v[12:15], a[96:99], a[88:91], v[12:15]
buffer_load_dwordx4 v245, s[40:43], s82 offen lds
v_mfma_f32_16x16x32_bf16 v[12:15], a[100:103], a[92:95], v[12:15]
ds_read_b64_tr_b16 a[38:39], v243 offset:6144
ds_read_b128 a[0:3], v144 offset:0
v_mfma_f32_16x16x32_bf16 v[16:19], a[104:107], a[64:67], v[16:19]
s_add_u32 m0, s83, 1024
s_add_u32 s82, s52, 8192
ds_read_b128 a[4:7], v144 offset:8192
v_mfma_f32_16x16x32_bf16 v[16:19], a[108:111], a[68:71], v[16:19]
buffer_load_dwordx4 v245, s[40:43], s82 offen lds
v_mfma_f32_16x16x32_bf16 v[20:23], a[104:107], a[72:75], v[20:23]
s_add_u32 m0, s83, 8192
s_add_u32 s82, s52, 128
ds_read_b128 a[8:11], v145 offset:0
v_mfma_f32_16x16x32_bf16 v[20:23], a[108:111], a[76:79], v[20:23]
ds_read_b128 a[12:15], v145 offset:8192
ds_read_b128 a[16:19], v146 offset:0
v_mfma_f32_16x16x32_bf16 v[24:27], a[104:107], a[80:83], v[24:27]
buffer_load_dwordx4 v245, s[40:43], s82 offen lds
v_mfma_f32_16x16x32_bf16 v[24:27], a[108:111], a[84:87], v[24:27]
s_add_u32 m0, s83, 9216
s_add_u32 s82, s52, 8320
ds_read_b128 a[20:23], v146 offset:8192
v_mfma_f32_16x16x32_bf16 v[28:31], a[104:107], a[88:91], v[28:31]
ds_read_b128 a[24:27], v147 offset:0
ds_read_b128 a[28:31], v147 offset:8192
v_mfma_f32_16x16x32_bf16 v[28:31], a[108:111], a[92:95], v[28:31]
buffer_load_dwordx4 v245, s[40:43], s82 offen lds
v_mfma_f32_16x16x32_bf16 v[32:35], a[112:115], a[64:67], v[32:35]
s_add_u32 m0, s57, s54
s_mov_b32 s82, s51
ds_read_b64_tr_b16 a[40:41], v244 offset:0
v_mfma_f32_16x16x32_bf16 v[32:35], a[116:119], a[68:71], v[32:35]
buffer_load_dwordx4 v252, s[44:47], s82 offen offset:0 sc1 lds
v_mfma_f32_16x16x32_bf16 v[36:39], a[112:115], a[72:75], v[36:39]
ds_read_b64_tr_b16 a[42:43], v244 offset:2048
ds_read_b64_tr_b16 a[44:45], v244 offset:4096
v_mfma_f32_16x16x32_bf16 v[36:39], a[116:119], a[76:79], v[36:39]
buffer_load_dwordx4 v252, s[44:47], s82 offen offset:1024 sc1 lds
v_mfma_f32_16x16x32_bf16 v[40:43], a[112:115], a[80:83], v[40:43]
buffer_load_dwordx4 v252, s[44:47], s82 offen offset:2048 sc1 lds
v_mfma_f32_16x16x32_bf16 v[40:43], a[116:119], a[84:87], v[40:43]
ds_read_b64_tr_b16 a[46:47], v244 offset:6144
ds_read_b64_tr_b16 a[48:49], v243 offset:512
v_mfma_f32_16x16x32_bf16 v[44:47], a[112:115], a[88:91], v[44:47]
buffer_load_dwordx4 v252, s[44:47], s82 offen offset:3072 sc1 lds
s_add_u32 s54, s54, 32768
v_mfma_f32_16x16x32_bf16 v[44:47], a[116:119], a[92:95], v[44:47]
s_cmp_eq_u32 s54, 131072
s_cselect_b32 s54, 0, s54
s_add_u32 s51, s51, 16384
v_mfma_f32_16x16x32_bf16 v[48:51], a[120:123], a[64:67], v[48:51]
s_add_u32 s52, s52, 65536
ds_read_b64_tr_b16 a[50:51], v243 offset:2560
ds_read_b64_tr_b16 a[52:53], v243 offset:4608
v_mfma_f32_16x16x32_bf16 v[48:51], a[124:127], a[68:71], v[48:51]
ds_read_b64_tr_b16 a[54:55], v243 offset:6656
ds_read_b64_tr_b16 a[56:57], v244 offset:512
v_mfma_f32_16x16x32_bf16 v[52:55], a[120:123], a[72:75], v[52:55]
)ASM"
R"ASM(ds_read_b64_tr_b16 a[58:59], v244 offset:2560
ds_read_b64_tr_b16 a[60:61], v244 offset:4608
v_mfma_f32_16x16x32_bf16 v[52:55], a[124:127], a[76:79], v[52:55]
ds_read_b64_tr_b16 a[62:63], v244 offset:6656
v_mfma_f32_16x16x32_bf16 v[56:59], a[120:123], a[80:83], v[56:59]
v_mfma_f32_16x16x32_bf16 v[56:59], a[124:127], a[84:87], v[56:59]
v_mfma_f32_16x16x32_bf16 v[60:63], a[120:123], a[88:91], v[60:63]
v_mfma_f32_16x16x32_bf16 v[60:63], a[124:127], a[92:95], v[60:63]
s_waitcnt vmcnt(16) lgkmcnt(0)
s_barrier
v_mfma_f32_16x16x32_bf16 v[0:3], a[32:35], a[0:3], v[0:3]
s_add_u32 s55, s55, 32768
s_cmp_eq_u32 s55, 131072
s_cselect_b32 s55, 0, s55
v_mfma_f32_16x16x32_bf16 v[0:3], a[36:39], a[4:7], v[0:3]
v_add_u32 v243, s55, v240
v_add_u32 v244, s55, v241
v_mfma_f32_16x16x32_bf16 v[4:7], a[32:35], a[8:11], v[4:7]
v_add_u32 v144, s55, v140
v_add_u32 v145, s55, v141
v_mfma_f32_16x16x32_bf16 v[4:7], a[36:39], a[12:15], v[4:7]
v_add_u32 v146, s55, v142
v_add_u32 v147, s55, v143
v_mfma_f32_16x16x32_bf16 v[8:11], a[32:35], a[16:19], v[8:11]
s_add_u32 s83, s58, s54
ds_read_b64_tr_b16 a[96:97], v243 offset:0
ds_read_b64_tr_b16 a[98:99], v243 offset:2048
v_mfma_f32_16x16x32_bf16 v[8:11], a[36:39], a[20:23], v[8:11]
s_add_u32 m0, s83, 0
s_add_u32 s82, s52, 0
ds_read_b64_tr_b16 a[100:101], v243 offset:4096
v_mfma_f32_16x16x32_bf16 v[12:15], a[32:35], a[24:27], v[12:15]
buffer_load_dwordx4 v245, s[40:43], s82 offen lds
v_mfma_f32_16x16x32_bf16 v[12:15], a[36:39], a[28:31], v[12:15]
ds_read_b64_tr_b16 a[102:103], v243 offset:6144
ds_read_b128 a[64:67], v144 offset:0
v_mfma_f32_16x16x32_bf16 v[16:19], a[40:43], a[0:3], v[16:19]
s_add_u32 m0, s83, 1024
s_add_u32 s82, s52, 8192
ds_read_b128 a[68:71], v144 offset:8192
v_mfma_f32_16x16x32_bf16 v[16:19], a[44:47], a[4:7], v[16:19]
buffer_load_dwordx4 v245, s[40:43], s82 offen lds
v_mfma_f32_16x16x32_bf16 v[20:23], a[40:43], a[8:11], v[20:23]
s_add_u32 m0, s83, 8192
s_add_u32 s82, s52, 128
ds_read_b128 a[72:75], v145 offset:0
v_mfma_f32_16x16x32_bf16 v[20:23], a[44:47], a[12:15], v[20:23]
ds_read_b128 a[76:79], v145 offset:8192
ds_read_b128 a[80:83], v146 offset:0
v_mfma_f32_16x16x32_bf16 v[24:27], a[40:43], a[16:19], v[24:27]
buffer_load_dwordx4 v245, s[40:43], s82 offen lds
v_mfma_f32_16x16x32_bf16 v[24:27], a[44:47], a[20:23], v[24:27]
s_add_u32 m0, s83, 9216
s_add_u32 s82, s52, 8320
ds_read_b128 a[84:87], v146 offset:8192
v_mfma_f32_16x16x32_bf16 v[28:31], a[40:43], a[24:27], v[28:31]
ds_read_b128 a[88:91], v147 offset:0
ds_read_b128 a[92:95], v147 offset:8192
v_mfma_f32_16x16x32_bf16 v[28:31], a[44:47], a[28:31], v[28:31]
buffer_load_dwordx4 v245, s[40:43], s82 offen lds
v_mfma_f32_16x16x32_bf16 v[32:35], a[48:51], a[0:3], v[32:35]
s_add_u32 m0, s57, s54
s_mov_b32 s82, s51
ds_read_b64_tr_b16 a[104:105], v244 offset:0
v_mfma_f32_16x16x32_bf16 v[32:35], a[52:55], a[4:7], v[32:35]
buffer_load_dwordx4 v252, s[44:47], s82 offen offset:0 sc1 lds
v_mfma_f32_16x16x32_bf16 v[36:39], a[48:51], a[8:11], v[36:39]
ds_read_b64_tr_b16 a[106:107], v244 offset:2048
ds_read_b64_tr_b16 a[108:109], v244 offset:4096
v_mfma_f32_16x16x32_bf16 v[36:39], a[52:55], a[12:15], v[36:39]
buffer_load_dwordx4 v252, s[44:47], s82 offen offset:1024 sc1 lds
v_mfma_f32_16x16x32_bf16 v[40:43], a[48:51], a[16:19], v[40:43]
buffer_load_dwordx4 v252, s[44:47], s82 offen offset:2048 sc1 lds
v_mfma_f32_16x16x32_bf16 v[40:43], a[52:55], a[20:23], v[40:43]
ds_read_b64_tr_b16 a[110:111], v244 offset:6144
ds_read_b64_tr_b16 a[112:113], v243 offset:512
v_mfma_f32_16x16x32_bf16 v[44:47], a[48:51], a[24:27], v[44:47]
buffer_load_dwordx4 v252, s[44:47], s82 offen offset:3072 sc1 lds
s_add_u32 s54, s54, 32768
v_mfma_f32_16x16x32_bf16 v[44:47], a[52:55], a[28:31], v[44:47]
s_cmp_eq_u32 s54, 131072
s_cselect_b32 s54, 0, s54
s_add_u32 s51, s51, 16384
v_mfma_f32_16x16x32_bf16 v[48:51], a[56:59], a[0:3], v[48:51]
s_add_u32 s52, s52, 65536
ds_read_b64_tr_b16 a[114:115], v243 offset:2560
ds_read_b64_tr_b16 a[116:117], v243 offset:4608
v_mfma_f32_16x16x32_bf16 v[48:51], a[60:63], a[4:7], v[48:51]
ds_read_b64_tr_b16 a[118:119], v243 offset:6656
ds_read_b64_tr_b16 a[120:121], v244 offset:512
v_mfma_f32_16x16x32_bf16 v[52:55], a[56:59], a[8:11], v[52:55]
ds_read_b64_tr_b16 a[122:123], v244 offset:2560
ds_read_b64_tr_b16 a[124:125], v244 offset:4608
v_mfma_f32_16x16x32_bf16 v[52:55], a[60:63], a[12:15], v[52:55]
ds_read_b64_tr_b16 a[126:127], v244 offset:6656
v_mfma_f32_16x16x32_bf16 v[56:59], a[56:59], a[16:19], v[56:59]
v_mfma_f32_16x16x32_bf16 v[56:59], a[60:63], a[20:23], v[56:59]
v_mfma_f32_16x16x32_bf16 v[60:63], a[56:59], a[24:27], v[60:63]
v_mfma_f32_16x16x32_bf16 v[60:63], a[60:63], a[28:31], v[60:63]
v_and_b32 v254, 15, %[tid]
v_lshlrev_b32 v254, 2, v254
s_add_u32 s82, s48, 0
s_and_b32 s82, s82, 3
s_lshl_b32 s82, s82, 6
s_lshl_b32 s83, s49, 9
s_add_u32 s82, s82, s83
s_lshl_b32 s83, s69, 8
s_add_u32 s82, s82, s83
s_add_u32 s82, s82, 0x4700000
buffer_load_dword v128, v254, s[36:39], s82 offen sc1
s_add_u32 s82, s48, 1
s_and_b32 s82, s82, 3
s_lshl_b32 s82, s82, 6
s_lshl_b32 s83, s49, 9
s_add_u32 s82, s82, s83
s_lshl_b32 s83, s69, 8
s_add_u32 s82, s82, s83
s_add_u32 s82, s82, 0x4700000
buffer_load_dword v129, v254, s[36:39], s82 offen sc1
s_add_u32 s82, s48, 2
s_and_b32 s82, s82, 3
s_lshl_b32 s82, s82, 6
s_lshl_b32 s83, s49, 9
s_add_u32 s82, s82, s83
s_lshl_b32 s83, s69, 8
s_add_u32 s82, s82, s83
s_add_u32 s82, s82, 0x4700000
buffer_load_dword v130, v254, s[36:39], s82 offen sc1
s_add_u32 s82, s48, 3
s_and_b32 s82, s82, 3
s_lshl_b32 s82, s82, 6
s_lshl_b32 s83, s49, 9
s_add_u32 s82, s82, s83
s_lshl_b32 s83, s69, 8
s_add_u32 s82, s82, s83
s_add_u32 s82, s82, 0x4700000
buffer_load_dword v131, v254, s[36:39], s82 offen sc1
s_mov_b32 s56, 8
mla_fused_c_loopc:
s_waitcnt vmcnt(16) lgkmcnt(0)
s_barrier
v_mfma_f32_16x16x32_bf16 v[64:67], a[96:99], a[64:67], v[64:67]
s_add_u32 s55, s55, 32768
s_cmp_eq_u32 s55, 131072
s_cselect_b32 s55, 0, s55
v_mfma_f32_16x16x32_bf16 v[64:67], a[100:103], a[68:71], v[64:67]
v_add_u32 v243, s55, v240
v_add_u32 v244, s55, v241
v_mfma_f32_16x16x32_bf16 v[68:71], a[96:99], a[72:75], v[68:71]
v_add_u32 v144, s55, v140
v_add_u32 v145, s55, v141
v_mfma_f32_16x16x32_bf16 v[68:71], a[100:103], a[76:79], v[68:71]
v_add_u32 v146, s55, v142
v_add_u32 v147, s55, v143
v_mfma_f32_16x16x32_bf16 v[72:75], a[96:99], a[80:83], v[72:75]
s_add_u32 s83, s58, s54
ds_read_b64_tr_b16 a[32:33], v243 offset:0
ds_read_b64_tr_b16 a[34:35], v243 offset:2048
v_mfma_f32_16x16x32_bf16 v[72:75], a[100:103], a[84:87], v[72:75]
s_add_u32 m0, s83, 0
s_add_u32 s82, s52, 0
ds_read_b64_tr_b16 a[36:37], v243 offset:4096
v_mfma_f32_16x16x32_bf16 v[76:79], a[96:99], a[88:91], v[76:79]
buffer_load_dwordx4 v245, s[40:43], s82 offen lds
v_mfma_f32_16x16x32_bf16 v[76:79], a[100:103], a[92:95], v[76:79]
ds_read_b64_tr_b16 a[38:39], v243 offset:6144
ds_read_b128 a[0:3], v144 offset:0
v_mfma_f32_16x16x32_bf16 v[80:83], a[104:107], a[64:67], v[80:83]
s_add_u32 m0, s83, 1024
s_add_u32 s82, s52, 8192
ds_read_b128 a[4:7], v144 offset:8192
v_mfma_f32_16x16x32_bf16 v[80:83], a[108:111], a[68:71], v[80:83]
buffer_load_dwordx4 v245, s[40:43], s82 offen lds
v_mfma_f32_16x16x32_bf16 v[84:87], a[104:107], a[72:75], v[84:87]
s_add_u32 m0, s83, 8192
s_add_u32 s82, s52, 128
ds_read_b128 a[8:11], v145 offset:0
v_mfma_f32_16x16x32_bf16 v[84:87], a[108:111], a[76:79], v[84:87]
ds_read_b128 a[12:15], v145 offset:8192
ds_read_b128 a[16:19], v146 offset:0
v_mfma_f32_16x16x32_bf16 v[88:91], a[104:107], a[80:83], v[88:91]
buffer_load_dwordx4 v245, s[40:43], s82 offen lds
v_mfma_f32_16x16x32_bf16 v[88:91], a[108:111], a[84:87], v[88:91]
s_add_u32 m0, s83, 9216
s_add_u32 s82, s52, 8320
ds_read_b128 a[20:23], v146 offset:8192
v_mfma_f32_16x16x32_bf16 v[92:95], a[104:107], a[88:91], v[92:95]
ds_read_b128 a[24:27], v147 offset:0
ds_read_b128 a[28:31], v147 offset:8192
v_mfma_f32_16x16x32_bf16 v[92:95], a[108:111], a[92:95], v[92:95]
buffer_load_dwordx4 v245, s[40:43], s82 offen lds
v_mfma_f32_16x16x32_bf16 v[96:99], a[112:115], a[64:67], v[96:99]
s_add_u32 m0, s57, s54
s_mov_b32 s82, s51
ds_read_b64_tr_b16 a[40:41], v244 offset:0
v_mfma_f32_16x16x32_bf16 v[96:99], a[116:119], a[68:71], v[96:99]
buffer_load_dwordx4 v252, s[44:47], s82 offen offset:0 sc1 lds
v_mfma_f32_16x16x32_bf16 v[100:103], a[112:115], a[72:75], v[100:103]
ds_read_b64_tr_b16 a[42:43], v244 offset:2048
ds_read_b64_tr_b16 a[44:45], v244 offset:4096
v_mfma_f32_16x16x32_bf16 v[100:103], a[116:119], a[76:79], v[100:103]
buffer_load_dwordx4 v252, s[44:47], s82 offen offset:1024 sc1 lds
v_mfma_f32_16x16x32_bf16 v[104:107], a[112:115], a[80:83], v[104:107]
buffer_load_dwordx4 v252, s[44:47], s82 offen offset:2048 sc1 lds
v_mfma_f32_16x16x32_bf16 v[104:107], a[116:119], a[84:87], v[104:107]
ds_read_b64_tr_b16 a[46:47], v244 offset:6144
ds_read_b64_tr_b16 a[48:49], v243 offset:512
v_mfma_f32_16x16x32_bf16 v[108:111], a[112:115], a[88:91], v[108:111]
buffer_load_dwordx4 v252, s[44:47], s82 offen offset:3072 sc1 lds
s_add_u32 s54, s54, 32768
v_mfma_f32_16x16x32_bf16 v[108:111], a[116:119], a[92:95], v[108:111]
s_cmp_eq_u32 s54, 131072
s_cselect_b32 s54, 0, s54
s_add_u32 s51, s51, 16384
v_mfma_f32_16x16x32_bf16 v[112:115], a[120:123], a[64:67], v[112:115]
s_add_u32 s52, s52, 65536
ds_read_b64_tr_b16 a[50:51], v243 offset:2560
ds_read_b64_tr_b16 a[52:53], v243 offset:4608
v_mfma_f32_16x16x32_bf16 v[112:115], a[124:127], a[68:71], v[112:115]
s_cmp_le_u32 s56, 2
s_cselect_b32 s42, 0, s42
s_cselect_b32 s46, 0, s46
v_mfma_f32_16x16x32_bf16 v[116:119], a[120:123], a[72:75], v[116:119]
ds_read_b64_tr_b16 a[54:55], v243 offset:6656
ds_read_b64_tr_b16 a[56:57], v244 offset:512
v_mfma_f32_16x16x32_bf16 v[116:119], a[124:127], a[76:79], v[116:119]
ds_read_b64_tr_b16 a[58:59], v244 offset:2560
ds_read_b64_tr_b16 a[60:61], v244 offset:4608
v_mfma_f32_16x16x32_bf16 v[120:123], a[120:123], a[80:83], v[120:123]
ds_read_b64_tr_b16 a[62:63], v244 offset:6656
v_mfma_f32_16x16x32_bf16 v[120:123], a[124:127], a[84:87], v[120:123]
v_mfma_f32_16x16x32_bf16 v[124:127], a[120:123], a[88:91], v[124:127]
v_mfma_f32_16x16x32_bf16 v[124:127], a[124:127], a[92:95], v[124:127]
s_waitcnt vmcnt(16) lgkmcnt(0)
s_barrier
v_mfma_f32_16x16x32_bf16 v[64:67], a[32:35], a[0:3], v[64:67]
s_add_u32 s55, s55, 32768
s_cmp_eq_u32 s55, 131072
s_cselect_b32 s55, 0, s55
v_mfma_f32_16x16x32_bf16 v[64:67], a[36:39], a[4:7], v[64:67]
v_add_u32 v243, s55, v240
v_add_u32 v244, s55, v241
v_mfma_f32_16x16x32_bf16 v[68:71], a[32:35], a[8:11], v[68:71]
v_add_u32 v144, s55, v140
v_add_u32 v145, s55, v141
v_mfma_f32_16x16x32_bf16 v[68:71], a[36:39], a[12:15], v[68:71]
v_add_u32 v146, s55, v142
v_add_u32 v147, s55, v143
v_mfma_f32_16x16x32_bf16 v[72:75], a[32:35], a[16:19], v[72:75]
s_add_u32 s83, s58, s54
ds_read_b64_tr_b16 a[96:97], v243 offset:0
ds_read_b64_tr_b16 a[98:99], v243 offset:2048
v_mfma_f32_16x16x32_bf16 v[72:75], a[36:39], a[20:23], v[72:75]
s_add_u32 m0, s83, 0
s_add_u32 s82, s52, 0
ds_read_b64_tr_b16 a[100:101], v243 offset:4096
v_mfma_f32_16x16x32_bf16 v[76:79], a[32:35], a[24:27], v[76:79]
buffer_load_dwordx4 v245, s[40:43], s82 offen lds
v_mfma_f32_16x16x32_bf16 v[76:79], a[36:39], a[28:31], v[76:79]
ds_read_b64_tr_b16 a[102:103], v243 offset:6144
ds_read_b128 a[64:67], v144 offset:0
v_mfma_f32_16x16x32_bf16 v[80:83], a[40:43], a[0:3], v[80:83]
s_add_u32 m0, s83, 1024
s_add_u32 s82, s52, 8192
ds_read_b128 a[68:71], v144 offset:8192
v_mfma_f32_16x16x32_bf16 v[80:83], a[44:47], a[4:7], v[80:83]
buffer_load_dwordx4 v245, s[40:43], s82 offen lds
v_mfma_f32_16x16x32_bf16 v[84:87], a[40:43], a[8:11], v[84:87]
s_add_u32 m0, s83, 8192
s_add_u32 s82, s52, 128
ds_read_b128 a[72:75], v145 offset:0
v_mfma_f32_16x16x32_bf16 v[84:87], a[44:47], a[12:15], v[84:87]
ds_read_b128 a[76:79], v145 offset:8192
ds_read_b128 a[80:83], v146 offset:0
v_mfma_f32_16x16x32_bf16 v[88:91], a[40:43], a[16:19], v[88:91]
buffer_load_dwordx4 v245, s[40:43], s82 offen lds
v_mfma_f32_16x16x32_bf16 v[88:91], a[44:47], a[20:23], v[88:91]
s_add_u32 m0, s83, 9216
s_add_u32 s82, s52, 8320
)ASM"
R"ASM(ds_read_b128 a[84:87], v146 offset:8192
v_mfma_f32_16x16x32_bf16 v[92:95], a[40:43], a[24:27], v[92:95]
ds_read_b128 a[88:91], v147 offset:0
ds_read_b128 a[92:95], v147 offset:8192
v_mfma_f32_16x16x32_bf16 v[92:95], a[44:47], a[28:31], v[92:95]
buffer_load_dwordx4 v245, s[40:43], s82 offen lds
v_mfma_f32_16x16x32_bf16 v[96:99], a[48:51], a[0:3], v[96:99]
s_add_u32 m0, s57, s54
s_mov_b32 s82, s51
ds_read_b64_tr_b16 a[104:105], v244 offset:0
v_mfma_f32_16x16x32_bf16 v[96:99], a[52:55], a[4:7], v[96:99]
buffer_load_dwordx4 v252, s[44:47], s82 offen offset:0 sc1 lds
v_mfma_f32_16x16x32_bf16 v[100:103], a[48:51], a[8:11], v[100:103]
ds_read_b64_tr_b16 a[106:107], v244 offset:2048
ds_read_b64_tr_b16 a[108:109], v244 offset:4096
v_mfma_f32_16x16x32_bf16 v[100:103], a[52:55], a[12:15], v[100:103]
buffer_load_dwordx4 v252, s[44:47], s82 offen offset:1024 sc1 lds
v_mfma_f32_16x16x32_bf16 v[104:107], a[48:51], a[16:19], v[104:107]
buffer_load_dwordx4 v252, s[44:47], s82 offen offset:2048 sc1 lds
v_mfma_f32_16x16x32_bf16 v[104:107], a[52:55], a[20:23], v[104:107]
ds_read_b64_tr_b16 a[110:111], v244 offset:6144
ds_read_b64_tr_b16 a[112:113], v243 offset:512
v_mfma_f32_16x16x32_bf16 v[108:111], a[48:51], a[24:27], v[108:111]
buffer_load_dwordx4 v252, s[44:47], s82 offen offset:3072 sc1 lds
s_add_u32 s54, s54, 32768
v_mfma_f32_16x16x32_bf16 v[108:111], a[52:55], a[28:31], v[108:111]
s_cmp_eq_u32 s54, 131072
s_cselect_b32 s54, 0, s54
s_add_u32 s51, s51, 16384
v_mfma_f32_16x16x32_bf16 v[112:115], a[56:59], a[0:3], v[112:115]
s_add_u32 s52, s52, 65536
ds_read_b64_tr_b16 a[114:115], v243 offset:2560
ds_read_b64_tr_b16 a[116:117], v243 offset:4608
v_mfma_f32_16x16x32_bf16 v[112:115], a[60:63], a[4:7], v[112:115]
s_cmp_le_u32 s56, 3
s_cselect_b32 s42, 0, s42
s_cselect_b32 s46, 0, s46
v_mfma_f32_16x16x32_bf16 v[116:119], a[56:59], a[8:11], v[116:119]
ds_read_b64_tr_b16 a[118:119], v243 offset:6656
ds_read_b64_tr_b16 a[120:121], v244 offset:512
v_mfma_f32_16x16x32_bf16 v[116:119], a[60:63], a[12:15], v[116:119]
ds_read_b64_tr_b16 a[122:123], v244 offset:2560
ds_read_b64_tr_b16 a[124:125], v244 offset:4608
v_mfma_f32_16x16x32_bf16 v[120:123], a[56:59], a[16:19], v[120:123]
ds_read_b64_tr_b16 a[126:127], v244 offset:6656
v_mfma_f32_16x16x32_bf16 v[120:123], a[60:63], a[20:23], v[120:123]
v_mfma_f32_16x16x32_bf16 v[124:127], a[56:59], a[24:27], v[124:127]
v_mfma_f32_16x16x32_bf16 v[124:127], a[60:63], a[28:31], v[124:127]
s_sub_u32 s56, s56, 1
s_cmp_lg_u32 s56, 0
s_cbranch_scc1 mla_fused_c_loopc
v_mov_b32 v132, v128
v_mov_b32 v134, v129
v_mov_b32 v136, v130
v_mov_b32 v138, v131
v_mov_b32 v133, v128
v_mov_b32 v135, v129
v_mov_b32 v137, v130
v_mov_b32 v139, v131
s_lshl_b32 s76, s49, 2
s_add_u32 s76, s76, s50
s_add_u32 s82, s48, 1
s_and_b32 s82, s82, 3
s_lshl_b32 s83, s76, 2
s_add_u32 s82, s82, s83
s_lshl_b32 s82, s82, 2
s_add_u32 s82, s82, s68
s_lshl_b32 s82, s82, 14
s_add_u32 s82, s82, 0x4704000
v_pk_add_f32 v[68:69], v[68:69], v[4:5] neg_lo:[0,1] neg_hi:[0,1]
v_pk_add_f32 v[70:71], v[70:71], v[6:7] neg_lo:[0,1] neg_hi:[0,1]
v_pk_fma_f32 v[68:69], v[68:69], v[134:135], v[4:5]
v_pk_fma_f32 v[70:71], v[70:71], v[134:135], v[6:7]
v_pk_add_f32 v[84:85], v[84:85], v[20:21] neg_lo:[0,1] neg_hi:[0,1]
v_pk_add_f32 v[86:87], v[86:87], v[22:23] neg_lo:[0,1] neg_hi:[0,1]
v_pk_fma_f32 v[84:85], v[84:85], v[134:135], v[20:21]
v_pk_fma_f32 v[86:87], v[86:87], v[134:135], v[22:23]
v_cvt_pk_bf16_f32 v68, v68, v69
v_cvt_pk_bf16_f32 v69, v70, v71
v_cvt_pk_bf16_f32 v70, v84, v85
v_cvt_pk_bf16_f32 v71, v86, v87
buffer_store_dwordx4 v[68:71], v252, s[36:39], s82 offen offset:0 sc1
v_pk_add_f32 v[100:101], v[100:101], v[36:37] neg_lo:[0,1] neg_hi:[0,1]
v_pk_add_f32 v[102:103], v[102:103], v[38:39] neg_lo:[0,1] neg_hi:[0,1]
v_pk_fma_f32 v[100:101], v[100:101], v[134:135], v[36:37]
v_pk_fma_f32 v[102:103], v[102:103], v[134:135], v[38:39]
v_pk_add_f32 v[116:117], v[116:117], v[52:53] neg_lo:[0,1] neg_hi:[0,1]
v_pk_add_f32 v[118:119], v[118:119], v[54:55] neg_lo:[0,1] neg_hi:[0,1]
v_pk_fma_f32 v[116:117], v[116:117], v[134:135], v[52:53]
v_pk_fma_f32 v[118:119], v[118:119], v[134:135], v[54:55]
v_cvt_pk_bf16_f32 v100, v100, v101
v_cvt_pk_bf16_f32 v101, v102, v103
v_cvt_pk_bf16_f32 v102, v116, v117
v_cvt_pk_bf16_f32 v103, v118, v119
buffer_store_dwordx4 v[100:103], v252, s[36:39], s82 offen offset:1024 sc1
s_add_u32 s82, s48, 2
s_and_b32 s82, s82, 3
s_lshl_b32 s83, s76, 2
s_add_u32 s82, s82, s83
s_lshl_b32 s82, s82, 2
s_add_u32 s82, s82, s68
s_lshl_b32 s82, s82, 14
s_add_u32 s82, s82, 0x4703000
v_pk_add_f32 v[72:73], v[72:73], v[8:9] neg_lo:[0,1] neg_hi:[0,1]
v_pk_add_f32 v[74:75], v[74:75], v[10:11] neg_lo:[0,1] neg_hi:[0,1]
v_pk_fma_f32 v[72:73], v[72:73], v[136:137], v[8:9]
v_pk_fma_f32 v[74:75], v[74:75], v[136:137], v[10:11]
v_pk_add_f32 v[88:89], v[88:89], v[24:25] neg_lo:[0,1] neg_hi:[0,1]
v_pk_add_f32 v[90:91], v[90:91], v[26:27] neg_lo:[0,1] neg_hi:[0,1]
v_pk_fma_f32 v[88:89], v[88:89], v[136:137], v[24:25]
v_pk_fma_f32 v[90:91], v[90:91], v[136:137], v[26:27]
v_cvt_pk_bf16_f32 v72, v72, v73
v_cvt_pk_bf16_f32 v73, v74, v75
v_cvt_pk_bf16_f32 v74, v88, v89
v_cvt_pk_bf16_f32 v75, v90, v91
buffer_store_dwordx4 v[72:75], v252, s[36:39], s82 offen offset:0 sc1
v_pk_add_f32 v[104:105], v[104:105], v[40:41] neg_lo:[0,1] neg_hi:[0,1]
v_pk_add_f32 v[106:107], v[106:107], v[42:43] neg_lo:[0,1] neg_hi:[0,1]
v_pk_fma_f32 v[104:105], v[104:105], v[136:137], v[40:41]
v_pk_fma_f32 v[106:107], v[106:107], v[136:137], v[42:43]
v_pk_add_f32 v[120:121], v[120:121], v[56:57] neg_lo:[0,1] neg_hi:[0,1]
v_pk_add_f32 v[122:123], v[122:123], v[58:59] neg_lo:[0,1] neg_hi:[0,1]
v_pk_fma_f32 v[120:121], v[120:121], v[136:137], v[56:57]
v_pk_fma_f32 v[122:123], v[122:123], v[136:137], v[58:59]
v_cvt_pk_bf16_f32 v104, v104, v105
v_cvt_pk_bf16_f32 v105, v106, v107
v_cvt_pk_bf16_f32 v106, v120, v121
v_cvt_pk_bf16_f32 v107, v122, v123
buffer_store_dwordx4 v[104:107], v252, s[36:39], s82 offen offset:1024 sc1
s_add_u32 s82, s48, 3
s_and_b32 s82, s82, 3
s_lshl_b32 s83, s76, 2
s_add_u32 s82, s82, s83
s_lshl_b32 s82, s82, 2
s_add_u32 s82, s82, s68
s_lshl_b32 s82, s82, 14
s_add_u32 s82, s82, 0x4702000
v_pk_add_f32 v[76:77], v[76:77], v[12:13] neg_lo:[0,1] neg_hi:[0,1]
v_pk_add_f32 v[78:79], v[78:79], v[14:15] neg_lo:[0,1] neg_hi:[0,1]
v_pk_fma_f32 v[76:77], v[76:77], v[138:139], v[12:13]
v_pk_fma_f32 v[78:79], v[78:79], v[138:139], v[14:15]
v_pk_add_f32 v[92:93], v[92:93], v[28:29] neg_lo:[0,1] neg_hi:[0,1]
v_pk_add_f32 v[94:95], v[94:95], v[30:31] neg_lo:[0,1] neg_hi:[0,1]
v_pk_fma_f32 v[92:93], v[92:93], v[138:139], v[28:29]
v_pk_fma_f32 v[94:95], v[94:95], v[138:139], v[30:31]
v_cvt_pk_bf16_f32 v76, v76, v77
v_cvt_pk_bf16_f32 v77, v78, v79
v_cvt_pk_bf16_f32 v78, v92, v93
v_cvt_pk_bf16_f32 v79, v94, v95
buffer_store_dwordx4 v[76:79], v252, s[36:39], s82 offen offset:0 sc1
v_pk_add_f32 v[108:109], v[108:109], v[44:45] neg_lo:[0,1] neg_hi:[0,1]
v_pk_add_f32 v[110:111], v[110:111], v[46:47] neg_lo:[0,1] neg_hi:[0,1]
v_pk_fma_f32 v[108:109], v[108:109], v[138:139], v[44:45]
v_pk_fma_f32 v[110:111], v[110:111], v[138:139], v[46:47]
v_pk_add_f32 v[124:125], v[124:125], v[60:61] neg_lo:[0,1] neg_hi:[0,1]
v_pk_add_f32 v[126:127], v[126:127], v[62:63] neg_lo:[0,1] neg_hi:[0,1]
v_pk_fma_f32 v[124:125], v[124:125], v[138:139], v[60:61]
v_pk_fma_f32 v[126:127], v[126:127], v[138:139], v[62:63]
v_cvt_pk_bf16_f32 v108, v108, v109
v_cvt_pk_bf16_f32 v109, v110, v111
v_cvt_pk_bf16_f32 v110, v124, v125
v_cvt_pk_bf16_f32 v111, v126, v127
buffer_store_dwordx4 v[108:111], v252, s[36:39], s82 offen offset:1024 sc1
v_pk_add_f32 v[64:65], v[64:65], v[0:1] neg_lo:[0,1] neg_hi:[0,1]
v_pk_add_f32 v[66:67], v[66:67], v[2:3] neg_lo:[0,1] neg_hi:[0,1]
v_pk_fma_f32 v[64:65], v[64:65], v[132:133], v[0:1]
v_pk_fma_f32 v[66:67], v[66:67], v[132:133], v[2:3]
v_pk_add_f32 v[80:81], v[80:81], v[16:17] neg_lo:[0,1] neg_hi:[0,1]
v_pk_add_f32 v[82:83], v[82:83], v[18:19] neg_lo:[0,1] neg_hi:[0,1]
v_pk_fma_f32 v[80:81], v[80:81], v[132:133], v[16:17]
v_pk_fma_f32 v[82:83], v[82:83], v[132:133], v[18:19]
v_pk_add_f32 v[96:97], v[96:97], v[32:33] neg_lo:[0,1] neg_hi:[0,1]
v_pk_add_f32 v[98:99], v[98:99], v[34:35] neg_lo:[0,1] neg_hi:[0,1]
v_pk_fma_f32 v[96:97], v[96:97], v[132:133], v[32:33]
v_pk_fma_f32 v[98:99], v[98:99], v[132:133], v[34:35]
v_pk_add_f32 v[112:113], v[112:113], v[48:49] neg_lo:[0,1] neg_hi:[0,1]
v_pk_add_f32 v[114:115], v[114:115], v[50:51] neg_lo:[0,1] neg_hi:[0,1]
v_pk_fma_f32 v[112:113], v[112:113], v[132:133], v[48:49]
v_pk_fma_f32 v[114:115], v[114:115], v[132:133], v[50:51]
s_lshl_b32 s71, s76, 5
s_add_u32 s71, s71, 0x5705000
s_cmp_lg_u32 s68, 0
s_cbranch_scc1 mla_fused_c_pi15
v_and_b32 v152, 63, %[tid]
v_cmp_gt_u32 vcc, 4, v152
v_lshlrev_b32 v152, 3, v152
s_nop 2
s_mov_b64 s[92:93], vcc
v_mov_b32 v150, 0x7ffffff0
v_cndmask_b32 v152, v150, v152, vcc
buffer_load_dwordx2 v[150:151], v152, s[36:39], s71 offen sc1
mla_fused_c_pi15:
s_bitset0_b32 s92, s48
s_waitcnt vmcnt(0)
s_barrier
s_lshl_b32 s82, s48, 3
s_add_u32 s82, s82, s71
s_cmp_lg_u32 s68, 0
s_cbranch_scc1 mla_fused_c_pub16
v_and_b32 v158, 63, %[tid]
v_cmp_eq_u32 vcc, 0, v158
v_mov_b32 v156, 0x7ffffff0
v_mov_b32 v157, 0
v_cndmask_b32 v158, v156, v157, vcc
v_mov_b32 v156, s98
v_mov_b32 v157, s99
buffer_store_dwordx2 v[156:157], v158, s[36:39], s82 offen sc1
mla_fused_c_pub16:
s_cmp_lg_u32 s68, 0
s_cbranch_scc1 mla_fused_c_pc17x
s_mov_b32 s90, 262144
s_waitcnt vmcnt(1)
s_branch mla_fused_c_pc17c
mla_fused_c_pc17:
buffer_load_dwordx2 v[150:151], v152, s[36:39], s71 offen sc1
s_waitcnt vmcnt(0)
mla_fused_c_pc17c:
v_cmp_eq_u32 vcc, s98, v150
v_cmp_eq_u32 s[94:95], s99, v151
s_nop 3
s_and_b64 vcc, vcc, s[94:95]
s_and_b64 vcc, vcc, s[92:93]
s_cmp_eq_u64 vcc, s[92:93]
s_cbranch_scc1 mla_fused_c_pc17d
s_sub_u32 s90, s90, 1
s_cmp_eq_u32 s90, 0
s_cbranch_scc1 mla_fused_c_pc17d
s_sleep 2
s_branch mla_fused_c_pc17
mla_fused_c_pc17d:
mla_fused_c_pc17x:
s_barrier
s_lshl_b32 s82, s76, 2
s_add_u32 s82, s82, s48
s_lshl_b32 s82, s82, 2
s_add_u32 s82, s82, s68
s_lshl_b32 s82, s82, 14
s_add_u32 s82, s82, 0x4702000
s_add_u32 s83, s82, 0
buffer_load_dwordx4 v[0:3], v252, s[36:39], s83 offen offset:0 sc1
buffer_load_dwordx4 v[4:7], v252, s[36:39], s83 offen offset:1024 sc1
s_add_u32 s83, s82, 4096
buffer_load_dwordx4 v[16:19], v252, s[36:39], s83 offen offset:0 sc1
buffer_load_dwordx4 v[20:23], v252, s[36:39], s83 offen offset:1024 sc1
s_add_u32 s83, s82, 8192
buffer_load_dwordx4 v[32:35], v252, s[36:39], s83 offen offset:0 sc1
buffer_load_dwordx4 v[36:39], v252, s[36:39], s83 offen offset:1024 sc1
s_mov_b64 s[72:73], %[out]
s_and_b32 s73, s73, 0xffff
s_mov_b32 s74, 0x200000
s_mov_b32 s75, 0x20000
v_and_b32 v253, 63, %[tid]
v_and_b32 v254, 15, v253
v_lshlrev_b32 v254, 10, v254
v_lshrrev_b32 v253, 4, v253
v_lshl_add_u32 v254, v253, 3, v254
s_lshl_b32 s82, s49, 7
s_lshl_b32 s83, s69, 6
s_add_u32 s82, s82, s83
s_lshl_b32 s83, s48, 4
s_add_u32 s82, s82, s83
s_lshl_b32 s82, s82, 10
s_lshl_b32 s83, s50, 8
s_add_u32 s82, s82, s83
s_lshl_b32 s83, s70, 7
s_add_u32 s82, s82, s83
s_waitcnt vmcnt(5)
v_lshlrev_b32 v48, 16, v0
v_and_b32 v49, 0xffff0000, v0
v_lshlrev_b32 v50, 16, v1
v_and_b32 v51, 0xffff0000, v1
v_pk_add_f32 v[64:65], v[64:65], v[48:49]
v_pk_add_f32 v[66:67], v[66:67], v[50:51]
s_waitcnt vmcnt(3)
v_lshlrev_b32 v52, 16, v16
v_and_b32 v53, 0xffff0000, v16
v_lshlrev_b32 v54, 16, v17
v_and_b32 v55, 0xffff0000, v17
v_pk_add_f32 v[64:65], v[64:65], v[52:53]
v_pk_add_f32 v[66:67], v[66:67], v[54:55]
s_waitcnt vmcnt(1)
v_lshlrev_b32 v48, 16, v32
v_and_b32 v49, 0xffff0000, v32
v_lshlrev_b32 v50, 16, v33
v_and_b32 v51, 0xffff0000, v33
v_pk_add_f32 v[64:65], v[64:65], v[48:49]
v_pk_add_f32 v[66:67], v[66:67], v[50:51]
v_cvt_pk_bf16_f32 v64, v64, v65
v_cvt_pk_bf16_f32 v65, v66, v67
buffer_store_dwordx2 v[64:65], v254, s[72:75], s82 offen offset:0
v_lshlrev_b32 v48, 16, v2
v_and_b32 v49, 0xffff0000, v2
v_lshlrev_b32 v50, 16, v3
v_and_b32 v51, 0xffff0000, v3
v_pk_add_f32 v[80:81], v[80:81], v[48:49]
v_pk_add_f32 v[82:83], v[82:83], v[50:51]
v_lshlrev_b32 v52, 16, v18
)ASM"
R"ASM(v_and_b32 v53, 0xffff0000, v18
v_lshlrev_b32 v54, 16, v19
v_and_b32 v55, 0xffff0000, v19
v_pk_add_f32 v[80:81], v[80:81], v[52:53]
v_pk_add_f32 v[82:83], v[82:83], v[54:55]
v_lshlrev_b32 v48, 16, v34
v_and_b32 v49, 0xffff0000, v34
v_lshlrev_b32 v50, 16, v35
v_and_b32 v51, 0xffff0000, v35
v_pk_add_f32 v[80:81], v[80:81], v[48:49]
v_pk_add_f32 v[82:83], v[82:83], v[50:51]
v_cvt_pk_bf16_f32 v80, v80, v81
v_cvt_pk_bf16_f32 v81, v82, v83
buffer_store_dwordx2 v[80:81], v254, s[72:75], s82 offen offset:32
v_lshlrev_b32 v48, 16, v4
v_and_b32 v49, 0xffff0000, v4
v_lshlrev_b32 v50, 16, v5
v_and_b32 v51, 0xffff0000, v5
v_pk_add_f32 v[96:97], v[96:97], v[48:49]
v_pk_add_f32 v[98:99], v[98:99], v[50:51]
v_lshlrev_b32 v52, 16, v20
v_and_b32 v53, 0xffff0000, v20
v_lshlrev_b32 v54, 16, v21
v_and_b32 v55, 0xffff0000, v21
v_pk_add_f32 v[96:97], v[96:97], v[52:53]
v_pk_add_f32 v[98:99], v[98:99], v[54:55]
s_waitcnt vmcnt(2)
v_lshlrev_b32 v48, 16, v36
v_and_b32 v49, 0xffff0000, v36
v_lshlrev_b32 v50, 16, v37
v_and_b32 v51, 0xffff0000, v37
v_pk_add_f32 v[96:97], v[96:97], v[48:49]
v_pk_add_f32 v[98:99], v[98:99], v[50:51]
v_cvt_pk_bf16_f32 v96, v96, v97
v_cvt_pk_bf16_f32 v97, v98, v99
buffer_store_dwordx2 v[96:97], v254, s[72:75], s82 offen offset:64
v_lshlrev_b32 v48, 16, v6
v_and_b32 v49, 0xffff0000, v6
v_lshlrev_b32 v50, 16, v7
v_and_b32 v51, 0xffff0000, v7
v_pk_add_f32 v[112:113], v[112:113], v[48:49]
v_pk_add_f32 v[114:115], v[114:115], v[50:51]
v_lshlrev_b32 v52, 16, v22
v_and_b32 v53, 0xffff0000, v22
v_lshlrev_b32 v54, 16, v23
v_and_b32 v55, 0xffff0000, v23
v_pk_add_f32 v[112:113], v[112:113], v[52:53]
v_pk_add_f32 v[114:115], v[114:115], v[54:55]
v_lshlrev_b32 v48, 16, v38
v_and_b32 v49, 0xffff0000, v38
v_lshlrev_b32 v50, 16, v39
v_and_b32 v51, 0xffff0000, v39
v_pk_add_f32 v[112:113], v[112:113], v[48:49]
v_pk_add_f32 v[114:115], v[114:115], v[50:51]
v_cvt_pk_bf16_f32 v112, v112, v113
v_cvt_pk_bf16_f32 v113, v114, v115
buffer_store_dwordx2 v[112:113], v254, s[72:75], s82 offen offset:96
mla_fused_b_pfend:
)ASM"
  :
  : [wo]"s"(wo), [w2]"s"(w2), [ws]"s"(ws), [out]"s"(out), [tid]"v"(tid), [bid]"s"(bid), [tl]"s"(tl), [th]"s"(th)
  : "v0", "v1", "v2", "v3", "v4", "v5", "v6", "v7", "v8", "v9", "v10", "v11", "v12", "v13", "v14", "v15", "v16", "v17", "v18", "v19", "v20", "v21", "v22", "v23", "v24", "v25", "v26", "v27", "v28", "v29", "v30", "v31", "v32", "v33", "v34", "v35", "v36", "v37", "v38", "v39", "v40", "v41", "v42", "v43", "v44", "v45", "v46", "v47", "v48", "v49", "v50", "v51", "v52", "v53", "v54", "v55", "v56", "v57", "v58", "v59", "v60", "v61", "v62", "v63", "v64", "v65", "v66", "v67", "v68", "v69", "v70", "v71", "v72", "v73", "v74", "v75", "v76", "v77", "v78", "v79", "v80", "v81", "v82", "v83", "v84", "v85", "v86", "v87", "v88", "v89", "v90", "v91", "v92", "v93", "v94", "v95", "v96", "v97", "v98", "v99", "v100", "v101", "v102", "v103", "v104", "v105", "v106", "v107", "v108", "v109", "v110", "v111", "v112", "v113", "v114", "v115", "v116", "v117", "v118", "v119", "v120", "v121", "v122", "v123", "v124", "v125", "v126", "v127", "v128", "v129", "v130", "v131", "v132", "v133", "v134", "v135", "v136", "v137", "v138", "v139", "v140", "v141", "v142", "v143", "v144", "v145", "v146", "v147", "v148", "v149", "v150", "v151", "v152", "v153", "v154", "v155", "v156", "v157", "v158", "v159", "v234", "v235", "v236", "v237", "v238", "v239", "v240", "v241", "v242", "v243", "v244", "v245", "v246", "v247", "v248", "v249", "v250", "v251", "v252", "v253", "v254", "a0", "a1", "a2", "a3", "a4", "a5", "a6", "a7", "a8", "a9", "a10", "a11", "a12", "a13", "a14", "a15", "a16", "a17", "a18", "a19", "a20", "a21", "a22", "a23", "a24", "a25", "a26", "a27", "a28", "a29", "a30", "a31", "a32", "a33", "a34", "a35", "a36", "a37", "a38", "a39", "a40", "a41", "a42", "a43", "a44", "a45", "a46", "a47", "a48", "a49", "a50", "a51", "a52", "a53", "a54", "a55", "a56", "a57", "a58", "a59", "a60", "a61", "a62", "a63", "a64", "a65", "a66", "a67", "a68", "a69", "a70", "a71", "a72", "a73", "a74", "a75", "a76", "a77", "a78", "a79", "a80", "a81", "a82", "a83", "a84", "a85", "a86", "a87", "a88", "a89", "a90", "a91", "a92", "a93", "a94", "a95", "a96", "a97", "a98", "a99", "a100", "a101", "a102", "a103", "a104", "a105", "a106", "a107", "a108", "a109", "a110", "a111", "a112", "a113", "a114", "a115", "a116", "a117", "a118", "a119", "a120", "a121", "a122", "a123", "a124", "a125", "a126", "a127", "a128", "a129", "a130", "a131", "a132", "a133", "a134", "a135", "a136", "a137", "a138", "a139", "a140", "a141", "a142", "a143", "a144", "a145", "a146", "a147", "a148", "a149", "a150", "a151", "a152", "a153", "a154", "a155", "a156", "a157", "a158", "a159", "a160", "a161", "a162", "a163", "a164", "a165", "a166", "a167", "a168", "a169", "a170", "a171", "a172", "a173", "a174", "a175", "a176", "a177", "a178", "a179", "a180", "a181", "a182", "a183", "a184", "a185", "a186", "a187", "a188", "a189", "a190", "a191", "a192", "a193", "a194", "a195", "a196", "a197", "a198", "a199", "a200", "a201", "a202", "a203", "a204", "a205", "a206", "a207", "a208", "a209", "a210", "a211", "a212", "a213", "a214", "a215", "a216", "a217", "a218", "a219", "a220", "a221", "a222", "a223", "a224", "a225", "a226", "a227", "a228", "a229", "a230", "a231", "a232", "a233", "a234", "a235", "a236", "a237", "a238", "a239", "a240", "a241", "a242", "a243", "a244", "a245", "a246", "a247", "a248", "a249", "a250", "a251", "a252", "a253", "a254", "a255", "s34", "s35", "s36", "s37", "s38", "s39", "s40", "s41", "s42", "s43", "s44", "s45", "s46", "s47", "s48", "s49", "s50", "s51", "s52", "s53", "s54", "s55", "s56", "s57", "s58", "s59", "s60", "s61", "s62", "s63", "s64", "s65", "s66", "s67", "s68", "s69", "s70", "s71", "s72", "s73", "s74", "s75", "s76", "s77", "s78", "s79", "s80", "s81", "s82", "s83", "s84", "s85", "s86", "s87", "s88", "s89", "s90", "s91", "s92", "s93", "s94", "s95", "s96", "s97", "s98", "s99", "vcc", "memory");
}

extern "C" void kernel_launch(void* const* d_in, const int* in_sizes, int n_in, void* d_out, int out_size, void* d_ws, size_t ws_size, hipStream_t stream) {
  (void)in_sizes; (void)n_in; (void)out_size; (void)ws_size;
  (void)hipFuncSetAttribute((const void*)mla_fused, hipFuncAttributeMaxDynamicSharedMemorySize, 163840);
  mla_fused<<<256, 256, 163840, stream>>>(d_in[0], d_in[1], d_in[2], d_in[3], d_in[4], d_in[7], d_in[8], d_in[10], d_in[5], d_in[9], d_ws, d_out);
}
```
